```python
import torch

def _softmax(x, axis=-1):
    return torch.softmax(x, dim=axis)
def _gelu(x):
    return torch.nn.functional.gelu(x, approximate="tanh")
def _silu(x):
    return torch.nn.functional.silu(x)
def _sigmoid(x):
    return torch.sigmoid(x)
def _layernorm(x, axis=-1, eps=1e-5):
    m = x.mean(dim=axis, keepdim=True)
    v = x.var(dim=axis, keepdim=True, unbiased=False)
    return (x - m) / torch.sqrt(v + eps)
def _scatter_add(tgt, idx, src, axis=0):
    return tgt.clone().index_add_(axis, idx, src)
def _segment_sum(src, seg, n):
    out = torch.zeros((n,) + src.shape[1:], dtype=src.dtype)
    return out.index_add_(0, seg, src)
def _pad(x, pw):
    flat = tuple(v for pair in reversed(pw) for v in pair)
    return torch.nn.functional.pad(x, flat)

kernel_name = 'parallel_sum__spectral_fnet__geglu_ffn__prompt'

def setup_inputs():

    _w = (lambda a: (lambda t: t.to(torch.bfloat16) if t.is_floating_point() else t)(torch.as_tensor(a)))
    def _A_setup():
        B, S, D = 1, 2048, 512
        ns = torch.arange(S)[:, None] * torch.arange(S)[None, :]
        nd = torch.arange(D)[:, None] * torch.arange(D)[None, :]
        Ws_re = torch.cos(-2.0 * torch.pi * ns / S).to(float)
        Ws_im = torch.sin(-2.0 * torch.pi * ns / S).to(float)
        Wd_re = torch.cos(-2.0 * torch.pi * nd / D).to(float)
        Wd_im = torch.sin(-2.0 * torch.pi * nd / D).to(float)
        return {
            "x": _w((lambda *s: torch.randn(*s, dtype=torch.bfloat16) * 0.05)(B, S, D)),
            "dft_seq_re": _w(Ws_re),
            "dft_seq_im": _w(Ws_im),
            "dft_hid_re": _w(Wd_re),
            "dft_hid_im": _w(Wd_im),
        }
    _A_in = _A_setup()

    def _B_setup():
        B, S, D, F = 1, 2048, 512, 1376
        return {
            "x": _w((lambda *s: torch.randn(*s, dtype=torch.bfloat16) * 0.05)(B, S, D)),
            "w_gate": _w((lambda *s: torch.randn(*s, dtype=torch.bfloat16) * 0.2)(D, F)),
            "w_up": _w((lambda *s: torch.randn(*s, dtype=torch.bfloat16) * 0.2)(D, F)),
            "w_down": _w((lambda *s: torch.randn(*s, dtype=torch.bfloat16) * 0.2)(F, D)),
        }
    _B_in = _B_setup()

    return {
        "A_x": _A_in["x"],
        "A_dft_seq_re": _A_in["dft_seq_re"],
        "A_dft_seq_im": _A_in["dft_seq_im"],
        "A_dft_hid_re": _A_in["dft_hid_re"],
        "A_dft_hid_im": _A_in["dft_hid_im"],
        "B_x": _B_in["x"],
        "B_w_gate": _B_in["w_gate"],
        "B_w_up": _B_in["w_up"],
        "B_w_down": _B_in["w_down"],
    }

def reference(A_x, A_dft_seq_re, A_dft_seq_im, A_dft_hid_re, A_dft_hid_im, B_x, B_w_gate, B_w_up, B_w_down):
    def _A_ref(x, dft_seq_re, dft_seq_im, dft_hid_re, dft_hid_im):
        a = torch.einsum("bsd,ed->bse", x, dft_hid_re)
        b = torch.einsum("bsd,ed->bse", x, dft_hid_im)
        y = torch.einsum("ts,bse->bte", dft_seq_re, a) - torch.einsum("ts,bse->bte", dft_seq_im, b)
        return y

    def _B_ref(x, w_gate, w_up, w_down):
        return (_gelu(x @ w_gate) * (x @ w_up)) @ w_down

    x = A_x
    xn = _layernorm(x, axis=-1)
    return x + _A_ref(xn, A_dft_seq_re, A_dft_seq_im, A_dft_hid_re, A_dft_hid_im) + _B_ref(xn, B_w_gate, B_w_up, B_w_down)
```

```cpp
#include <hip/hip_runtime.h>
#include <hip/hip_bf16.h>
#include <stdint.h>

typedef unsigned short u16;
typedef short bf16x8 __attribute__((ext_vector_type(8)));
typedef short bf16x4 __attribute__((ext_vector_type(4)));
typedef float f32x4 __attribute__((ext_vector_type(4)));
typedef unsigned u32x4 __attribute__((ext_vector_type(4)));
typedef unsigned u32x2 __attribute__((ext_vector_type(2)));
typedef __bf16 bf2_t __attribute__((ext_vector_type(2)));

constexpr int S_ = 2048, D_ = 512, F_ = 1376, FP_ = 1408;

#define LDS_AS __attribute__((address_space(3)))
#define GLDS16(gptr, ldsptr) \
  __builtin_amdgcn_global_load_lds((const unsigned*)(gptr), (unsigned*)(ldsptr), 16, 0, 0)
#define WAIT_V(n) asm volatile("s_waitcnt vmcnt(" #n ")" ::: "memory")
#define WAIT_L0() asm volatile("s_waitcnt lgkmcnt(0)" ::: "memory")

__device__ __forceinline__ float bf16r(float x) { return __bfloat162float(__float2bfloat16(x)); }
__device__ __forceinline__ unsigned pack2(float lo, float hi) {
  __hip_bfloat16 a = __float2bfloat16(lo), b = __float2bfloat16(hi);
  return (unsigned)__builtin_bit_cast(u16, a) | ((unsigned)__builtin_bit_cast(u16, b) << 16);
}
__device__ __forceinline__ float bflo(unsigned v) { return __builtin_bit_cast(float, v << 16); }
__device__ __forceinline__ float bfhi(unsigned v) { return __builtin_bit_cast(float, v & 0xffff0000u); }

__device__ __forceinline__ float dot2(unsigned a, unsigned b, float c) {
#if defined(__HIP_DEVICE_COMPILE__)
  return __builtin_amdgcn_fdot2_f32_bf16(__builtin_bit_cast(bf2_t, a), __builtin_bit_cast(bf2_t, b), c, false);
#else
  return c;
#endif
}
__device__ __forceinline__ bf16x4 tr_read(const char* p) {
#if defined(__HIP_DEVICE_COMPILE__)
  return __builtin_amdgcn_ds_read_tr16_b64_v4i16((LDS_AS bf16x4*)(p));
#else
  return bf16x4{};
#endif
}
__device__ __forceinline__ bf16x8 cat8(bf16x4 a, bf16x4 b) {
  return __builtin_shufflevector(a, b, 0, 1, 2, 3, 4, 5, 6, 7);
}
__device__ __forceinline__ float gelu_tanh(float g) {
  float z = 0.7978845608028654f * (g + 0.044715f * g * g * g);
  float e = __expf(2.0f * z);
  float t = 1.0f - 2.0f * __builtin_amdgcn_rcpf(e + 1.0f);
  return 0.5f * g * (1.0f + t);
}

constexpr int K1_A = 32768, K1_B = 16384, K1_STAGE = K1_A + K1_B;

__global__ __launch_bounds__(512, 2) void k1_kernel(const u16* __restrict__ x, const u16* __restrict__ wd_re,
                                                    const u16* __restrict__ wd_im, const u16* __restrict__ w_gate,
                                                    const u16* __restrict__ w_up, u16* __restrict__ ab,
                                                    u16* __restrict__ h) {
  __shared__ __attribute__((aligned(1024))) char smem[3 * K1_STAGE];
  const int tid = threadIdx.x, wid = __builtin_amdgcn_readfirstlane(tid >> 6), lane = tid & 63;
  const int wm = wid >> 1, wn = wid & 1, fr = lane & 15, fq = lane >> 4;
  const int bid = blockIdx.x;
  const int nb = (bid & 7) * 30 + (bid >> 3);
  const int cb = nb >> 3, rb = nb & 7;
  const bool nk = cb < 8;
  const int row0 = rb * 256;

  const int l3 = lane >> 3, l7 = lane & 7;
  const int rt_c0 = (l7 ^ (fq & 7)) * 8, rt_c1 = (l7 ^ ((fq + 4) & 7)) * 8;
  const int kt_c0 = (l7 ^ ((fq & 1) << 1)) * 8, kt_c1 = (l7 ^ (((fq & 1) | 2) << 1)) * 8;

  const u16* xa = x + (size_t)row0 * D_;
  const u16* wnk = (cb < 4 ? wd_re : wd_im) + (size_t)((cb & 3) * 128) * D_;
  const int f0 = (cb - 8) * 64;

  auto stage = [&](int t, int buf) {
    char* sa = smem + buf * K1_STAGE;
    char* sb = sa + K1_A;
    const int k0 = t * 64;
#pragma unroll
    for (int i = 0; i < 4; ++i) {
      const int j = wid * 4 + i;
      const u16* src = xa + (j * 8 + l3) * D_ + k0 + ((i & 1) ? rt_c1 : rt_c0);
      GLDS16(src, sa + j * 1024);
    }
    if (nk) {
#pragma unroll
      for (int i = 0; i < 2; ++i) {
        const int j = wid * 2 + i;
        const u16* src = wnk + (j * 8 + l3) * D_ + k0 + ((i & 1) ? rt_c1 : rt_c0);
        GLDS16(src, sb + j * 1024);
      }
    } else {
      const u16* w = (wid < 4) ? w_gate : w_up;
#pragma unroll
      for (int i = 0; i < 2; ++i) {
        const int j = (wid & 3) * 2 + i;
        int col = f0 + ((i & 1) ? kt_c1 : kt_c0);
        if (col >= F_) col -= 32;
        const u16* src = w + (k0 + j * 8 + l3) * F_ + col;
        GLDS16(src, sb + (wid * 2 + i) * 1024);
      }
    }
  };

  f32x4 acc[4][4];
#pragma unroll
  for (int i = 0; i < 4; ++i)
#pragma unroll
    for (int j = 0; j < 4; ++j) acc[i][j] = f32x4{0.f, 0.f, 0.f, 0.f};
  float st_s[4] = {0.f, 0.f, 0.f, 0.f}, st_q[4] = {0.f, 0.f, 0.f, 0.f};
  float cs[4] = {0.f, 0.f, 0.f, 0.f};

  const int a_off = (wm * 64 + fr) * 128;
  const int sw = (fr >> 1) & 7;
  const int ch0 = ((fq) ^ sw) * 16, ch1 = ((4 + fq) ^ sw) * 16;
  const int bnk_off = (wn * 64 + fr) * 128;
  const int tq = (lane >> 2) & 3, tp = lane & 3;
  const int tswz = ((tq >> 1) & 1) | ((fq & 1) << 1);
  const int tr_row = (8 * fq + tq) * 128 + 8 * tp;

  stage(0, 0);
  stage(1, 1);
  const unsigned ONES = 0x3f803f80u;
  for (int t = 0; t < 8; ++t) {
    const int buf = t % 3;
    if (t < 7) WAIT_V(6); else WAIT_V(0);
    WAIT_L0();
    __builtin_amdgcn_s_barrier();
    if (t + 2 < 8) stage(t + 2, (t + 2) % 3);
    const char* sa = smem + buf * K1_STAGE;
    const char* sb = sa + K1_A;
#pragma unroll
    for (int ks = 0; ks < 2; ++ks) {
      bf16x8 xf[4], wf[4];
      const int ch = ks ? ch1 : ch0;
#pragma unroll
      for (int mi = 0; mi < 4; ++mi) xf[mi] = *(const bf16x8*)(sa + a_off + mi * 2048 + ch);
      if (nk) {
#pragma unroll
        for (int ni = 0; ni < 4; ++ni) wf[ni] = *(const bf16x8*)(sb + bnk_off + ni * 2048 + ch);
      } else {
#pragma unroll
        for (int ni = 0; ni < 4; ++ni) {
          const int u = wn * 2 + (ni & 1);
          const char* p = sb + (ni >> 1) * 8192 + ks * 4096 + tr_row + 32 * (u ^ tswz);
          wf[ni] = cat8(tr_read(p), tr_read(p + 512));
        }
      }
#pragma unroll
      for (int ni = 0; ni < 4; ++ni)
#pragma unroll
        for (int mi = 0; mi < 4; ++mi)
          acc[ni][mi] = __builtin_amdgcn_mfma_f32_16x16x32_bf16(wf[ni], xf[mi], acc[ni][mi], 0, 0, 0);
      if (ks == wn) {
#pragma unroll
        for (int mi = 0; mi < 4; ++mi) {
          u32x4 v = __builtin_bit_cast(u32x4, xf[mi]);
#pragma unroll
          for (int e = 0; e < 4; ++e) {
            st_s[mi] = dot2(v[e], ONES, st_s[mi]);
            st_q[mi] = dot2(v[e], v[e], st_q[mi]);
          }
        }
      }
      if (((t & 1) * 2 + ks) == wm) {
#pragma unroll
        for (int ni = 0; ni < 4; ++ni) {
          u32x4 v = __builtin_bit_cast(u32x4, wf[ni]);
#pragma unroll
          for (int e = 0; e < 4; ++e) cs[ni] = dot2(v[e], ONES, cs[ni]);
        }
      }
    }
  }
#pragma unroll
  for (int i = 0; i < 4; ++i) {
    st_s[i] += __shfl_xor(st_s[i], 16); st_s[i] += __shfl_xor(st_s[i], 32);
    st_q[i] += __shfl_xor(st_q[i], 16); st_q[i] += __shfl_xor(st_q[i], 32);
    cs[i] += __shfl_xor(cs[i], 16); cs[i] += __shfl_xor(cs[i], 32);
  }
  __syncthreads();
  float* sst = (float*)smem;
  float* scs = (float*)(smem + 8192);
  if (fq == 0) {
#pragma unroll
    for (int i = 0; i < 4; ++i) {
      sst[((wm * 2 + wn) * 2 + 0) * 64 + i * 16 + fr] = st_s[i];
      sst[((wm * 2 + wn) * 2 + 1) * 64 + i * 16 + fr] = st_q[i];
      scs[(wn * 4 + wm) * 64 + i * 16 + fr] = cs[i];
    }
  }
  __syncthreads();
  float mb[4], rinv[4];
#pragma unroll
  for (int mi = 0; mi < 4; ++mi) {
    float s = sst[((wm * 2 + 0) * 2 + 0) * 64 + mi * 16 + fr] + sst[((wm * 2 + 1) * 2 + 0) * 64 + mi * 16 + fr];
    float q = sst[((wm * 2 + 0) * 2 + 1) * 64 + mi * 16 + fr] + sst[((wm * 2 + 1) * 2 + 1) * 64 + mi * 16 + fr];
    float mean = s * (1.0f / D_);
    float var = fmaxf(q * (1.0f / D_) - mean * mean, 0.f);
    mb[mi] = bf16r(mean);
    float sd = bf16r(sqrtf(bf16r(bf16r(var) + 1e-5f)));
    rinv[mi] = 1.0f / sd;
  }
  f32x4 csv[4];
#pragma unroll
  for (int ni = 0; ni < 4; ++ni) {
    f32x4 c = *(const f32x4*)(scs + (wn * 4 + 0) * 64 + ni * 16 + fq * 4);
#pragma unroll
    for (int w = 1; w < 4; ++w) c += *(const f32x4*)(scs + (wn * 4 + w) * 64 + ni * 16 + fq * 4);
    csv[ni] = c;
  }
  if (nk) {
    const float sgn = cb < 4 ? 1.0f : -1.0f;
    u16* dst = ab + (size_t)(cb >> 2) * (S_ * D_) + (size_t)(row0 + wm * 64 + fr) * D_ + (cb & 3) * 128 + wn * 64 + fq * 4;
#pragma unroll
    for (int mi = 0; mi < 4; ++mi) {
      const float r = rinv[mi] * sgn;
#pragma unroll
      for (int ni = 0; ni < 4; ++ni) {
        f32x4 v = (acc[ni][mi] - mb[mi] * csv[ni]) * r;
        u32x2 o = {pack2(v[0], v[1]), pack2(v[2], v[3])};
        *(u32x2*)(dst + mi * 16 * D_ + ni * 16) = o;
      }
    }
  } else {
    const int fc = f0 + wn * 32 + fq * 4;
    u16* dst = h + (size_t)(row0 + wm * 64 + fr) * FP_ + fc;
    const bool valid = (f0 + wn * 32) < F_;
#pragma unroll
    for (int mi = 0; mi < 4; ++mi) {
#pragma unroll
      for (int ni = 0; ni < 2; ++ni) {
        f32x4 g = (acc[ni][mi] - mb[mi] * csv[ni]) * rinv[mi];
        f32x4 u = (acc[ni + 2][mi] - mb[mi] * csv[ni + 2]) * rinv[mi];
        float hv[4];
#pragma unroll
        for (int e = 0; e < 4; ++e) {
          float gl = bf16r(gelu_tanh(bf16r(g[e])));
          hv[e] = valid ? gl * bf16r(u[e]) : 0.f;
        }
        u32x2 o = {pack2(hv[0], hv[1]), pack2(hv[2], hv[3])};
        *(u32x2*)(dst + mi * 16 * FP_ + ni * 16) = o;
      }
    }
  }
}

constexpr int K2_RED_LD = 68;
constexpr int K2_WAVE_LDS = 64 * K2_RED_LD * 4;

__global__ __launch_bounds__(512, 2) void k2_kernel(const u16* __restrict__ x, const u16* __restrict__ ws_re,
                                                    const u16* __restrict__ ws_im, const u16* __restrict__ w_down,
                                                    const u16* __restrict__ ab, const u16* __restrict__ h,
                                                    u16* __restrict__ out) {
  __shared__ __attribute__((aligned(1024))) char smem[8 * K2_WAVE_LDS];
  const int tid = threadIdx.x, wid = __builtin_amdgcn_readfirstlane(tid >> 6), lane = tid & 63;
  const int fr = lane & 15, fq = lane >> 4;
  const int bid = blockIdx.x;
  const int idx = bid >> 3;
  const int rb = (bid & 7) * 4 + (idx >> 3), cbk = idx & 7;
  const int row0 = rb * 64, n0 = cbk * 64;

  const int l3 = lane >> 3, l7 = lane & 7;
  const int rt_c0 = (l7 ^ (fq & 7)) * 8, rt_c1 = (l7 ^ ((fq + 4) & 7)) * 8;
  const int kt_c0 = (l7 ^ ((fq & 1) << 1)) * 8, kt_c1 = (l7 ^ (((fq & 1) | 2) << 1)) * 8;

  char* sa = smem + wid * K2_WAVE_LDS;
  char* sb = sa + 8192;

  auto stage = [&](int t) {
    const u16* A;
    const u16* B;
    int lda, k0, kmax;
    if (t < 64) {
      A = (t < 32 ? ws_re : ws_im) + (size_t)row0 * S_;
      lda = S_;
      k0 = (t & 31) * 64;
      B = ab + (size_t)(t >> 5) * (S_ * D_);
      kmax = S_ - 1;
    } else {
      A = h + (size_t)row0 * FP_;
      lda = FP_;
      k0 = (t - 64) * 64;
      B = w_down;
      kmax = F_ - 1;
    }
#pragma unroll
    for (int j = 0; j < 8; ++j) {
      const u16* src = A + (j * 8 + l3) * lda + k0 + ((j & 1) ? rt_c1 : rt_c0);
      GLDS16(src, sa + j * 1024);
    }
#pragma unroll
    for (int j = 0; j < 8; ++j) {
      int krow = min(k0 + j * 8 + l3, kmax);
      const u16* src = B + krow * D_ + n0 + ((j & 1) ? kt_c1 : kt_c0);
      GLDS16(src, sb + j * 1024);
    }
  };

  f32x4 acc[4][4];
#pragma unroll
  for (int i = 0; i < 4; ++i)
#pragma unroll
    for (int j = 0; j < 4; ++j) acc[i][j] = f32x4{0.f, 0.f, 0.f, 0.f};

  const int sw = (fr >> 1) & 7;
  const int a_off0 = fr * 128 + ((fq) ^ sw) * 16, a_off1 = fr * 128 + ((4 + fq) ^ sw) * 16;
  const int tq = (lane >> 2) & 3, tp = lane & 3;
  const int tswz = ((tq >> 1) & 1) | ((fq & 1) << 1);
  const int tr_row = (8 * fq + tq) * 128 + 8 * tp;

  stage(wid);
  for (int t = wid; t < 86; t += 8) {
    WAIT_V(0);
    bf16x8 af[2][4], bfr[2][4];
#pragma unroll
    for (int ks = 0; ks < 2; ++ks) {
#pragma unroll
      for (int mi = 0; mi < 4; ++mi) af[ks][mi] = *(const bf16x8*)(sa + (ks ? a_off1 : a_off0) + mi * 2048);
#pragma unroll
      for (int ni = 0; ni < 4; ++ni) {
        const char* p = sb + ks * 4096 + tr_row + 32 * (ni ^ tswz);
        bfr[ks][ni] = cat8(tr_read(p), tr_read(p + 512));
      }
    }
    WAIT_L0();
    __builtin_amdgcn_sched_barrier(0);
    if (t + 8 < 86) stage(t + 8);
    __builtin_amdgcn_sched_barrier(0);
#pragma unroll
    for (int ks = 0; ks < 2; ++ks)
#pragma unroll
      for (int ni = 0; ni < 4; ++ni)
#pragma unroll
        for (int mi = 0; mi < 4; ++mi)
          acc[ni][mi] = __builtin_amdgcn_mfma_f32_16x16x32_bf16(bfr[ks][ni], af[ks][mi], acc[ni][mi], 0, 0, 0);
  }
  float* red = (float*)sa;
#pragma unroll
  for (int mi = 0; mi < 4; ++mi)
#pragma unroll
    for (int ni = 0; ni < 4; ++ni)
      *(f32x4*)(red + (mi * 16 + fr) * K2_RED_LD + ni * 16 + fq * 4) = acc[ni][mi];
  __syncthreads();
  {
    const int m = tid >> 3, c = (tid & 7) * 8;
    f32x4 s0 = {0.f, 0.f, 0.f, 0.f}, s1 = {0.f, 0.f, 0.f, 0.f};
#pragma unroll
    for (int w = 0; w < 8; ++w) {
      const float* p = (const float*)(smem + w * K2_WAVE_LDS) + m * K2_RED_LD + c;
      s0 += *(const f32x4*)p;
      s1 += *(const f32x4*)(p + 4);
    }
    const size_t o = (size_t)(row0 + m) * D_ + n0 + c;
    u32x4 xv = *(const u32x4*)(x + o);
    u32x4 r;
    r[0] = pack2(s0[0] + bflo(xv[0]), s0[1] + bfhi(xv[0]));
    r[1] = pack2(s0[2] + bflo(xv[1]), s0[3] + bfhi(xv[1]));
    r[2] = pack2(s1[0] + bflo(xv[2]), s1[1] + bfhi(xv[2]));
    r[3] = pack2(s1[2] + bflo(xv[3]), s1[3] + bfhi(xv[3]));
    *(u32x4*)(out + o) = r;
  }
}

extern "C" void kernel_launch(void* const* d_in, const int* in_sizes, int n_in, void* d_out, int out_size,
                              void* d_ws, size_t ws_size, hipStream_t stream) {
  const u16* x = (const u16*)d_in[0];
  const u16* ws_re = (const u16*)d_in[1];
  const u16* ws_im = (const u16*)d_in[2];
  const u16* wd_re = (const u16*)d_in[3];
  const u16* wd_im = (const u16*)d_in[4];
  const u16* w_gate = (const u16*)d_in[6];
  const u16* w_up = (const u16*)d_in[7];
  const u16* w_down = (const u16*)d_in[8];
  u16* ab = (u16*)d_ws;
  u16* h = ab + (size_t)2 * S_ * D_;
  k1_kernel<<<240, 512, 0, stream>>>(x, wd_re, wd_im, w_gate, w_up, ab, h);
  k2_kernel<<<256, 512, 0, stream>>>(x, ws_re, ws_im, w_down, ab, h, (u16*)d_out);
}
```

```cpp
#include <hip/hip_runtime.h>
#include <hip/hip_bf16.h>
#include <stdint.h>

typedef unsigned short u16;
typedef short bf16x8 __attribute__((ext_vector_type(8)));
typedef short bf16x4 __attribute__((ext_vector_type(4)));
typedef float f32x4 __attribute__((ext_vector_type(4)));
typedef unsigned u32x4 __attribute__((ext_vector_type(4)));
typedef unsigned u32x2 __attribute__((ext_vector_type(2)));
typedef __bf16 bf2_t __attribute__((ext_vector_type(2)));

constexpr int S_ = 2048, D_ = 512, F_ = 1376, FP_ = 1408;

#define LDS_AS __attribute__((address_space(3)))
#define GLDS16(gptr, ldsptr) \
  __builtin_amdgcn_global_load_lds((const unsigned*)(gptr), (unsigned*)(ldsptr), 16, 0, 0)
#define WAIT_V(n) asm volatile("s_waitcnt vmcnt(" #n ")" ::: "memory")
#define BARRIER() asm volatile("s_barrier" ::: "memory")
#define WAIT_L0() asm volatile("s_waitcnt lgkmcnt(0)" ::: "memory")

__device__ __forceinline__ float bf16r(float x) { return __bfloat162float(__float2bfloat16(x)); }
__device__ __forceinline__ unsigned pack2(float lo, float hi) {
  __hip_bfloat16 a = __float2bfloat16(lo), b = __float2bfloat16(hi);
  return (unsigned)__builtin_bit_cast(u16, a) | ((unsigned)__builtin_bit_cast(u16, b) << 16);
}
__device__ __forceinline__ float bflo(unsigned v) { return __builtin_bit_cast(float, v << 16); }
__device__ __forceinline__ float bfhi(unsigned v) { return __builtin_bit_cast(float, v & 0xffff0000u); }

__device__ __forceinline__ float dot2(unsigned a, unsigned b, float c) {
#if defined(__HIP_DEVICE_COMPILE__)
  return __builtin_amdgcn_fdot2_f32_bf16(__builtin_bit_cast(bf2_t, a), __builtin_bit_cast(bf2_t, b), c, false);
#else
  return c;
#endif
}
__device__ __forceinline__ bf16x4 tr_read(const char* p) {
#if defined(__HIP_DEVICE_COMPILE__)
  return __builtin_amdgcn_ds_read_tr16_b64_v4i16((LDS_AS bf16x4*)(p));
#else
  return bf16x4{};
#endif
}
template <int OFF>
__device__ __forceinline__ bf16x8 tr_frag(unsigned addr) {
  bf16x4 a, b;
  asm volatile("ds_read_b64_tr_b16 %0, %2 offset:%3\n\tds_read_b64_tr_b16 %1, %2 offset:%4"
               : "=&v"(a), "=&v"(b)
               : "v"(addr), "i"(OFF), "i"(OFF + 512));
  return __builtin_shufflevector(a, b, 0, 1, 2, 3, 4, 5, 6, 7);
}
template <int OFF>
__device__ __forceinline__ bf16x8 lds_rd128(unsigned addr) {
  bf16x8 a;
  asm volatile("ds_read_b128 %0, %1 offset:%2" : "=&v"(a) : "v"(addr), "i"(OFF));
  return a;
}
__device__ __forceinline__ unsigned lds_addr(const void* p) {
  return (unsigned)(uintptr_t)(const LDS_AS char*)p;
}
__device__ __forceinline__ bf16x8 cat8(bf16x4 a, bf16x4 b) {
  return __builtin_shufflevector(a, b, 0, 1, 2, 3, 4, 5, 6, 7);
}
__device__ __forceinline__ float gelu_tanh(float g) {
  float z = 0.7978845608028654f * (g + 0.044715f * g * g * g);
  float e = __expf(2.0f * z);
  float t = 1.0f - 2.0f * __builtin_amdgcn_rcpf(e + 1.0f);
  return 0.5f * g * (1.0f + t);
}

constexpr int K1_A = 32768, K1_B = 16384, K1_STAGE = K1_A + K1_B;

__global__ __launch_bounds__(768, 3) void k1_kernel(const u16* __restrict__ x, const u16* __restrict__ wd_re,
                                                    const u16* __restrict__ wd_im, const u16* __restrict__ w_gate,
                                                    const u16* __restrict__ w_up, u16* __restrict__ ab,
                                                    u16* __restrict__ h) {
  __shared__ __attribute__((aligned(1024))) char smem[3 * K1_STAGE];
  const int tid = threadIdx.x, wid = __builtin_amdgcn_readfirstlane(tid >> 6), lane = tid & 63;
  const int fr = lane & 15, fq = lane >> 4;
  const int bid = blockIdx.x;
  const int nb = (bid & 7) * 30 + (bid >> 3);
  const int cb = nb >> 3, rb = nb & 7;
  const bool nk = cb < 8;
  const int row0 = rb * 256;
  const int f0 = (cb - 8) * 64;

  if (wid >= 8) {
    const int lw = wid - 8;
    const int l3 = lane >> 3, l7 = lane & 7;
    const int rt_c0 = (l7 ^ (fq & 7)) * 8, rt_c1 = (l7 ^ ((fq + 4) & 7)) * 8;
    const int kt_c0 = (l7 ^ ((fq & 1) << 1)) * 8, kt_c1 = (l7 ^ (((fq & 1) | 2) << 1)) * 8;
    const u16* xa0 = x + (size_t)(row0 + lw * 64 + l3) * D_ + rt_c0;
    const u16* xa1 = x + (size_t)(row0 + lw * 64 + 8 + l3) * D_ + rt_c1;
    const u16 *wb0, *wb1;
    int wstep_j, wstep_t;
    if (nk) {
      const u16* wnk = (cb < 4 ? wd_re : wd_im) + (size_t)((cb & 3) * 128 + lw * 32 + l3) * D_;
      wb0 = wnk + rt_c0;
      wb1 = wnk + 8 * D_ + rt_c1;
      wstep_j = 16 * D_;
      wstep_t = 64;
    } else {
      const u16* w = (lw < 2) ? w_gate : w_up;
      int c0 = f0 + kt_c0, c1 = f0 + kt_c1;
      if (c0 >= F_) c0 -= 32;
      if (c1 >= F_) c1 -= 32;
      const u16* wk = w + (size_t)((lw & 1) * 32 + l3) * F_;
      wb0 = wk + c0;
      wb1 = wk + 8 * F_ + c1;
      wstep_j = 16 * F_;
      wstep_t = 64 * F_;
    }
    auto stage = [&](int t, int buf) {
      char* sa = smem + buf * K1_STAGE + lw * 8192;
      char* sb = smem + buf * K1_STAGE + K1_A + lw * 4096;
#pragma unroll
      for (int i = 0; i < 4; ++i) {
        GLDS16(xa0 + i * 16 * D_ + t * 64, sa + i * 2048);
        GLDS16(xa1 + i * 16 * D_ + t * 64, sa + i * 2048 + 1024);
      }
#pragma unroll
      for (int i = 0; i < 2; ++i) {
        GLDS16(wb0 + i * wstep_j + t * wstep_t, sb + i * 2048);
        GLDS16(wb1 + i * wstep_j + t * wstep_t, sb + i * 2048 + 1024);
      }
    };
    stage(0, 0);
    stage(1, 1);
    int ib = 2;
    for (int t = 0; t < 8; ++t) {
      if (t < 7) WAIT_V(12); else WAIT_V(0);
      BARRIER();
      if (t + 2 < 8) stage(t + 2, ib);
      ib = (ib == 2) ? 0 : ib + 1;
    }
    return;
  }

  const int wm = wid >> 1, wn = wid & 1;
  f32x4 acc[4][4];
#pragma unroll
  for (int i = 0; i < 4; ++i)
#pragma unroll
    for (int j = 0; j < 4; ++j) acc[i][j] = f32x4{0.f, 0.f, 0.f, 0.f};
  float st_s[4] = {0.f, 0.f, 0.f, 0.f}, st_q[4] = {0.f, 0.f, 0.f, 0.f};
  float cs[4] = {0.f, 0.f, 0.f, 0.f};

  const int a_off = (wm * 64 + fr) * 128;
  const int sw = (fr >> 1) & 7;
  const int ch0 = ((fq) ^ sw) * 16, ch1 = ((4 + fq) ^ sw) * 16;
  const int bnk_off = (wn * 64 + fr) * 128;
  const int tq = (lane >> 2) & 3, tp = lane & 3;
  const int tswz = ((tq >> 1) & 1) | ((fq & 1) << 1);
  const int tr_row = (8 * fq + tq) * 128 + 8 * tp;

  const unsigned ONES = 0x3f803f80u;
  int buf = 0;
  for (int t = 0; t < 8; ++t) {
    BARRIER();
    const char* sa = smem + buf * K1_STAGE;
    const char* sb = sa + K1_A;
#pragma unroll
    for (int ks = 0; ks < 2; ++ks) {
      bf16x8 xf[4], wf[4];
      const int ch = ks ? ch1 : ch0;
#pragma unroll
      for (int mi = 0; mi < 4; ++mi) xf[mi] = *(const bf16x8*)(sa + a_off + mi * 2048 + ch);
      if (nk) {
#pragma unroll
        for (int ni = 0; ni < 4; ++ni) wf[ni] = *(const bf16x8*)(sb + bnk_off + ni * 2048 + ch);
      } else {
        const unsigned p0 = lds_addr(sb) + tr_row + 32 * ((wn * 2) ^ tswz);
        const unsigned p1 = lds_addr(sb) + tr_row + 32 * ((wn * 2 + 1) ^ tswz);
        if (ks == 0) {
          wf[0] = tr_frag<0>(p0); wf[1] = tr_frag<0>(p1); wf[2] = tr_frag<8192>(p0); wf[3] = tr_frag<8192>(p1);
        } else {
          wf[0] = tr_frag<4096>(p0); wf[1] = tr_frag<4096>(p1); wf[2] = tr_frag<12288>(p0); wf[3] = tr_frag<12288>(p1);
        }
      }
      WAIT_L0();
      __builtin_amdgcn_sched_barrier(0);
#pragma unroll
      for (int ni = 0; ni < 4; ++ni)
#pragma unroll
        for (int mi = 0; mi < 4; ++mi)
          acc[ni][mi] = __builtin_amdgcn_mfma_f32_16x16x32_bf16(wf[ni], xf[mi], acc[ni][mi], 0, 0, 0);
      if (ks == wn) {
#pragma unroll
        for (int mi = 0; mi < 4; ++mi) {
          u32x4 v = __builtin_bit_cast(u32x4, xf[mi]);
#pragma unroll
          for (int e = 0; e < 4; ++e) {
            st_s[mi] = dot2(v[e], ONES, st_s[mi]);
            st_q[mi] = dot2(v[e], v[e], st_q[mi]);
          }
        }
      }
      if (((t & 1) * 2 + ks) == wm) {
#pragma unroll
        for (int ni = 0; ni < 4; ++ni) {
          u32x4 v = __builtin_bit_cast(u32x4, wf[ni]);
#pragma unroll
          for (int e = 0; e < 4; ++e) cs[ni] = dot2(v[e], ONES, cs[ni]);
        }
      }
    }
    buf = (buf == 2) ? 0 : buf + 1;
  }
#pragma unroll
  for (int i = 0; i < 4; ++i) {
    st_s[i] += __shfl_xor(st_s[i], 16); st_s[i] += __shfl_xor(st_s[i], 32);
    st_q[i] += __shfl_xor(st_q[i], 16); st_q[i] += __shfl_xor(st_q[i], 32);
    cs[i] += __shfl_xor(cs[i], 16); cs[i] += __shfl_xor(cs[i], 32);
  }
  float* sst = (float*)(smem + 2 * K1_STAGE);
  float* scs = (float*)(smem + 2 * K1_STAGE + 8192);
  if (fq == 0) {
#pragma unroll
    for (int i = 0; i < 4; ++i) {
      sst[((wm * 2 + wn) * 2 + 0) * 64 + i * 16 + fr] = st_s[i];
      sst[((wm * 2 + wn) * 2 + 1) * 64 + i * 16 + fr] = st_q[i];
      scs[(wn * 4 + wm) * 64 + i * 16 + fr] = cs[i];
    }
  }
  __syncthreads();
  float mb[4], rinv[4];
#pragma unroll
  for (int mi = 0; mi < 4; ++mi) {
    float s = sst[((wm * 2 + 0) * 2 + 0) * 64 + mi * 16 + fr] + sst[((wm * 2 + 1) * 2 + 0) * 64 + mi * 16 + fr];
    float q = sst[((wm * 2 + 0) * 2 + 1) * 64 + mi * 16 + fr] + sst[((wm * 2 + 1) * 2 + 1) * 64 + mi * 16 + fr];
    float mean = s * (1.0f / D_);
    float var = fmaxf(q * (1.0f / D_) - mean * mean, 0.f);
    mb[mi] = bf16r(mean);
    float sd = bf16r(sqrtf(bf16r(bf16r(var) + 1e-5f)));
    rinv[mi] = 1.0f / sd;
  }
  f32x4 csv[4];
#pragma unroll
  for (int ni = 0; ni < 4; ++ni) {
    f32x4 c = *(const f32x4*)(scs + (wn * 4 + 0) * 64 + ni * 16 + fq * 4);
#pragma unroll
    for (int w = 1; w < 4; ++w) c += *(const f32x4*)(scs + (wn * 4 + w) * 64 + ni * 16 + fq * 4);
    csv[ni] = c;
  }
  if (nk) {
    const float sgn = cb < 4 ? 1.0f : -1.0f;
    u16* dst = ab + (size_t)(cb >> 2) * (S_ * D_) + (size_t)(row0 + wm * 64 + fr) * D_ + (cb & 3) * 128 + wn * 64 + fq * 4;
#pragma unroll
    for (int mi = 0; mi < 4; ++mi) {
      const float r = rinv[mi] * sgn;
#pragma unroll
      for (int ni = 0; ni < 4; ++ni) {
        f32x4 v = (acc[ni][mi] - mb[mi] * csv[ni]) * r;
        u32x2 o = {pack2(v[0], v[1]), pack2(v[2], v[3])};
        *(u32x2*)(dst + mi * 16 * D_ + ni * 16) = o;
      }
    }
  } else {
    const int fc = f0 + wn * 32 + fq * 4;
    u16* dst = h + (size_t)(row0 + wm * 64 + fr) * FP_ + fc;
    const bool valid = (f0 + wn * 32) < F_;
#pragma unroll
    for (int mi = 0; mi < 4; ++mi) {
#pragma unroll
      for (int ni = 0; ni < 2; ++ni) {
        f32x4 g = (acc[ni][mi] - mb[mi] * csv[ni]) * rinv[mi];
        f32x4 u = (acc[ni + 2][mi] - mb[mi] * csv[ni + 2]) * rinv[mi];
        float hv[4];
#pragma unroll
        for (int e = 0; e < 4; ++e) {
          float gl = bf16r(gelu_tanh(bf16r(g[e])));
          hv[e] = valid ? gl * bf16r(u[e]) : 0.f;
        }
        u32x2 o = {pack2(hv[0], hv[1]), pack2(hv[2], hv[3])};
        *(u32x2*)(dst + mi * 16 * FP_ + ni * 16) = o;
      }
    }
  }
}

constexpr int K2_STAGE = 32768, K2_NST = 5, K2_NT = 43;
constexpr int K2_RED_LD = 68;

__global__ __launch_bounds__(512, 2) void k2_kernel(const u16* __restrict__ x, const u16* __restrict__ ws_re,
                                                    const u16* __restrict__ ws_im, const u16* __restrict__ w_down,
                                                    const u16* __restrict__ ab, const u16* __restrict__ h,
                                                    u16* __restrict__ out) {
  __shared__ __attribute__((aligned(1024))) char smem[K2_NST * K2_STAGE];
  const int tid = threadIdx.x, wid = __builtin_amdgcn_readfirstlane(tid >> 6), lane = tid & 63;
  const int fr = lane & 15, fq = lane >> 4;
  const int bid = blockIdx.x;
  const int idx = bid >> 3;
  const int rb = (bid & 7) * 4 + (idx >> 3), cbk = idx & 7;
  const int row0 = rb * 64, n0 = cbk * 64;

  if (wid >= 4) {
    const int lw = wid - 4;
    const int l3 = lane >> 3, l7 = lane & 7;
    const int rt_c0 = (l7 ^ (fq & 7)) * 8, rt_c1 = (l7 ^ ((fq + 4) & 7)) * 8;
    const int kt_c0 = (l7 ^ ((fq & 1) << 1)) * 8, kt_c1 = (l7 ^ (((fq & 1) | 2) << 1)) * 8;
    const int sub = lw & 1;
    auto stage = [&](int t, int slot) {
      char* dst = smem + slot * K2_STAGE + lw * 8192;
      if (lw < 2) {
        const u16* A;
        int lda, k0;
        if (t < 32) { A = (t < 16 ? ws_re : ws_im) + (size_t)row0 * S_; lda = S_; k0 = (t & 15) * 128; }
        else { A = h + (size_t)row0 * FP_; lda = FP_; k0 = (t - 32) * 128; }
        k0 += sub * 64;
#pragma unroll
        for (int i = 0; i < 8; ++i) {
          const u16* src = A + (i * 8 + l3) * lda + k0 + ((i & 1) ? rt_c1 : rt_c0);
          GLDS16(src, dst + i * 1024);
        }
      } else {
        const u16* B;
        int k0, kmax;
        if (t < 32) { B = ab + (size_t)(t >> 4) * (S_ * D_); k0 = (t & 15) * 128; kmax = S_ - 1; }
        else { B = w_down; k0 = (t - 32) * 128; kmax = F_ - 1; }
        k0 += sub * 64;
#pragma unroll
        for (int i = 0; i < 8; ++i) {
          const int krow = min(k0 + i * 8 + l3, kmax);
          const u16* src = B + krow * D_ + n0 + ((i & 1) ? kt_c1 : kt_c0);
          GLDS16(src, dst + i * 1024);
        }
      }
    };
#pragma unroll
    for (int s = 0; s < K2_NST - 1; ++s) stage(s, s);
    int islot = K2_NST - 1;
    for (int t = 0; t < K2_NT; ++t) {
      if (t < K2_NT - 3) WAIT_V(24);
      else if (t == K2_NT - 3) WAIT_V(16);
      else if (t == K2_NT - 2) WAIT_V(8);
      else WAIT_V(0);
      BARRIER();
      if (t + K2_NST - 1 < K2_NT) stage(t + K2_NST - 1, islot);
      islot = (islot == K2_NST - 1) ? 0 : islot + 1;
    }
  } else {
    f32x4 acc[4][4];
#pragma unroll
    for (int i = 0; i < 4; ++i)
#pragma unroll
      for (int j = 0; j < 4; ++j) acc[i][j] = f32x4{0.f, 0.f, 0.f, 0.f};

    const int kq = wid;
    const int sw = (fr >> 1) & 7;
    const unsigned a_base = lds_addr(smem) + (kq >> 1) * 8192 + fr * 128 + (((kq & 1) * 4 + fq) ^ sw) * 16;
    const int tq = (lane >> 2) & 3, tp = lane & 3;
    const int tswz = ((tq >> 1) & 1) | ((fq & 1) << 1);
    const unsigned b_base = lds_addr(smem) + 16384 + (kq >> 1) * 8192 + (kq & 1) * 4096 + (8 * fq + tq) * 128 + 8 * tp;
    unsigned b_off[4];
#pragma unroll
    for (int ni = 0; ni < 4; ++ni) b_off[ni] = b_base + 32 * (ni ^ tswz);

    bf16x8 afA[4], bfA[4], afB[4], bfB[4];
    auto rd = [&](bf16x8* af, bf16x8* bfr, int slot) {
      const unsigned so = slot * K2_STAGE;
      af[0] = lds_rd128<0>(a_base + so);
      af[1] = lds_rd128<2048>(a_base + so);
      af[2] = lds_rd128<4096>(a_base + so);
      af[3] = lds_rd128<6144>(a_base + so);
#pragma unroll
      for (int ni = 0; ni < 4; ++ni) bfr[ni] = tr_frag<0>(b_off[ni] + so);
    };
    auto mm = [&](const bf16x8* af, const bf16x8* bfr) {
#pragma unroll
      for (int ni = 0; ni < 4; ++ni)
#pragma unroll
        for (int mi = 0; mi < 4; ++mi)
          acc[ni][mi] = __builtin_amdgcn_mfma_f32_16x16x32_bf16(bfr[ni], af[mi], acc[ni][mi], 0, 0, 0);
    };
    BARRIER();
    rd(afA, bfA, 0);
    WAIT_L0();
    __builtin_amdgcn_sched_barrier(0);
    int slot = 1;
    for (int t = 1; t < K2_NT; t += 2) {
      BARRIER();
      rd(afB, bfB, slot);
      __builtin_amdgcn_sched_barrier(0);
      mm(afA, bfA);
      __builtin_amdgcn_sched_barrier(0);
      WAIT_L0();
      __builtin_amdgcn_sched_barrier(0);
      slot = (slot == K2_NST - 1) ? 0 : slot + 1;
      BARRIER();
      rd(afA, bfA, slot);
      __builtin_amdgcn_sched_barrier(0);
      mm(afB, bfB);
      __builtin_amdgcn_sched_barrier(0);
      WAIT_L0();
      __builtin_amdgcn_sched_barrier(0);
      slot = (slot == K2_NST - 1) ? 0 : slot + 1;
    }
    mm(afA, bfA);
    BARRIER();
    float* red = (float*)smem + kq * 64 * K2_RED_LD;
#pragma unroll
    for (int mi = 0; mi < 4; ++mi)
#pragma unroll
      for (int ni = 0; ni < 4; ++ni)
        *(f32x4*)(red + (mi * 16 + fr) * K2_RED_LD + ni * 16 + fq * 4) = acc[ni][mi];
  }
  if (wid >= 4) BARRIER();
  __syncthreads();
  {
    const int m = tid >> 3, c = (tid & 7) * 8;
    f32x4 s0 = {0.f, 0.f, 0.f, 0.f}, s1 = {0.f, 0.f, 0.f, 0.f};
#pragma unroll
    for (int w = 0; w < 4; ++w) {
      const float* p = (const float*)smem + w * 64 * K2_RED_LD + m * K2_RED_LD + c;
      s0 += *(const f32x4*)p;
      s1 += *(const f32x4*)(p + 4);
    }
    const size_t o = (size_t)(row0 + m) * D_ + n0 + c;
    u32x4 xv = *(const u32x4*)(x + o);
    u32x4 r;
    r[0] = pack2(s0[0] + bflo(xv[0]), s0[1] + bfhi(xv[0]));
    r[1] = pack2(s0[2] + bflo(xv[1]), s0[3] + bfhi(xv[1]));
    r[2] = pack2(s1[0] + bflo(xv[2]), s1[1] + bfhi(xv[2]));
    r[3] = pack2(s1[2] + bflo(xv[3]), s1[3] + bfhi(xv[3]));
    *(u32x4*)(out + o) = r;
  }
}

extern "C" void kernel_launch(void* const* d_in, const int* in_sizes, int n_in, void* d_out, int out_size,
                              void* d_ws, size_t ws_size, hipStream_t stream) {
  const u16* x = (const u16*)d_in[0];
  const u16* ws_re = (const u16*)d_in[1];
  const u16* ws_im = (const u16*)d_in[2];
  const u16* wd_re = (const u16*)d_in[3];
  const u16* wd_im = (const u16*)d_in[4];
  const u16* w_gate = (const u16*)d_in[6];
  const u16* w_up = (const u16*)d_in[7];
  const u16* w_down = (const u16*)d_in[8];
  u16* ab = (u16*)d_ws;
  u16* h = ab + (size_t)2 * S_ * D_;
  k1_kernel<<<240, 768, 0, stream>>>(x, wd_re, wd_im, w_gate, w_up, ab, h);
  k2_kernel<<<256, 512, 0, stream>>>(x, ws_re, ws_im, w_down, ab, h, (u16*)d_out);
}
```

```cpp
#include <hip/hip_runtime.h>
#include <hip/hip_bf16.h>
#include <stdint.h>

typedef unsigned short u16;
typedef short bf16x8 __attribute__((ext_vector_type(8)));
typedef short bf16x4 __attribute__((ext_vector_type(4)));
typedef float f32x4 __attribute__((ext_vector_type(4)));
typedef float f32x2 __attribute__((ext_vector_type(2)));
typedef unsigned u32x4 __attribute__((ext_vector_type(4)));
typedef unsigned u32x2 __attribute__((ext_vector_type(2)));
typedef __bf16 bf2_t __attribute__((ext_vector_type(2)));

constexpr int S_ = 2048, D_ = 512, F_ = 1376, FP_ = 1408;

#define LDS_AS __attribute__((address_space(3)))
#define GLDS16(gptr, ldsptr) \
  __builtin_amdgcn_global_load_lds((const unsigned*)(gptr), (unsigned*)(ldsptr), 16, 0, 0)
#define WAIT_V(n) asm volatile("s_waitcnt vmcnt(" #n ")" ::: "memory")
#define BARRIER() asm volatile("s_barrier" ::: "memory")
#define WAIT_L0() asm volatile("s_waitcnt lgkmcnt(0)" ::: "memory")

__device__ __forceinline__ float bf16r(float x) { return __bfloat162float(__float2bfloat16(x)); }
__device__ __forceinline__ unsigned pack2(float lo, float hi) {
  __hip_bfloat16 a = __float2bfloat16(lo), b = __float2bfloat16(hi);
  return (unsigned)__builtin_bit_cast(u16, a) | ((unsigned)__builtin_bit_cast(u16, b) << 16);
}
__device__ __forceinline__ float bflo(unsigned v) { return __builtin_bit_cast(float, v << 16); }
__device__ __forceinline__ float bfhi(unsigned v) { return __builtin_bit_cast(float, v & 0xffff0000u); }

__device__ __forceinline__ float dot2(unsigned a, unsigned b, float c) {
#if defined(__HIP_DEVICE_COMPILE__)
  return __builtin_amdgcn_fdot2_f32_bf16(__builtin_bit_cast(bf2_t, a), __builtin_bit_cast(bf2_t, b), c, false);
#else
  return c;
#endif
}
__device__ __forceinline__ bf16x4 tr_read(const char* p) {
#if defined(__HIP_DEVICE_COMPILE__)
  return __builtin_amdgcn_ds_read_tr16_b64_v4i16((LDS_AS bf16x4*)(p));
#else
  return bf16x4{};
#endif
}
template <int OFF>
__device__ __forceinline__ bf16x8 tr_frag(unsigned addr) {
  bf16x4 a, b;
  asm volatile("ds_read_b64_tr_b16 %0, %2 offset:%3\n\tds_read_b64_tr_b16 %1, %2 offset:%4"
               : "=&v"(a), "=&v"(b)
               : "v"(addr), "i"(OFF), "i"(OFF + 512));
  return __builtin_shufflevector(a, b, 0, 1, 2, 3, 4, 5, 6, 7);
}
template <int OFF>
__device__ __forceinline__ bf16x8 lds_rd128(unsigned addr) {
  bf16x8 a;
  asm volatile("ds_read_b128 %0, %1 offset:%2" : "=&v"(a) : "v"(addr), "i"(OFF));
  return a;
}
__device__ __forceinline__ unsigned lds_addr(const void* p) {
  return (unsigned)(uintptr_t)(const LDS_AS char*)p;
}
__device__ __forceinline__ bf16x8 cat8(bf16x4 a, bf16x4 b) {
  return __builtin_shufflevector(a, b, 0, 1, 2, 3, 4, 5, 6, 7);
}
__device__ __forceinline__ float gelu_tanh(float g) {
  const float c1 = -2.0f * 0.7978845608028654f * 1.4426950408889634f;
  const float c2 = c1 * 0.044715f;
  float w = g * __builtin_fmaf(g * g, c2, c1);
  float e = __builtin_amdgcn_exp2f(w);
  return g * __builtin_amdgcn_rcpf(1.0f + e);
}

constexpr int K1_A = 32768, K1_B = 16384, K1_STAGE = K1_A + K1_B;

__global__ __launch_bounds__(768, 3) void k1_kernel(const u16* __restrict__ x, const u16* __restrict__ wd_re,
                                                    const u16* __restrict__ wd_im, const u16* __restrict__ w_gate,
                                                    const u16* __restrict__ w_up, u16* __restrict__ ab,
                                                    u16* __restrict__ h) {
  __shared__ __attribute__((aligned(1024))) char smem[3 * K1_STAGE];
  const int tid = threadIdx.x, wid = __builtin_amdgcn_readfirstlane(tid >> 6), lane = tid & 63;
  const int fr = lane & 15, fq = lane >> 4;
  const int bid = blockIdx.x;
  const int nb = (bid & 7) * 30 + (bid >> 3);
  const int cb = nb >> 3, rb = nb & 7;
  const bool nk = cb < 8;
  const int row0 = rb * 256;
  const int f0 = (cb - 8) * 64;
  const int sw = (fr >> 1) & 7;
  const int ch0 = ((fq) ^ sw) * 16, ch1 = ((4 + fq) ^ sw) * 16;
  const int tq = (lane >> 2) & 3, tp = lane & 3;
  const int tswz = ((tq >> 1) & 1) | ((fq & 1) << 1);
  const int tr_row = (8 * fq + tq) * 128 + 8 * tp;
  float* sst = (float*)(smem + 2 * K1_STAGE);
  float* scs = (float*)(smem + 2 * K1_STAGE + 8192);

  if (wid >= 8) {
    const int lw = wid - 8;
    const int l3 = lane >> 3, l7 = lane & 7;
    const int rt_c0 = (l7 ^ (fq & 7)) * 8, rt_c1 = (l7 ^ ((fq + 4) & 7)) * 8;
    const int kt_c0 = (l7 ^ ((fq & 1) << 1)) * 8, kt_c1 = (l7 ^ (((fq & 1) | 2) << 1)) * 8;
    const u16* xa0 = x + (size_t)(row0 + lw * 64 + l3) * D_ + rt_c0;
    const u16* xa1 = x + (size_t)(row0 + lw * 64 + 8 + l3) * D_ + rt_c1;
    const u16 *wb0, *wb1;
    int wstep_j, wstep_t;
    if (nk) {
      const u16* wnk = (cb < 4 ? wd_re : wd_im) + (size_t)((cb & 3) * 128 + lw * 32 + l3) * D_;
      wb0 = wnk + rt_c0;
      wb1 = wnk + 8 * D_ + rt_c1;
      wstep_j = 16 * D_;
      wstep_t = 64;
    } else {
      const u16* w = (lw < 2) ? w_gate : w_up;
      int c0 = f0 + kt_c0, c1 = f0 + kt_c1;
      if (c0 >= F_) c0 -= 32;
      if (c1 >= F_) c1 -= 32;
      const u16* wk = w + (size_t)((lw & 1) * 32 + l3) * F_;
      wb0 = wk + c0;
      wb1 = wk + 8 * F_ + c1;
      wstep_j = 16 * F_;
      wstep_t = 64 * F_;
    }
    auto stage = [&](int t, int buf) {
      char* sa = smem + buf * K1_STAGE + lw * 8192;
      char* sb = smem + buf * K1_STAGE + K1_A + lw * 4096;
#pragma unroll
      for (int i = 0; i < 4; ++i) {
        GLDS16(xa0 + i * 16 * D_ + t * 64, sa + i * 2048);
        GLDS16(xa1 + i * 16 * D_ + t * 64, sa + i * 2048 + 1024);
      }
#pragma unroll
      for (int i = 0; i < 2; ++i) {
        GLDS16(wb0 + i * wstep_j + t * wstep_t, sb + i * 2048);
        GLDS16(wb1 + i * wstep_j + t * wstep_t, sb + i * 2048 + 1024);
      }
    };
    stage(0, 0);
    stage(1, 1);
    f32x4 accS[4], accG[4], accC[2];
#pragma unroll
    for (int i = 0; i < 4; ++i) {
      accS[i] = f32x4{0.f, 0.f, 0.f, 0.f};
      accG[i] = f32x4{0.f, 0.f, 0.f, 0.f};
    }
    accC[0] = f32x4{0.f, 0.f, 0.f, 0.f};
    accC[1] = f32x4{0.f, 0.f, 0.f, 0.f};
    const short ONE = 0x3f80;
    const bf16x8 ones = {ONE, ONE, ONE, ONE, ONE, ONE, ONE, ONE};
    const unsigned la0 = lds_addr(smem) + (lw * 64 + fr) * 128 + ch0;
    const unsigned la1 = lds_addr(smem) + (lw * 64 + fr) * 128 + ch1;
    const unsigned lb0 = lds_addr(smem) + K1_A + (lw * 32 + fr) * 128 + ch0;
    const unsigned lb1 = lds_addr(smem) + K1_A + (lw * 32 + fr) * 128 + ch1;
    const unsigned lt0 = lds_addr(smem) + K1_A + (lw >> 1) * 8192 + tr_row + 32 * (((lw & 1) * 2) ^ tswz);
    const unsigned lt1 = lds_addr(smem) + K1_A + (lw >> 1) * 8192 + tr_row + 32 * (((lw & 1) * 2 + 1) ^ tswz);
    int ib = 2, buf = 0;
    for (int t = 0; t < 8; ++t) {
      if (t < 7) WAIT_V(12); else WAIT_V(0);
      BARRIER();
      if (t + 2 < 8) stage(t + 2, ib);
      ib = (ib == 2) ? 0 : ib + 1;
      const unsigned so = buf * K1_STAGE;
      bf16x8 xf[8], wf[4];
      xf[0] = lds_rd128<0>(la0 + so); xf[1] = lds_rd128<2048>(la0 + so);
      xf[2] = lds_rd128<4096>(la0 + so); xf[3] = lds_rd128<6144>(la0 + so);
      xf[4] = lds_rd128<0>(la1 + so); xf[5] = lds_rd128<2048>(la1 + so);
      xf[6] = lds_rd128<4096>(la1 + so); xf[7] = lds_rd128<6144>(la1 + so);
      if (nk) {
        wf[0] = lds_rd128<0>(lb0 + so); wf[1] = lds_rd128<2048>(lb0 + so);
        wf[2] = lds_rd128<0>(lb1 + so); wf[3] = lds_rd128<2048>(lb1 + so);
      } else {
        wf[0] = tr_frag<0>(lt0 + so); wf[1] = tr_frag<0>(lt1 + so);
        wf[2] = tr_frag<4096>(lt0 + so); wf[3] = tr_frag<4096>(lt1 + so);
      }
      WAIT_L0();
      __builtin_amdgcn_sched_barrier(0);
#pragma unroll
      for (int i = 0; i < 8; ++i) {
        accS[i & 3] = __builtin_amdgcn_mfma_f32_16x16x32_bf16(ones, xf[i], accS[i & 3], 0, 0, 0);
        accG[i & 3] = __builtin_amdgcn_mfma_f32_16x16x32_bf16(xf[i], xf[i], accG[i & 3], 0, 0, 0);
      }
#pragma unroll
      for (int i = 0; i < 4; ++i)
        accC[i & 1] = __builtin_amdgcn_mfma_f32_16x16x32_bf16(wf[i], ones, accC[i & 1], 0, 0, 0);
      buf = (buf == 2) ? 0 : buf + 1;
    }
    if (fq == (fr >> 2)) {
#pragma unroll
      for (int i = 0; i < 4; ++i) {
        const int r = fr & 3;
        const float q = r == 0 ? accG[i][0] : r == 1 ? accG[i][1] : r == 2 ? accG[i][2] : accG[i][3];
        const float mean = accS[i][0] * (1.0f / D_);
        const float var = fmaxf(q * (1.0f / D_) - mean * mean, 0.f);
        const float sd = bf16r(sqrtf(bf16r(bf16r(var) + 1e-5f)));
        f32x2 o = {bf16r(mean), __builtin_amdgcn_rcpf(sd)};
        *(f32x2*)(sst + (lw * 64 + i * 16 + fr) * 2) = o;
      }
    }
    if (fr == 0) {
      *(f32x4*)(scs + lw * 32 + fq * 4) = accC[0];
      *(f32x4*)(scs + lw * 32 + 16 + fq * 4) = accC[1];
    }
    WAIT_L0();
    BARRIER();
    return;
  }

  const int wm = wid >> 1, wn = wid & 1;
  f32x4 acc[4][4];
#pragma unroll
  for (int i = 0; i < 4; ++i)
#pragma unroll
    for (int j = 0; j < 4; ++j) acc[i][j] = f32x4{0.f, 0.f, 0.f, 0.f};
  const int a_off = (wm * 64 + fr) * 128;
  const int bnk_off = (wn * 64 + fr) * 128;

  int buf = 0;
  for (int t = 0; t < 8; ++t) {
    BARRIER();
    const char* sa = smem + buf * K1_STAGE;
    const char* sb = sa + K1_A;
#pragma unroll
    for (int ks = 0; ks < 2; ++ks) {
      bf16x8 xf[4], wf[4];
      const int ch = ks ? ch1 : ch0;
#pragma unroll
      for (int mi = 0; mi < 4; ++mi) xf[mi] = *(const bf16x8*)(sa + a_off + mi * 2048 + ch);
      if (nk) {
#pragma unroll
        for (int ni = 0; ni < 4; ++ni) wf[ni] = *(const bf16x8*)(sb + bnk_off + ni * 2048 + ch);
      } else {
        const unsigned p0 = lds_addr(sb) + tr_row + 32 * ((wn * 2) ^ tswz);
        const unsigned p1 = lds_addr(sb) + tr_row + 32 * ((wn * 2 + 1) ^ tswz);
        if (ks == 0) {
          wf[0] = tr_frag<0>(p0); wf[1] = tr_frag<0>(p1); wf[2] = tr_frag<8192>(p0); wf[3] = tr_frag<8192>(p1);
        } else {
          wf[0] = tr_frag<4096>(p0); wf[1] = tr_frag<4096>(p1); wf[2] = tr_frag<12288>(p0); wf[3] = tr_frag<12288>(p1);
        }
      }
      WAIT_L0();
      __builtin_amdgcn_sched_barrier(0);
#pragma unroll
      for (int ni = 0; ni < 4; ++ni)
#pragma unroll
        for (int mi = 0; mi < 4; ++mi)
          acc[ni][mi] = __builtin_amdgcn_mfma_f32_16x16x32_bf16(wf[ni], xf[mi], acc[ni][mi], 0, 0, 0);
    }
    buf = (buf == 2) ? 0 : buf + 1;
  }
  BARRIER();
  float mb[4], rinv[4];
#pragma unroll
  for (int mi = 0; mi < 4; ++mi) {
    f32x2 v = *(const f32x2*)(sst + (wm * 64 + mi * 16 + fr) * 2);
    mb[mi] = v[0];
    rinv[mi] = v[1];
  }
  f32x4 csv[4];
#pragma unroll
  for (int ni = 0; ni < 4; ++ni) {
    const int ci = nk ? (wn * 64 + ni * 16) : ((ni >> 1) * 64 + wn * 32 + (ni & 1) * 16);
    csv[ni] = *(const f32x4*)(scs + ci + fq * 4);
  }
  if (nk) {
    const float sgn = cb < 4 ? 1.0f : -1.0f;
    u16* dst = ab + (size_t)(cb >> 2) * (S_ * D_) + (size_t)(row0 + wm * 64 + fr) * D_ + (cb & 3) * 128 + wn * 64 + fq * 4;
#pragma unroll
    for (int mi = 0; mi < 4; ++mi) {
      const float r = rinv[mi] * sgn;
#pragma unroll
      for (int ni = 0; ni < 4; ++ni) {
        f32x4 v = (acc[ni][mi] - mb[mi] * csv[ni]) * r;
        u32x2 o = {pack2(v[0], v[1]), pack2(v[2], v[3])};
        *(u32x2*)(dst + mi * 16 * D_ + ni * 16) = o;
      }
    }
  } else {
    const int fc = f0 + wn * 32 + fq * 4;
    u16* dst = h + (size_t)(row0 + wm * 64 + fr) * FP_ + fc;
    const unsigned vmask = (f0 + wn * 32) < F_ ? 0xffffffffu : 0u;
#pragma unroll
    for (int mi = 0; mi < 4; ++mi) {
#pragma unroll
      for (int ni = 0; ni < 2; ++ni) {
        f32x4 g = (acc[ni][mi] - mb[mi] * csv[ni]) * rinv[mi];
        f32x4 u = (acc[ni + 2][mi] - mb[mi] * csv[ni + 2]) * rinv[mi];
        float hv[4];
#pragma unroll
        for (int e = 0; e < 4; ++e) {
          float gl = bf16r(gelu_tanh(bf16r(g[e])));
          hv[e] = gl * bf16r(u[e]);
        }
        u32x2 o = {pack2(hv[0], hv[1]) & vmask, pack2(hv[2], hv[3]) & vmask};
        *(u32x2*)(dst + mi * 16 * FP_ + ni * 16) = o;
      }
    }
  }
}

constexpr int K2_STAGE = 32768, K2_NST = 5, K2_NT = 43;
constexpr int K2_RED_LD = 68;
#define K2_PF 12

__device__ __forceinline__ void touch(const void* p, unsigned& sink) {
  asm volatile("global_load_dword %0, %1, off" : "+v"(sink) : "v"(p) : "memory");
}

__global__ __launch_bounds__(576, 3) void k2_kernel(const u16* __restrict__ x, const u16* __restrict__ ws_re,
                                                    const u16* __restrict__ ws_im, const u16* __restrict__ w_down,
                                                    const u16* __restrict__ ab, const u16* __restrict__ h,
                                                    u16* __restrict__ out) {
  __shared__ __attribute__((aligned(1024))) char smem[K2_NST * K2_STAGE];
  const int tid = threadIdx.x, wid = __builtin_amdgcn_readfirstlane(tid >> 6), lane = tid & 63;
  const int fr = lane & 15, fq = lane >> 4;
  const int bid = blockIdx.x;
  const int idx = bid >> 3;
  const int rb = (bid & 7) * 4 + (idx >> 3), cbk = idx & 7;
  const int row0 = rb * 64, n0 = cbk * 64;

  if (wid == 8) {
    const int l3 = lane >> 3, l7 = lane & 7;
    unsigned sink = 0;
    auto pf = [&](int tp) {
      if (tp >= K2_NT) return;
      if ((tp & 3) == 0) {
        const u16* A;
        int lda, k0;
        if (tp < 32) { A = (tp < 16 ? ws_re : ws_im); lda = S_; k0 = (tp & 15) * 128; }
        else { A = h; lda = FP_; k0 = (tp - 32) * 128; }
        touch(A + (size_t)(row0 + cbk * 8 + l3) * lda + min(k0 + l7 * 64, lda - 64), sink);
      }
      if ((tp & 1) == 0) {
        const u16* B;
        int k0, kmax;
        if (tp < 32) { B = ab + (size_t)(tp >> 4) * (S_ * D_); k0 = (tp & 15) * 128; kmax = S_ - 1; }
        else { B = w_down; k0 = (tp - 32) * 128; kmax = F_ - 1; }
        touch(B + (size_t)min(k0 + idx * 8 + l3, kmax) * D_ + l7 * 64, sink);
      }
    };
    for (int tp = K2_NST - 1; tp < K2_PF; ++tp) pf(tp);
    for (int t = 0; t < K2_NT; ++t) {
      BARRIER();
      pf(t + K2_PF);
    }
    WAIT_V(0);
    asm volatile("" ::"v"(sink));
  } else if (wid >= 4) {
    const int lw = wid - 4;
    const int l3 = lane >> 3, l7 = lane & 7;
    const int rt_c0 = (l7 ^ (fq & 7)) * 8, rt_c1 = (l7 ^ ((fq + 4) & 7)) * 8;
    const int kt_c0 = (l7 ^ ((fq & 1) << 1)) * 8, kt_c1 = (l7 ^ (((fq & 1) | 2) << 1)) * 8;
    const int sub = lw & 1;
    auto stage = [&](int t, int slot) {
      char* dst = smem + slot * K2_STAGE + lw * 8192;
      if (lw < 2) {
        const u16* A;
        int lda, k0;
        if (t < 32) { A = (t < 16 ? ws_re : ws_im) + (size_t)row0 * S_; lda = S_; k0 = (t & 15) * 128; }
        else { A = h + (size_t)row0 * FP_; lda = FP_; k0 = (t - 32) * 128; }
        k0 += sub * 64;
#pragma unroll
        for (int i = 0; i < 8; ++i) {
          const u16* src = A + (i * 8 + l3) * lda + k0 + ((i & 1) ? rt_c1 : rt_c0);
          GLDS16(src, dst + i * 1024);
        }
      } else {
        const u16* B;
        int k0, kmax;
        if (t < 32) { B = ab + (size_t)(t >> 4) * (S_ * D_); k0 = (t & 15) * 128; kmax = S_ - 1; }
        else { B = w_down; k0 = (t - 32) * 128; kmax = F_ - 1; }
        k0 += sub * 64;
#pragma unroll
        for (int i = 0; i < 8; ++i) {
          const int krow = min(k0 + i * 8 + l3, kmax);
          const u16* src = B + krow * D_ + n0 + ((i & 1) ? kt_c1 : kt_c0);
          GLDS16(src, dst + i * 1024);
        }
      }
    };
#pragma unroll
    for (int s = 0; s < K2_NST - 1; ++s) stage(s, s);
    int islot = K2_NST - 1;
    for (int t = 0; t < K2_NT; ++t) {
      if (t < K2_NT - 3) WAIT_V(24);
      else if (t == K2_NT - 3) WAIT_V(16);
      else if (t == K2_NT - 2) WAIT_V(8);
      else WAIT_V(0);
      BARRIER();
      if (t + K2_NST - 1 < K2_NT) stage(t + K2_NST - 1, islot);
      islot = (islot == K2_NST - 1) ? 0 : islot + 1;
    }
  } else {
    f32x4 acc[4][4];
#pragma unroll
    for (int i = 0; i < 4; ++i)
#pragma unroll
      for (int j = 0; j < 4; ++j) acc[i][j] = f32x4{0.f, 0.f, 0.f, 0.f};

    const int kq = wid;
    const int sw = (fr >> 1) & 7;
    const unsigned a_base = lds_addr(smem) + (kq >> 1) * 8192 + fr * 128 + (((kq & 1) * 4 + fq) ^ sw) * 16;
    const int tq = (lane >> 2) & 3, tp = lane & 3;
    const int tswz = ((tq >> 1) & 1) | ((fq & 1) << 1);
    const unsigned b_base = lds_addr(smem) + 16384 + (kq >> 1) * 8192 + (kq & 1) * 4096 + (8 * fq + tq) * 128 + 8 * tp;
    unsigned b_off[4];
#pragma unroll
    for (int ni = 0; ni < 4; ++ni) b_off[ni] = b_base + 32 * (ni ^ tswz);

    bf16x8 afA[4], bfA[4], afB[4], bfB[4];
    auto rd = [&](bf16x8* af, bf16x8* bfr, int slot) {
      const unsigned so = slot * K2_STAGE;
      af[0] = lds_rd128<0>(a_base + so);
      af[1] = lds_rd128<2048>(a_base + so);
      af[2] = lds_rd128<4096>(a_base + so);
      af[3] = lds_rd128<6144>(a_base + so);
#pragma unroll
      for (int ni = 0; ni < 4; ++ni) bfr[ni] = tr_frag<0>(b_off[ni] + so);
    };
    auto mm = [&](const bf16x8* af, const bf16x8* bfr) {
#pragma unroll
      for (int ni = 0; ni < 4; ++ni)
#pragma unroll
        for (int mi = 0; mi < 4; ++mi)
          acc[ni][mi] = __builtin_amdgcn_mfma_f32_16x16x32_bf16(bfr[ni], af[mi], acc[ni][mi], 0, 0, 0);
    };
    BARRIER();
    rd(afA, bfA, 0);
    WAIT_L0();
    __builtin_amdgcn_sched_barrier(0);
    int slot = 1;
    for (int t = 1; t < K2_NT; t += 2) {
      BARRIER();
      rd(afB, bfB, slot);
      __builtin_amdgcn_sched_barrier(0);
      mm(afA, bfA);
      __builtin_amdgcn_sched_barrier(0);
      WAIT_L0();
      __builtin_amdgcn_sched_barrier(0);
      slot = (slot == K2_NST - 1) ? 0 : slot + 1;
      BARRIER();
      rd(afA, bfA, slot);
      __builtin_amdgcn_sched_barrier(0);
      mm(afB, bfB);
      __builtin_amdgcn_sched_barrier(0);
      WAIT_L0();
      __builtin_amdgcn_sched_barrier(0);
      slot = (slot == K2_NST - 1) ? 0 : slot + 1;
    }
    mm(afA, bfA);
    BARRIER();
    float* red = (float*)smem + kq * 64 * K2_RED_LD;
#pragma unroll
    for (int mi = 0; mi < 4; ++mi)
#pragma unroll
      for (int ni = 0; ni < 4; ++ni)
        *(f32x4*)(red + (mi * 16 + fr) * K2_RED_LD + ni * 16 + fq * 4) = acc[ni][mi];
  }
  if (wid >= 4) BARRIER();
  __syncthreads();
  if (wid < 8) {
    const int m = tid >> 3, c = (tid & 7) * 8;
    f32x4 s0 = {0.f, 0.f, 0.f, 0.f}, s1 = {0.f, 0.f, 0.f, 0.f};
#pragma unroll
    for (int w = 0; w < 4; ++w) {
      const float* p = (const float*)smem + w * 64 * K2_RED_LD + m * K2_RED_LD + c;
      s0 += *(const f32x4*)p;
      s1 += *(const f32x4*)(p + 4);
    }
    const size_t o = (size_t)(row0 + m) * D_ + n0 + c;
    u32x4 xv = *(const u32x4*)(x + o);
    u32x4 r;
    r[0] = pack2(s0[0] + bflo(xv[0]), s0[1] + bfhi(xv[0]));
    r[1] = pack2(s0[2] + bflo(xv[1]), s0[3] + bfhi(xv[1]));
    r[2] = pack2(s1[0] + bflo(xv[2]), s1[1] + bfhi(xv[2]));
    r[3] = pack2(s1[2] + bflo(xv[3]), s1[3] + bfhi(xv[3]));
    *(u32x4*)(out + o) = r;
  }
}

extern "C" void kernel_launch(void* const* d_in, const int* in_sizes, int n_in, void* d_out, int out_size,
                              void* d_ws, size_t ws_size, hipStream_t stream) {
  const u16* x = (const u16*)d_in[0];
  const u16* ws_re = (const u16*)d_in[1];
  const u16* ws_im = (const u16*)d_in[2];
  const u16* wd_re = (const u16*)d_in[3];
  const u16* wd_im = (const u16*)d_in[4];
  const u16* w_gate = (const u16*)d_in[6];
  const u16* w_up = (const u16*)d_in[7];
  const u16* w_down = (const u16*)d_in[8];
  u16* ab = (u16*)d_ws;
  u16* h = ab + (size_t)2 * S_ * D_;
  k1_kernel<<<240, 768, 0, stream>>>(x, wd_re, wd_im, w_gate, w_up, ab, h);
  k2_kernel<<<256, 576, 0, stream>>>(x, ws_re, ws_im, w_down, ab, h, (u16*)d_out);
}
```

```cpp
#include <hip/hip_runtime.h>
#include <hip/hip_bf16.h>
#include <stdint.h>

typedef unsigned short u16;
typedef short bf16x8 __attribute__((ext_vector_type(8)));
typedef short bf16x4 __attribute__((ext_vector_type(4)));
typedef float f32x4 __attribute__((ext_vector_type(4)));
typedef float f32x2 __attribute__((ext_vector_type(2)));
typedef unsigned u32x4 __attribute__((ext_vector_type(4)));
typedef unsigned u32x2 __attribute__((ext_vector_type(2)));
typedef __bf16 bf2_t __attribute__((ext_vector_type(2)));

#define K2_PF 12
#define K2_ROT 1
#define K1_ROT 1
constexpr int S_ = 2048, D_ = 512, F_ = 1376, FP_ = 1408;

#define LDS_AS __attribute__((address_space(3)))
#define GLDS16(gptr, ldsptr) \
  __builtin_amdgcn_global_load_lds((const unsigned*)(gptr), (unsigned*)(ldsptr), 16, 0, 0)
#define WAIT_V(n) asm volatile("s_waitcnt vmcnt(" #n ")" ::: "memory")
#define BARRIER() asm volatile("s_barrier" ::: "memory")
#define WAIT_L0() asm volatile("s_waitcnt lgkmcnt(0)" ::: "memory")

__device__ __forceinline__ float bf16r(float x) { return __bfloat162float(__float2bfloat16(x)); }
__device__ __forceinline__ unsigned pack2(float lo, float hi) {
  __hip_bfloat16 a = __float2bfloat16(lo), b = __float2bfloat16(hi);
  return (unsigned)__builtin_bit_cast(u16, a) | ((unsigned)__builtin_bit_cast(u16, b) << 16);
}
__device__ __forceinline__ float bflo(unsigned v) { return __builtin_bit_cast(float, v << 16); }
__device__ __forceinline__ float bfhi(unsigned v) { return __builtin_bit_cast(float, v & 0xffff0000u); }

__device__ __forceinline__ float dot2(unsigned a, unsigned b, float c) {
#if defined(__HIP_DEVICE_COMPILE__)
  return __builtin_amdgcn_fdot2_f32_bf16(__builtin_bit_cast(bf2_t, a), __builtin_bit_cast(bf2_t, b), c, false);
#else
  return c;
#endif
}
__device__ __forceinline__ bf16x4 tr_read(const char* p) {
#if defined(__HIP_DEVICE_COMPILE__)
  return __builtin_amdgcn_ds_read_tr16_b64_v4i16((LDS_AS bf16x4*)(p));
#else
  return bf16x4{};
#endif
}
template <int OFF>
__device__ __forceinline__ bf16x8 tr_frag(unsigned addr) {
  bf16x4 a, b;
  asm volatile("ds_read_b64_tr_b16 %0, %2 offset:%3\n\tds_read_b64_tr_b16 %1, %2 offset:%4"
               : "=&v"(a), "=&v"(b)
               : "v"(addr), "i"(OFF), "i"(OFF + 512));
  return __builtin_shufflevector(a, b, 0, 1, 2, 3, 4, 5, 6, 7);
}
template <int OFF>
__device__ __forceinline__ bf16x8 lds_rd128(unsigned addr) {
  bf16x8 a;
  asm volatile("ds_read_b128 %0, %1 offset:%2" : "=&v"(a) : "v"(addr), "i"(OFF));
  return a;
}
__device__ __forceinline__ unsigned lds_addr(const void* p) {
  return (unsigned)(uintptr_t)(const LDS_AS char*)p;
}
__device__ __forceinline__ bf16x8 cat8(bf16x4 a, bf16x4 b) {
  return __builtin_shufflevector(a, b, 0, 1, 2, 3, 4, 5, 6, 7);
}
__device__ __forceinline__ float gelu_tanh(float g) {
  const float c1 = -2.0f * 0.7978845608028654f * 1.4426950408889634f;
  const float c2 = c1 * 0.044715f;
  float w = g * __builtin_fmaf(g * g, c2, c1);
  float e = __builtin_amdgcn_exp2f(w);
  return g * __builtin_amdgcn_rcpf(1.0f + e);
}

constexpr int K1_A = 32768, K1_B = 16384, K1_STAGE = K1_A + K1_B;

__global__ __launch_bounds__(768, 3) void k1_kernel(const u16* __restrict__ x, const u16* __restrict__ wd_re,
                                                    const u16* __restrict__ wd_im, const u16* __restrict__ w_gate,
                                                    const u16* __restrict__ w_up, u16* __restrict__ ab,
                                                    u16* __restrict__ h, unsigned* __restrict__ cnt) {
  __shared__ __attribute__((aligned(1024))) char smem[3 * K1_STAGE];
  const int tid = threadIdx.x, wid = __builtin_amdgcn_readfirstlane(tid >> 6), lane = tid & 63;
  const int fr = lane & 15, fq = lane >> 4;
  const int bid = blockIdx.x;
  const int nb = (bid & 7) * 30 + (bid >> 3);
  const int cb = nb >> 3, rb = nb & 7;
  const bool nk = cb < 8;
  const int row0 = rb * 256;
  const int f0 = (cb - 8) * 64;
  if (bid == 0 && tid < 64) cnt[tid * 64] = 0u;
  const int sw = (fr >> 1) & 7;
  const int ch0 = ((fq) ^ sw) * 16, ch1 = ((4 + fq) ^ sw) * 16;
  const int tq = (lane >> 2) & 3, tp = lane & 3;
  const int tswz = ((tq >> 1) & 1) | ((fq & 1) << 1);
  const int tr_row = (8 * fq + tq) * 128 + 8 * tp;
  float* sst = (float*)(smem + 2 * K1_STAGE);
  float* scs = (float*)(smem + 2 * K1_STAGE + 8192);

  if (wid >= 8) {
    const int lw = wid - 8;
    __builtin_amdgcn_s_setprio(2);
    const int l3 = lane >> 3, l7 = lane & 7;
    const int rt_c0 = (l7 ^ (fq & 7)) * 8, rt_c1 = (l7 ^ ((fq + 4) & 7)) * 8;
    const int kt_c0 = (l7 ^ ((fq & 1) << 1)) * 8, kt_c1 = (l7 ^ (((fq & 1) | 2) << 1)) * 8;
    const u16* xa0 = x + (size_t)(row0 + lw * 64 + l3) * D_ + rt_c0;
    const u16* xa1 = x + (size_t)(row0 + lw * 64 + 8 + l3) * D_ + rt_c1;
    const u16 *wb0, *wb1;
    int wstep_j, wstep_t;
    if (nk) {
      const u16* wnk = (cb < 4 ? wd_re : wd_im) + (size_t)((cb & 3) * 128 + lw * 32 + l3) * D_;
      wb0 = wnk + rt_c0;
      wb1 = wnk + 8 * D_ + rt_c1;
      wstep_j = 16 * D_;
      wstep_t = 64;
    } else {
      const u16* w = (lw < 2) ? w_gate : w_up;
      int c0 = f0 + kt_c0, c1 = f0 + kt_c1;
      if (c0 >= F_) c0 -= 32;
      if (c1 >= F_) c1 -= 32;
      const u16* wk = w + (size_t)((lw & 1) * 32 + l3) * F_;
      wb0 = wk + c0;
      wb1 = wk + 8 * F_ + c1;
      wstep_j = 16 * F_;
      wstep_t = 64 * F_;
    }
    auto stage = [&](int tj, int buf) {
      const int t = (tj + K1_ROT * cb) & 7;
      char* sa = smem + buf * K1_STAGE + lw * 8192;
      char* sb = smem + buf * K1_STAGE + K1_A + lw * 4096;
#pragma unroll
      for (int i = 0; i < 4; ++i) {
        GLDS16(xa0 + i * 16 * D_ + t * 64, sa + i * 2048);
        GLDS16(xa1 + i * 16 * D_ + t * 64, sa + i * 2048 + 1024);
      }
#pragma unroll
      for (int i = 0; i < 2; ++i) {
        GLDS16(wb0 + i * wstep_j + t * wstep_t, sb + i * 2048);
        GLDS16(wb1 + i * wstep_j + t * wstep_t, sb + i * 2048 + 1024);
      }
    };
    stage(0, 0);
    stage(1, 1);
    f32x4 accS[4], accG[4], accC[2];
#pragma unroll
    for (int i = 0; i < 4; ++i) {
      accS[i] = f32x4{0.f, 0.f, 0.f, 0.f};
      accG[i] = f32x4{0.f, 0.f, 0.f, 0.f};
    }
    accC[0] = f32x4{0.f, 0.f, 0.f, 0.f};
    accC[1] = f32x4{0.f, 0.f, 0.f, 0.f};
    const short ONE = 0x3f80;
    const bf16x8 ones = {ONE, ONE, ONE, ONE, ONE, ONE, ONE, ONE};
    const unsigned la0 = lds_addr(smem) + (lw * 64 + fr) * 128 + ch0;
    const unsigned la1 = lds_addr(smem) + (lw * 64 + fr) * 128 + ch1;
    const unsigned lb0 = lds_addr(smem) + K1_A + (lw * 32 + fr) * 128 + ch0;
    const unsigned lb1 = lds_addr(smem) + K1_A + (lw * 32 + fr) * 128 + ch1;
    const unsigned lt0 = lds_addr(smem) + K1_A + (lw >> 1) * 8192 + tr_row + 32 * (((lw & 1) * 2) ^ tswz);
    const unsigned lt1 = lds_addr(smem) + K1_A + (lw >> 1) * 8192 + tr_row + 32 * (((lw & 1) * 2 + 1) ^ tswz);
    int ib = 2, buf = 0;
    for (int t = 0; t < 8; ++t) {
      if (t < 7) WAIT_V(12); else WAIT_V(0);
      BARRIER();
      const unsigned so = buf * K1_STAGE;
      bf16x8 xf[8], wf[4];
      xf[0] = lds_rd128<0>(la0 + so); xf[1] = lds_rd128<2048>(la0 + so);
      xf[2] = lds_rd128<4096>(la0 + so); xf[3] = lds_rd128<6144>(la0 + so);
      xf[4] = lds_rd128<0>(la1 + so); xf[5] = lds_rd128<2048>(la1 + so);
      xf[6] = lds_rd128<4096>(la1 + so); xf[7] = lds_rd128<6144>(la1 + so);
      if (nk) {
        wf[0] = lds_rd128<0>(lb0 + so); wf[1] = lds_rd128<2048>(lb0 + so);
        wf[2] = lds_rd128<0>(lb1 + so); wf[3] = lds_rd128<2048>(lb1 + so);
      } else {
        wf[0] = tr_frag<0>(lt0 + so); wf[1] = tr_frag<0>(lt1 + so);
        wf[2] = tr_frag<4096>(lt0 + so); wf[3] = tr_frag<4096>(lt1 + so);
      }
      if (t + 2 < 8) stage(t + 2, ib);
      ib = (ib == 2) ? 0 : ib + 1;
      WAIT_L0();
      __builtin_amdgcn_sched_barrier(0);
#pragma unroll
      for (int i = 0; i < 8; ++i) {
        accS[i & 3] = __builtin_amdgcn_mfma_f32_16x16x32_bf16(ones, xf[i], accS[i & 3], 0, 0, 0);
        accG[i & 3] = __builtin_amdgcn_mfma_f32_16x16x32_bf16(xf[i], xf[i], accG[i & 3], 0, 0, 0);
      }
#pragma unroll
      for (int i = 0; i < 4; ++i)
        accC[i & 1] = __builtin_amdgcn_mfma_f32_16x16x32_bf16(wf[i], ones, accC[i & 1], 0, 0, 0);
      buf = (buf == 2) ? 0 : buf + 1;
    }
    if (fq == (fr >> 2)) {
#pragma unroll
      for (int i = 0; i < 4; ++i) {
        const int r = fr & 3;
        const float q = r == 0 ? accG[i][0] : r == 1 ? accG[i][1] : r == 2 ? accG[i][2] : accG[i][3];
        const float mean = accS[i][0] * (1.0f / D_);
        const float var = fmaxf(q * (1.0f / D_) - mean * mean, 0.f);
        const float sd = bf16r(sqrtf(bf16r(bf16r(var) + 1e-5f)));
        f32x2 o = {bf16r(mean), __builtin_amdgcn_rcpf(sd)};
        *(f32x2*)(sst + (lw * 64 + i * 16 + fr) * 2) = o;
      }
    }
    if (fr == 0) {
      *(f32x4*)(scs + lw * 32 + fq * 4) = accC[0];
      *(f32x4*)(scs + lw * 32 + 16 + fq * 4) = accC[1];
    }
    WAIT_L0();
    BARRIER();
    return;
  }

  const int wm = wid >> 1, wn = wid & 1;
  f32x4 acc[4][4];
#pragma unroll
  for (int i = 0; i < 4; ++i)
#pragma unroll
    for (int j = 0; j < 4; ++j) acc[i][j] = f32x4{0.f, 0.f, 0.f, 0.f};
  const int a_off = (wm * 64 + fr) * 128;
  const int bnk_off = (wn * 64 + fr) * 128;

  int buf = 0;
  for (int t = 0; t < 8; ++t) {
    BARRIER();
    const char* sa = smem + buf * K1_STAGE;
    const char* sb = sa + K1_A;
#pragma unroll
    for (int ks = 0; ks < 2; ++ks) {
      bf16x8 xf[4], wf[4];
      const int ch = ks ? ch1 : ch0;
#pragma unroll
      for (int mi = 0; mi < 4; ++mi) xf[mi] = *(const bf16x8*)(sa + a_off + mi * 2048 + ch);
      if (nk) {
#pragma unroll
        for (int ni = 0; ni < 4; ++ni) wf[ni] = *(const bf16x8*)(sb + bnk_off + ni * 2048 + ch);
      } else {
        const unsigned p0 = lds_addr(sb) + tr_row + 32 * ((wn * 2) ^ tswz);
        const unsigned p1 = lds_addr(sb) + tr_row + 32 * ((wn * 2 + 1) ^ tswz);
        if (ks == 0) {
          wf[0] = tr_frag<0>(p0); wf[1] = tr_frag<0>(p1); wf[2] = tr_frag<8192>(p0); wf[3] = tr_frag<8192>(p1);
        } else {
          wf[0] = tr_frag<4096>(p0); wf[1] = tr_frag<4096>(p1); wf[2] = tr_frag<12288>(p0); wf[3] = tr_frag<12288>(p1);
        }
      }
      WAIT_L0();
      __builtin_amdgcn_sched_barrier(0);
#pragma unroll
      for (int ni = 0; ni < 4; ++ni)
#pragma unroll
        for (int mi = 0; mi < 4; ++mi)
          acc[ni][mi] = __builtin_amdgcn_mfma_f32_16x16x32_bf16(wf[ni], xf[mi], acc[ni][mi], 0, 0, 0);
    }
    buf = (buf == 2) ? 0 : buf + 1;
  }
  BARRIER();
  float mb[4], rinv[4];
#pragma unroll
  for (int mi = 0; mi < 4; ++mi) {
    f32x2 v = *(const f32x2*)(sst + (wm * 64 + mi * 16 + fr) * 2);
    mb[mi] = v[0];
    rinv[mi] = v[1];
  }
  f32x4 csv[4];
#pragma unroll
  for (int ni = 0; ni < 4; ++ni) {
    const int ci = nk ? (wn * 64 + ni * 16) : ((ni >> 1) * 64 + wn * 32 + (ni & 1) * 16);
    csv[ni] = *(const f32x4*)(scs + ci + fq * 4);
  }
  if (nk) {
    const float sgn = cb < 4 ? 1.0f : -1.0f;
    u16* dst = ab + (size_t)(cb >> 2) * (S_ * D_) + (size_t)(row0 + wm * 64 + fr) * D_ + (cb & 3) * 128 + wn * 64 + fq * 4;
#pragma unroll
    for (int mi = 0; mi < 4; ++mi) {
      const float r = rinv[mi] * sgn;
#pragma unroll
      for (int ni = 0; ni < 4; ++ni) {
        f32x4 v = (acc[ni][mi] - mb[mi] * csv[ni]) * r;
        u32x2 o = {pack2(v[0], v[1]), pack2(v[2], v[3])};
        *(u32x2*)(dst + mi * 16 * D_ + ni * 16) = o;
      }
    }
  } else {
    const int fc = f0 + wn * 32 + fq * 4;
    u16* dst = h + (size_t)(row0 + wm * 64 + fr) * FP_ + fc;
    const unsigned vmask = (f0 + wn * 32) < F_ ? 0xffffffffu : 0u;
#pragma unroll
    for (int mi = 0; mi < 4; ++mi) {
#pragma unroll
      for (int ni = 0; ni < 2; ++ni) {
        f32x4 g = (acc[ni][mi] - mb[mi] * csv[ni]) * rinv[mi];
        f32x4 u = (acc[ni + 2][mi] - mb[mi] * csv[ni + 2]) * rinv[mi];
        float hv[4];
#pragma unroll
        for (int e = 0; e < 4; ++e) {
          float gl = bf16r(gelu_tanh(bf16r(g[e])));
          hv[e] = gl * bf16r(u[e]);
        }
        u32x2 o = {pack2(hv[0], hv[1]) & vmask, pack2(hv[2], hv[3]) & vmask};
        *(u32x2*)(dst + mi * 16 * FP_ + ni * 16) = o;
      }
    }
  }
}

constexpr int K2_STAGE = 32768, K2_NST = 5;
constexpr int CNT_STRIDE = 64;

__global__ __launch_bounds__(512, 2) void k2_kernel(const u16* __restrict__ x, const u16* __restrict__ ws_re,
                                                    const u16* __restrict__ ws_im, const u16* __restrict__ w_down,
                                                    const u16* __restrict__ ab, const u16* __restrict__ h,
                                                    u16* __restrict__ out, float* slab, unsigned* cnt) {
  __shared__ __attribute__((aligned(1024))) char smem[K2_NST * K2_STAGE];
  const int tid = threadIdx.x, wid = __builtin_amdgcn_readfirstlane(tid >> 6), lane = tid & 63;
  const int fr = lane & 15, fq = lane >> 4;
  const int bid = blockIdx.x;
  const int idx = bid >> 3;
  const int q = (bid & 7) >> 1;
  const int rb = (bid & 1) * 8 + (idx >> 2), cbk = idx & 3;
  const int row0 = rb * 128, n0 = cbk * 128, tile = rb * 4 + cbk;
  const int t_begin = q < 2 ? 22 * q : 44 + 21 * (q - 2);
  const int nt = q < 2 ? 22 : 21;

  if (wid >= 4) {
    const int lw = wid - 4;
    const int l3 = lane >> 3, l7 = lane & 7;
    const int rt_c0 = (l7 ^ (fq & 7)) * 8, rt_c1 = (l7 ^ ((fq + 4) & 7)) * 8;
    const int kt_c0 = (l7 ^ ((fq & 1) << 1)) * 8, kt_c1 = (l7 ^ (((fq & 1) | 2) << 1)) * 8;
    const int sub = lw & 1;
    auto stage = [&](int j, int slot) {
      const int t = t_begin + j;
      char* dst = smem + slot * K2_STAGE + lw * 8192;
      if (lw < 2) {
        const u16* A;
        int lda, k0;
        if (t < 64) { A = (t < 32 ? ws_re : ws_im); lda = S_; k0 = (t & 31) * 64; }
        else { A = h; lda = FP_; k0 = (t - 64) * 64; }
        A += (size_t)(row0 + sub * 64) * lda;
#pragma unroll
        for (int i = 0; i < 8; ++i) {
          const u16* src = A + (i * 8 + l3) * lda + k0 + ((i & 1) ? rt_c1 : rt_c0);
          GLDS16(src, dst + i * 1024);
        }
      } else {
        const u16* B;
        int k0, kmax;
        if (t < 64) { B = ab + (size_t)(t >> 5) * (S_ * D_); k0 = (t & 31) * 64; kmax = S_ - 1; }
        else { B = w_down; k0 = (t - 64) * 64; kmax = F_ - 1; }
#pragma unroll
        for (int i = 0; i < 8; ++i) {
          const int krow = min(k0 + i * 8 + l3, kmax);
          const u16* src = B + krow * D_ + n0 + sub * 64 + ((i & 1) ? kt_c1 : kt_c0);
          GLDS16(src, dst + i * 1024);
        }
      }
    };
#pragma unroll
    for (int s = 0; s < K2_NST - 1; ++s) stage(s, s);
    int islot = K2_NST - 1;
    for (int j = 0; j < nt; ++j) {
      const int rem = nt - 1 - j;
      if (rem >= 3) WAIT_V(24);
      else if (rem == 2) WAIT_V(16);
      else if (rem == 1) WAIT_V(8);
      else WAIT_V(0);
      BARRIER();
      if (j + K2_NST - 1 < nt) stage(j + K2_NST - 1, islot);
      islot = (islot == K2_NST - 1) ? 0 : islot + 1;
    }
    return;
  }
  const int wm = wid >> 1, wn = wid & 1;
  f32x4 acc[4][4];
#pragma unroll
  for (int i = 0; i < 4; ++i)
#pragma unroll
    for (int j = 0; j < 4; ++j) acc[i][j] = f32x4{0.f, 0.f, 0.f, 0.f};

  const int sw = (fr >> 1) & 7;
  const unsigned a_base0 = lds_addr(smem) + (wm * 64 + fr) * 128 + ((fq) ^ sw) * 16;
  const unsigned a_base1 = lds_addr(smem) + (wm * 64 + fr) * 128 + ((4 + fq) ^ sw) * 16;
  const int tq = (lane >> 2) & 3, tp = lane & 3;
  const int tswz = ((tq >> 1) & 1) | ((fq & 1) << 1);
  const unsigned b_base = lds_addr(smem) + 16384 + wn * 8192 + (8 * fq + tq) * 128 + 8 * tp;
  unsigned b_off[4];
#pragma unroll
  for (int ni = 0; ni < 4; ++ni) b_off[ni] = b_base + 32 * (ni ^ tswz);

  bf16x8 af0[4], bf0[4], af1[4], bf1[4];
  auto rd0 = [&](int slot) {
    const unsigned so = slot * K2_STAGE;
    af0[0] = lds_rd128<0>(a_base0 + so);
    af0[1] = lds_rd128<2048>(a_base0 + so);
    af0[2] = lds_rd128<4096>(a_base0 + so);
    af0[3] = lds_rd128<6144>(a_base0 + so);
#pragma unroll
    for (int ni = 0; ni < 4; ++ni) bf0[ni] = tr_frag<0>(b_off[ni] + so);
  };
  auto rd1 = [&](int slot) {
    const unsigned so = slot * K2_STAGE;
    af1[0] = lds_rd128<0>(a_base1 + so);
    af1[1] = lds_rd128<2048>(a_base1 + so);
    af1[2] = lds_rd128<4096>(a_base1 + so);
    af1[3] = lds_rd128<6144>(a_base1 + so);
#pragma unroll
    for (int ni = 0; ni < 4; ++ni) bf1[ni] = tr_frag<4096>(b_off[ni] + so);
  };
  auto mm = [&](const bf16x8* af, const bf16x8* bfr) {
#pragma unroll
    for (int ni = 0; ni < 4; ++ni)
#pragma unroll
      for (int mi = 0; mi < 4; ++mi)
        acc[ni][mi] = __builtin_amdgcn_mfma_f32_16x16x32_bf16(bfr[ni], af[mi], acc[ni][mi], 0, 0, 0);
  };
  BARRIER();
  rd0(0);
  WAIT_L0();
  __builtin_amdgcn_sched_barrier(0);
  int slot = 0;
  for (int j = 0; j < nt; ++j) {
    rd1(slot);
    __builtin_amdgcn_sched_barrier(0);
    mm(af0, bf0);
    __builtin_amdgcn_sched_barrier(0);
    WAIT_L0();
    __builtin_amdgcn_sched_barrier(0);
    slot = (slot == K2_NST - 1) ? 0 : slot + 1;
    if (j + 1 < nt) {
      BARRIER();
      rd0(slot);
    }
    __builtin_amdgcn_sched_barrier(0);
    mm(af1, bf1);
    __builtin_amdgcn_sched_barrier(0);
    WAIT_L0();
    __builtin_amdgcn_sched_barrier(0);
  }
  const __amdgpu_buffer_rsrc_t rs = __builtin_amdgcn_make_buffer_rsrc(slab, 0, 64 * 256 * 1024, 0x00020000);
  f32x4 own[4];
#pragma unroll
  for (int mi = 0; mi < 4; ++mi) {
    if (mi == q) {
#pragma unroll
      for (int ni = 0; ni < 4; ++ni) own[ni] = acc[ni][mi];
    } else {
      const unsigned base = (unsigned)((((tile * 4 + mi) * 4 + q) * 4 + wid) * 4) * 1024u + lane * 16u;
#pragma unroll
      for (int ni = 0; ni < 4; ++ni)
        __builtin_amdgcn_raw_buffer_store_b128(__builtin_bit_cast(u32x4, acc[ni][mi]), rs, base + ni * 1024u, 0, 16);
    }
  }
  const size_t orow = (size_t)(row0 + wm * 64 + q * 16 + fr) * D_ + n0 + wn * 64 + fq * 4;
  u32x2 xv[4];
#pragma unroll
  for (int ni = 0; ni < 4; ++ni) xv[ni] = *(const u32x2*)(x + orow + ni * 16);
  WAIT_V(0);
  BARRIER();
  if (wid == 0) {
    unsigned* c = cnt + tile * CNT_STRIDE;
    if (lane == 0) __hip_atomic_fetch_add(c, 1u, __ATOMIC_RELAXED, __HIP_MEMORY_SCOPE_AGENT);
    for (unsigned spins = 0; spins < (1u << 22); ++spins) {
      const unsigned v = __builtin_amdgcn_readfirstlane(__hip_atomic_load(c, __ATOMIC_RELAXED, __HIP_MEMORY_SCOPE_AGENT));
      if (v >= 4u) break;
      __builtin_amdgcn_s_sleep(1);
    }
  }
  BARRIER();
  u32x4 pv[3][4];
#pragma unroll
  for (int sidx = 0; sidx < 3; ++sidx) {
    const int src = sidx + (sidx >= q ? 1 : 0);
    const unsigned base = (unsigned)((((tile * 4 + q) * 4 + src) * 4 + wid) * 4) * 1024u + lane * 16u;
#pragma unroll
    for (int ni = 0; ni < 4; ++ni) pv[sidx][ni] = __builtin_amdgcn_raw_buffer_load_b128(rs, base + ni * 1024u, 0, 16);
  }
#pragma unroll
  for (int ni = 0; ni < 4; ++ni) {
    f32x4 v = own[ni];
#pragma unroll
    for (int sidx = 0; sidx < 3; ++sidx) v += __builtin_bit_cast(f32x4, pv[sidx][ni]);
    u32x2 o = {pack2(v[0] + bflo(xv[ni][0]), v[1] + bfhi(xv[ni][0])), pack2(v[2] + bflo(xv[ni][1]), v[3] + bfhi(xv[ni][1]))};
    *(u32x2*)(out + orow + ni * 16) = o;
  }
}

extern "C" void kernel_launch(void* const* d_in, const int* in_sizes, int n_in, void* d_out, int out_size,
                              void* d_ws, size_t ws_size, hipStream_t stream) {
  const u16* x = (const u16*)d_in[0];
  const u16* ws_re = (const u16*)d_in[1];
  const u16* ws_im = (const u16*)d_in[2];
  const u16* wd_re = (const u16*)d_in[3];
  const u16* wd_im = (const u16*)d_in[4];
  const u16* w_gate = (const u16*)d_in[6];
  const u16* w_up = (const u16*)d_in[7];
  const u16* w_down = (const u16*)d_in[8];
  u16* ab = (u16*)d_ws;
  u16* h = ab + (size_t)2 * S_ * D_;
  float* slab = (float*)((char*)d_ws + (16u << 20));
  unsigned* cnt = (unsigned*)((char*)d_ws + (32u << 20));
  k1_kernel<<<240, 768, 0, stream>>>(x, wd_re, wd_im, w_gate, w_up, ab, h, cnt);
  k2_kernel<<<256, 512, 0, stream>>>(x, ws_re, ws_im, w_down, ab, h, (u16*)d_out, slab, cnt);
}
```

```cpp
#include <hip/hip_runtime.h>
#include <hip/hip_bf16.h>
#include <stdint.h>
#include <type_traits>

typedef unsigned short u16;
typedef short bf16x8 __attribute__((ext_vector_type(8)));
typedef short bf16x4 __attribute__((ext_vector_type(4)));
typedef float f32x4 __attribute__((ext_vector_type(4)));
typedef float f32x2 __attribute__((ext_vector_type(2)));
typedef unsigned u32x4 __attribute__((ext_vector_type(4)));
typedef unsigned u32x2 __attribute__((ext_vector_type(2)));
typedef __bf16 bf2_t __attribute__((ext_vector_type(2)));

#define K2_PF 12
#define K2_ROT 1
#define K1_ROT 1
constexpr int S_ = 2048, D_ = 512, F_ = 1376, FP_ = 1408;

#define LDS_AS __attribute__((address_space(3)))
#define GLDS16(gptr, ldsptr) \
  __builtin_amdgcn_global_load_lds((const unsigned*)(gptr), (unsigned*)(ldsptr), 16, 0, 0)
#define WAIT_V(n) asm volatile("s_waitcnt vmcnt(" #n ")" ::: "memory")
#define BARRIER() asm volatile("s_barrier" ::: "memory")
#define WAIT_L0() asm volatile("s_waitcnt lgkmcnt(0)" ::: "memory")

__device__ __forceinline__ float bf16r(float x) { return __bfloat162float(__float2bfloat16(x)); }
__device__ __forceinline__ unsigned pack2(float lo, float hi) {
  __hip_bfloat16 a = __float2bfloat16(lo), b = __float2bfloat16(hi);
  return (unsigned)__builtin_bit_cast(u16, a) | ((unsigned)__builtin_bit_cast(u16, b) << 16);
}
__device__ __forceinline__ float bflo(unsigned v) { return __builtin_bit_cast(float, v << 16); }
__device__ __forceinline__ float bfhi(unsigned v) { return __builtin_bit_cast(float, v & 0xffff0000u); }

__device__ __forceinline__ float dot2(unsigned a, unsigned b, float c) {
#if defined(__HIP_DEVICE_COMPILE__)
  return __builtin_amdgcn_fdot2_f32_bf16(__builtin_bit_cast(bf2_t, a), __builtin_bit_cast(bf2_t, b), c, false);
#else
  return c;
#endif
}
__device__ __forceinline__ bf16x4 tr_read(const char* p) {
#if defined(__HIP_DEVICE_COMPILE__)
  return __builtin_amdgcn_ds_read_tr16_b64_v4i16((LDS_AS bf16x4*)(p));
#else
  return bf16x4{};
#endif
}
template <int OFF>
__device__ __forceinline__ bf16x8 tr_frag(unsigned addr) {
  bf16x4 a, b;
  asm volatile("ds_read_b64_tr_b16 %0, %2 offset:%3\n\tds_read_b64_tr_b16 %1, %2 offset:%4"
               : "=&v"(a), "=&v"(b)
               : "v"(addr), "i"(OFF), "i"(OFF + 512));
  return __builtin_shufflevector(a, b, 0, 1, 2, 3, 4, 5, 6, 7);
}
template <int OFF>
__device__ __forceinline__ bf16x8 lds_rd128(unsigned addr) {
  bf16x8 a;
  asm volatile("ds_read_b128 %0, %1 offset:%2" : "=&v"(a) : "v"(addr), "i"(OFF));
  return a;
}
__device__ __forceinline__ unsigned lds_addr(const void* p) {
  return (unsigned)(uintptr_t)(const LDS_AS char*)p;
}
__device__ __forceinline__ bf16x8 cat8(bf16x4 a, bf16x4 b) {
  return __builtin_shufflevector(a, b, 0, 1, 2, 3, 4, 5, 6, 7);
}
__device__ __forceinline__ float gelu_tanh(float g) {
  const float c1 = -2.0f * 0.7978845608028654f * 1.4426950408889634f;
  const float c2 = c1 * 0.044715f;
  float w = g * __builtin_fmaf(g * g, c2, c1);
  float e = __builtin_amdgcn_exp2f(w);
  return g * __builtin_amdgcn_rcpf(1.0f + e);
}

#define SCHED0() __builtin_amdgcn_sched_barrier(0)
template <class RD>
__device__ __forceinline__ void mm16(f32x4 (&acc)[4][4], const bf16x8 (&wf)[4], const bf16x8 (&xf)[4], RD rd) {
#pragma unroll
  for (int ni = 0; ni < 4; ++ni) {
    acc[ni][0] = __builtin_amdgcn_mfma_f32_16x16x32_bf16(wf[ni], xf[0], acc[ni][0], 0, 0, 0);
    SCHED0();
    rd(2 * ni);
    SCHED0();
    acc[ni][1] = __builtin_amdgcn_mfma_f32_16x16x32_bf16(wf[ni], xf[1], acc[ni][1], 0, 0, 0);
    acc[ni][2] = __builtin_amdgcn_mfma_f32_16x16x32_bf16(wf[ni], xf[2], acc[ni][2], 0, 0, 0);
    SCHED0();
    rd(2 * ni + 1);
    SCHED0();
    acc[ni][3] = __builtin_amdgcn_mfma_f32_16x16x32_bf16(wf[ni], xf[3], acc[ni][3], 0, 0, 0);
    SCHED0();
  }
}

constexpr int K1_A = 32768, K1_B = 16384, K1_STAGE = K1_A + K1_B;

__global__ __launch_bounds__(768, 3) void k1_kernel(const u16* __restrict__ x, const u16* __restrict__ wd_re,
                                                    const u16* __restrict__ wd_im, const u16* __restrict__ w_gate,
                                                    const u16* __restrict__ w_up, u16* __restrict__ ab,
                                                    u16* __restrict__ h, unsigned* __restrict__ cnt) {
  __shared__ __attribute__((aligned(1024))) char smem[3 * K1_STAGE];
  const int tid = threadIdx.x, wid = __builtin_amdgcn_readfirstlane(tid >> 6), lane = tid & 63;
  const int fr = lane & 15, fq = lane >> 4;
  const int bid = blockIdx.x;
  const int nb = (bid & 7) * 30 + (bid >> 3);
  const int cb = nb >> 3, rb = nb & 7;
  const bool nk = cb < 8;
  const int row0 = rb * 256;
  const int f0 = (cb - 8) * 64;
  if (bid == 0 && tid < 64) cnt[tid * 64] = 0u;
  const int sw = (fr >> 1) & 7;
  const int ch0 = ((fq) ^ sw) * 16, ch1 = ((4 + fq) ^ sw) * 16;
  const int tq = (lane >> 2) & 3, tp = lane & 3;
  const int tswz = ((tq >> 1) & 1) | ((fq & 1) << 1);
  const int tr_row = (8 * fq + tq) * 128 + 8 * tp;
  float* sst = (float*)(smem + 2 * K1_STAGE);
  float* scs = (float*)(smem + 2 * K1_STAGE + 8192);

  if (wid >= 8) {
    const int lw = wid - 8;
    __builtin_amdgcn_s_setprio(2);
    const int l3 = lane >> 3, l7 = lane & 7;
    const int rt_c0 = (l7 ^ (fq & 7)) * 8, rt_c1 = (l7 ^ ((fq + 4) & 7)) * 8;
    const int kt_c0 = (l7 ^ ((fq & 1) << 1)) * 8, kt_c1 = (l7 ^ (((fq & 1) | 2) << 1)) * 8;
    const u16* xa0 = x + (size_t)(row0 + lw * 64 + l3) * D_ + rt_c0;
    const u16* xa1 = x + (size_t)(row0 + lw * 64 + 8 + l3) * D_ + rt_c1;
    const u16 *wb0, *wb1;
    int wstep_j, wstep_t;
    if (nk) {
      const u16* wnk = (cb < 4 ? wd_re : wd_im) + (size_t)((cb & 3) * 128 + lw * 32 + l3) * D_;
      wb0 = wnk + rt_c0;
      wb1 = wnk + 8 * D_ + rt_c1;
      wstep_j = 16 * D_;
      wstep_t = 64;
    } else {
      const u16* w = (lw < 2) ? w_gate : w_up;
      int c0 = f0 + kt_c0, c1 = f0 + kt_c1;
      if (c0 >= F_) c0 -= 32;
      if (c1 >= F_) c1 -= 32;
      const u16* wk = w + (size_t)((lw & 1) * 32 + l3) * F_;
      wb0 = wk + c0;
      wb1 = wk + 8 * F_ + c1;
      wstep_j = 16 * F_;
      wstep_t = 64 * F_;
    }
    auto stage = [&](int tj, int buf) {
      const int t = (tj + K1_ROT * cb) & 7;
      char* sa = smem + buf * K1_STAGE + lw * 8192;
      char* sb = smem + buf * K1_STAGE + K1_A + lw * 4096;
#pragma unroll
      for (int i = 0; i < 4; ++i) {
        GLDS16(xa0 + i * 16 * D_ + t * 64, sa + i * 2048);
        GLDS16(xa1 + i * 16 * D_ + t * 64, sa + i * 2048 + 1024);
      }
#pragma unroll
      for (int i = 0; i < 2; ++i) {
        GLDS16(wb0 + i * wstep_j + t * wstep_t, sb + i * 2048);
        GLDS16(wb1 + i * wstep_j + t * wstep_t, sb + i * 2048 + 1024);
      }
    };
    stage(0, 0);
    stage(1, 1);
    f32x4 accS[4], accG[4], accC[2];
#pragma unroll
    for (int i = 0; i < 4; ++i) {
      accS[i] = f32x4{0.f, 0.f, 0.f, 0.f};
      accG[i] = f32x4{0.f, 0.f, 0.f, 0.f};
    }
    accC[0] = f32x4{0.f, 0.f, 0.f, 0.f};
    accC[1] = f32x4{0.f, 0.f, 0.f, 0.f};
    const short ONE = 0x3f80;
    const bf16x8 ones = {ONE, ONE, ONE, ONE, ONE, ONE, ONE, ONE};
    const unsigned la0 = lds_addr(smem) + (lw * 64 + fr) * 128 + ch0;
    const unsigned la1 = lds_addr(smem) + (lw * 64 + fr) * 128 + ch1;
    const unsigned lb0 = lds_addr(smem) + K1_A + (lw * 32 + fr) * 128 + ch0;
    const unsigned lb1 = lds_addr(smem) + K1_A + (lw * 32 + fr) * 128 + ch1;
    const unsigned lt0 = lds_addr(smem) + K1_A + (lw >> 1) * 8192 + tr_row + 32 * (((lw & 1) * 2) ^ tswz);
    const unsigned lt1 = lds_addr(smem) + K1_A + (lw >> 1) * 8192 + tr_row + 32 * (((lw & 1) * 2 + 1) ^ tswz);
    int ib = 2, buf = 0;
    for (int t = 0; t < 8; ++t) {
      if (t < 7) WAIT_V(12); else WAIT_V(0);
      BARRIER();
      const unsigned so = buf * K1_STAGE;
      bf16x8 xf[8], wf[4];
      xf[0] = lds_rd128<0>(la0 + so); xf[1] = lds_rd128<2048>(la0 + so);
      xf[2] = lds_rd128<4096>(la0 + so); xf[3] = lds_rd128<6144>(la0 + so);
      xf[4] = lds_rd128<0>(la1 + so); xf[5] = lds_rd128<2048>(la1 + so);
      xf[6] = lds_rd128<4096>(la1 + so); xf[7] = lds_rd128<6144>(la1 + so);
      if (nk) {
        wf[0] = lds_rd128<0>(lb0 + so); wf[1] = lds_rd128<2048>(lb0 + so);
        wf[2] = lds_rd128<0>(lb1 + so); wf[3] = lds_rd128<2048>(lb1 + so);
      } else {
        wf[0] = tr_frag<0>(lt0 + so); wf[1] = tr_frag<0>(lt1 + so);
        wf[2] = tr_frag<4096>(lt0 + so); wf[3] = tr_frag<4096>(lt1 + so);
      }
      if (t + 2 < 8) stage(t + 2, ib);
      ib = (ib == 2) ? 0 : ib + 1;
      WAIT_L0();
      __builtin_amdgcn_sched_barrier(0);
#pragma unroll
      for (int i = 0; i < 8; ++i) {
        accS[i & 3] = __builtin_amdgcn_mfma_f32_16x16x32_bf16(ones, xf[i], accS[i & 3], 0, 0, 0);
        accG[i & 3] = __builtin_amdgcn_mfma_f32_16x16x32_bf16(xf[i], xf[i], accG[i & 3], 0, 0, 0);
      }
#pragma unroll
      for (int i = 0; i < 4; ++i)
        accC[i & 1] = __builtin_amdgcn_mfma_f32_16x16x32_bf16(wf[i], ones, accC[i & 1], 0, 0, 0);
      buf = (buf == 2) ? 0 : buf + 1;
    }
    if (fq == (fr >> 2)) {
#pragma unroll
      for (int i = 0; i < 4; ++i) {
        const int r = fr & 3;
        const float q = r == 0 ? accG[i][0] : r == 1 ? accG[i][1] : r == 2 ? accG[i][2] : accG[i][3];
        const float mean = accS[i][0] * (1.0f / D_);
        const float var = fmaxf(q * (1.0f / D_) - mean * mean, 0.f);
        const float sd = bf16r(sqrtf(bf16r(bf16r(var) + 1e-5f)));
        f32x2 o = {bf16r(mean), __builtin_amdgcn_rcpf(sd)};
        *(f32x2*)(sst + (lw * 64 + i * 16 + fr) * 2) = o;
      }
    }
    if (fr == 0) {
      *(f32x4*)(scs + lw * 32 + fq * 4) = accC[0];
      *(f32x4*)(scs + lw * 32 + 16 + fq * 4) = accC[1];
    }
    WAIT_L0();
    BARRIER();
    return;
  }

  const int wm = wid >> 1, wn = wid & 1;
  f32x4 acc[4][4];
#pragma unroll
  for (int i = 0; i < 4; ++i)
#pragma unroll
    for (int j = 0; j < 4; ++j) acc[i][j] = f32x4{0.f, 0.f, 0.f, 0.f};
  const unsigned a_b0 = lds_addr(smem) + (wm * 64 + fr) * 128 + ch0, a_b1 = lds_addr(smem) + (wm * 64 + fr) * 128 + ch1;
  const unsigned n_b0 = lds_addr(smem) + K1_A + (wn * 64 + fr) * 128 + ch0;
  const unsigned n_b1 = lds_addr(smem) + K1_A + (wn * 64 + fr) * 128 + ch1;
  const unsigned t_p0 = lds_addr(smem) + K1_A + tr_row + 32 * ((wn * 2) ^ tswz);
  const unsigned t_p1 = lds_addr(smem) + K1_A + tr_row + 32 * ((wn * 2 + 1) ^ tswz);

  auto mainloop = [&](auto NKC) {
    constexpr bool NK = decltype(NKC)::value;
    bf16x8 xf0[4], wf0[4], xf1[4], wf1[4];
    auto rdg = [&](auto KSC, bf16x8(&xf)[4], bf16x8(&wf)[4], unsigned so, int g) {
      constexpr int KS = decltype(KSC)::value;
      const unsigned ab_ = (KS ? a_b1 : a_b0) + so;
      switch (g) {
        case 0: xf[0] = lds_rd128<0>(ab_); break;
        case 2: xf[1] = lds_rd128<2048>(ab_); break;
        case 4: xf[2] = lds_rd128<4096>(ab_); break;
        case 6: xf[3] = lds_rd128<6144>(ab_); break;
        default:
          if constexpr (NK) {
            const unsigned nb_ = (KS ? n_b1 : n_b0) + so;
            if (g == 1) wf[0] = lds_rd128<0>(nb_);
            if (g == 3) wf[1] = lds_rd128<2048>(nb_);
            if (g == 5) wf[2] = lds_rd128<4096>(nb_);
            if (g == 7) wf[3] = lds_rd128<6144>(nb_);
          } else {
            if (g == 1) wf[0] = tr_frag<KS * 4096>(t_p0 + so);
            if (g == 3) wf[1] = tr_frag<KS * 4096>(t_p1 + so);
            if (g == 5) wf[2] = tr_frag<KS * 4096 + 8192>(t_p0 + so);
            if (g == 7) wf[3] = tr_frag<KS * 4096 + 8192>(t_p1 + so);
          }
      }
    };
    using C0 = std::integral_constant<int, 0>;
    using C1 = std::integral_constant<int, 1>;
    BARRIER();
#pragma unroll
    for (int g = 0; g < 8; ++g) rdg(C0{}, xf0, wf0, 0u, g);
    WAIT_L0();
    SCHED0();
    unsigned so = 0;
    for (int t = 0; t < 8; ++t) {
      mm16(acc, wf0, xf0, [&](int g) { rdg(C1{}, xf1, wf1, so, g); });
      WAIT_L0();
      SCHED0();
      so = (so == 2 * K1_STAGE) ? 0u : so + K1_STAGE;
      if (t < 7) {
        BARRIER();
        mm16(acc, wf1, xf1, [&](int g) { rdg(C0{}, xf0, wf0, so, g); });
      } else {
        mm16(acc, wf1, xf1, [&](int) {});
      }
      WAIT_L0();
      SCHED0();
    }
  };
  if (nk) mainloop(std::true_type{});
  else mainloop(std::false_type{});
  BARRIER();
  float mb[4], rinv[4];
#pragma unroll
  for (int mi = 0; mi < 4; ++mi) {
    f32x2 v = *(const f32x2*)(sst + (wm * 64 + mi * 16 + fr) * 2);
    mb[mi] = v[0];
    rinv[mi] = v[1];
  }
  f32x4 csv[4];
#pragma unroll
  for (int ni = 0; ni < 4; ++ni) {
    const int ci = nk ? (wn * 64 + ni * 16) : ((ni >> 1) * 64 + wn * 32 + (ni & 1) * 16);
    csv[ni] = *(const f32x4*)(scs + ci + fq * 4);
  }
  if (nk) {
    const float sgn = cb < 4 ? 1.0f : -1.0f;
    u16* dst = ab + (size_t)(cb >> 2) * (S_ * D_) + (size_t)(row0 + wm * 64 + fr) * D_ + (cb & 3) * 128 + wn * 64 + fq * 4;
#pragma unroll
    for (int mi = 0; mi < 4; ++mi) {
      const float r = rinv[mi] * sgn;
#pragma unroll
      for (int ni = 0; ni < 4; ++ni) {
        f32x4 v = (acc[ni][mi] - mb[mi] * csv[ni]) * r;
        u32x2 o = {pack2(v[0], v[1]), pack2(v[2], v[3])};
        *(u32x2*)(dst + mi * 16 * D_ + ni * 16) = o;
      }
    }
  } else {
    const int fc = f0 + wn * 32 + fq * 4;
    u16* dst = h + (size_t)(row0 + wm * 64 + fr) * FP_ + fc;
    const unsigned vmask = (f0 + wn * 32) < F_ ? 0xffffffffu : 0u;
#pragma unroll
    for (int mi = 0; mi < 4; ++mi) {
#pragma unroll
      for (int ni = 0; ni < 2; ++ni) {
        f32x4 g = (acc[ni][mi] - mb[mi] * csv[ni]) * rinv[mi];
        f32x4 u = (acc[ni + 2][mi] - mb[mi] * csv[ni + 2]) * rinv[mi];
        float hv[4];
#pragma unroll
        for (int e = 0; e < 4; ++e) {
          float gl = bf16r(gelu_tanh(bf16r(g[e])));
          hv[e] = gl * bf16r(u[e]);
        }
        u32x2 o = {pack2(hv[0], hv[1]) & vmask, pack2(hv[2], hv[3]) & vmask};
        *(u32x2*)(dst + mi * 16 * FP_ + ni * 16) = o;
      }
    }
  }
}

constexpr int K2_STAGE = 32768, K2_NST = 5;
constexpr int CNT_STRIDE = 64;

__global__ __launch_bounds__(512, 2) void k2_kernel(const u16* __restrict__ x, const u16* __restrict__ ws_re,
                                                    const u16* __restrict__ ws_im, const u16* __restrict__ w_down,
                                                    const u16* __restrict__ ab, const u16* __restrict__ h,
                                                    u16* __restrict__ out, float* slab, unsigned* cnt) {
  __shared__ __attribute__((aligned(1024))) char smem[K2_NST * K2_STAGE];
  const int tid = threadIdx.x, wid = __builtin_amdgcn_readfirstlane(tid >> 6), lane = tid & 63;
  const int fr = lane & 15, fq = lane >> 4;
  const int bid = blockIdx.x;
  const int idx = bid >> 3;
  const int q = (bid & 7) >> 1;
  const int rb = (bid & 1) * 8 + (idx >> 2), cbk = idx & 3;
  const int row0 = rb * 128, n0 = cbk * 128, tile = rb * 4 + cbk;
  const int t_begin = q < 2 ? 22 * q : 44 + 21 * (q - 2);
  const int nt = q < 2 ? 22 : 21;

  if (wid >= 4) {
    const int lw = wid - 4;
    const int l3 = lane >> 3, l7 = lane & 7;
    const int rt_c0 = (l7 ^ (fq & 7)) * 8, rt_c1 = (l7 ^ ((fq + 4) & 7)) * 8;
    const int kt_c0 = (l7 ^ ((fq & 1) << 1)) * 8, kt_c1 = (l7 ^ (((fq & 1) | 2) << 1)) * 8;
    const int sub = lw & 1;
    auto stage = [&](int j, int slot) {
      const int t = t_begin + j;
      char* dst = smem + slot * K2_STAGE + lw * 8192;
      if (lw < 2) {
        const u16* A;
        int lda, k0;
        if (t < 64) { A = (t < 32 ? ws_re : ws_im); lda = S_; k0 = (t & 31) * 64; }
        else { A = h; lda = FP_; k0 = (t - 64) * 64; }
        A += (size_t)(row0 + sub * 64) * lda;
#pragma unroll
        for (int i = 0; i < 8; ++i) {
          const u16* src = A + (i * 8 + l3) * lda + k0 + ((i & 1) ? rt_c1 : rt_c0);
          GLDS16(src, dst + i * 1024);
        }
      } else {
        const u16* B;
        int k0, kmax;
        if (t < 64) { B = ab + (size_t)(t >> 5) * (S_ * D_); k0 = (t & 31) * 64; kmax = S_ - 1; }
        else { B = w_down; k0 = (t - 64) * 64; kmax = F_ - 1; }
#pragma unroll
        for (int i = 0; i < 8; ++i) {
          const int krow = min(k0 + i * 8 + l3, kmax);
          const u16* src = B + krow * D_ + n0 + sub * 64 + ((i & 1) ? kt_c1 : kt_c0);
          GLDS16(src, dst + i * 1024);
        }
      }
    };
#pragma unroll
    for (int s = 0; s < K2_NST - 1; ++s) stage(s, s);
    int islot = K2_NST - 1;
    for (int j = 0; j < nt; ++j) {
      const int rem = nt - 1 - j;
      if (rem >= 3) WAIT_V(24);
      else if (rem == 2) WAIT_V(16);
      else if (rem == 1) WAIT_V(8);
      else WAIT_V(0);
      BARRIER();
      if (j + K2_NST - 1 < nt) stage(j + K2_NST - 1, islot);
      islot = (islot == K2_NST - 1) ? 0 : islot + 1;
    }
    return;
  }
  const int wm = wid >> 1, wn = wid & 1;
  f32x4 acc[4][4];
#pragma unroll
  for (int i = 0; i < 4; ++i)
#pragma unroll
    for (int j = 0; j < 4; ++j) acc[i][j] = f32x4{0.f, 0.f, 0.f, 0.f};

  const int sw = (fr >> 1) & 7;
  const unsigned a_base0 = lds_addr(smem) + (wm * 64 + fr) * 128 + ((fq) ^ sw) * 16;
  const unsigned a_base1 = lds_addr(smem) + (wm * 64 + fr) * 128 + ((4 + fq) ^ sw) * 16;
  const int tq = (lane >> 2) & 3, tp = lane & 3;
  const int tswz = ((tq >> 1) & 1) | ((fq & 1) << 1);
  const unsigned b_base = lds_addr(smem) + 16384 + wn * 8192 + (8 * fq + tq) * 128 + 8 * tp;
  unsigned b_off[4];
#pragma unroll
  for (int ni = 0; ni < 4; ++ni) b_off[ni] = b_base + 32 * (ni ^ tswz);

  bf16x8 af0[4], bf0[4], af1[4], bf1[4];
  auto rdg = [&](auto KSC, bf16x8(&af)[4], bf16x8(&bfr)[4], unsigned so, int g) {
    constexpr int KS = decltype(KSC)::value;
    const unsigned ab_ = (KS ? a_base1 : a_base0) + so;
    switch (g) {
      case 0: af[0] = lds_rd128<0>(ab_); break;
      case 2: af[1] = lds_rd128<2048>(ab_); break;
      case 4: af[2] = lds_rd128<4096>(ab_); break;
      case 6: af[3] = lds_rd128<6144>(ab_); break;
      case 1: bfr[0] = tr_frag<KS * 4096>(b_off[0] + so); break;
      case 3: bfr[1] = tr_frag<KS * 4096>(b_off[1] + so); break;
      case 5: bfr[2] = tr_frag<KS * 4096>(b_off[2] + so); break;
      default: bfr[3] = tr_frag<KS * 4096>(b_off[3] + so); break;
    }
  };
  using C0 = std::integral_constant<int, 0>;
  using C1 = std::integral_constant<int, 1>;
  BARRIER();
#pragma unroll
  for (int g = 0; g < 8; ++g) rdg(C0{}, af0, bf0, 0u, g);
  WAIT_L0();
  SCHED0();
  unsigned so = 0;
  for (int j = 0; j < nt; ++j) {
    mm16(acc, bf0, af0, [&](int g) { rdg(C1{}, af1, bf1, so, g); });
    WAIT_L0();
    SCHED0();
    so = (so == (K2_NST - 1) * K2_STAGE) ? 0u : so + K2_STAGE;
    if (j + 1 < nt) {
      BARRIER();
      mm16(acc, bf1, af1, [&](int g) { rdg(C0{}, af0, bf0, so, g); });
    } else {
      mm16(acc, bf1, af1, [&](int) {});
    }
    WAIT_L0();
    SCHED0();
  }
  const __amdgpu_buffer_rsrc_t rs = __builtin_amdgcn_make_buffer_rsrc(slab, 0, 64 * 256 * 1024, 0x00020000);
  f32x4 own[4];
#pragma unroll
  for (int mi = 0; mi < 4; ++mi) {
    if (mi == q) {
#pragma unroll
      for (int ni = 0; ni < 4; ++ni) own[ni] = acc[ni][mi];
    } else {
      const unsigned base = (unsigned)((((tile * 4 + mi) * 4 + q) * 4 + wid) * 4) * 1024u + lane * 16u;
#pragma unroll
      for (int ni = 0; ni < 4; ++ni)
        __builtin_amdgcn_raw_buffer_store_b128(__builtin_bit_cast(u32x4, acc[ni][mi]), rs, base + ni * 1024u, 0, 16);
    }
  }
  const size_t orow = (size_t)(row0 + wm * 64 + q * 16 + fr) * D_ + n0 + wn * 64 + fq * 4;
  u32x2 xv[4];
#pragma unroll
  for (int ni = 0; ni < 4; ++ni) xv[ni] = *(const u32x2*)(x + orow + ni * 16);
  WAIT_V(0);
  BARRIER();
  if (wid == 0) {
    unsigned* c = cnt + tile * CNT_STRIDE;
    if (lane == 0) __hip_atomic_fetch_add(c, 1u, __ATOMIC_RELAXED, __HIP_MEMORY_SCOPE_AGENT);
    for (unsigned spins = 0; spins < (1u << 22); ++spins) {
      const unsigned v = __builtin_amdgcn_readfirstlane(__hip_atomic_load(c, __ATOMIC_RELAXED, __HIP_MEMORY_SCOPE_AGENT));
      if (v >= 4u) break;
      __builtin_amdgcn_s_sleep(1);
    }
  }
  BARRIER();
  u32x4 pv[3][4];
#pragma unroll
  for (int sidx = 0; sidx < 3; ++sidx) {
    const int src = sidx + (sidx >= q ? 1 : 0);
    const unsigned base = (unsigned)((((tile * 4 + q) * 4 + src) * 4 + wid) * 4) * 1024u + lane * 16u;
#pragma unroll
    for (int ni = 0; ni < 4; ++ni) pv[sidx][ni] = __builtin_amdgcn_raw_buffer_load_b128(rs, base + ni * 1024u, 0, 16);
  }
#pragma unroll
  for (int ni = 0; ni < 4; ++ni) {
    f32x4 v = own[ni];
#pragma unroll
    for (int sidx = 0; sidx < 3; ++sidx) v += __builtin_bit_cast(f32x4, pv[sidx][ni]);
    u32x2 o = {pack2(v[0] + bflo(xv[ni][0]), v[1] + bfhi(xv[ni][0])), pack2(v[2] + bflo(xv[ni][1]), v[3] + bfhi(xv[ni][1]))};
    *(u32x2*)(out + orow + ni * 16) = o;
  }
}

extern "C" void kernel_launch(void* const* d_in, const int* in_sizes, int n_in, void* d_out, int out_size,
                              void* d_ws, size_t ws_size, hipStream_t stream) {
  const u16* x = (const u16*)d_in[0];
  const u16* ws_re = (const u16*)d_in[1];
  const u16* ws_im = (const u16*)d_in[2];
  const u16* wd_re = (const u16*)d_in[3];
  const u16* wd_im = (const u16*)d_in[4];
  const u16* w_gate = (const u16*)d_in[6];
  const u16* w_up = (const u16*)d_in[7];
  const u16* w_down = (const u16*)d_in[8];
  u16* ab = (u16*)d_ws;
  u16* h = ab + (size_t)2 * S_ * D_;
  float* slab = (float*)((char*)d_ws + (16u << 20));
  unsigned* cnt = (unsigned*)((char*)d_ws + (32u << 20));
  k1_kernel<<<240, 768, 0, stream>>>(x, wd_re, wd_im, w_gate, w_up, ab, h, cnt);
  k2_kernel<<<256, 512, 0, stream>>>(x, ws_re, ws_im, w_down, ab, h, (u16*)d_out, slab, cnt);
}
```

```cpp
#include <hip/hip_runtime.h>
#include <hip/hip_bf16.h>
#include <stdint.h>
#include <type_traits>

typedef unsigned short u16;
typedef short bf16x8 __attribute__((ext_vector_type(8)));
typedef short bf16x4 __attribute__((ext_vector_type(4)));
typedef float f32x4 __attribute__((ext_vector_type(4)));
typedef float f32x2 __attribute__((ext_vector_type(2)));
typedef unsigned u32x4 __attribute__((ext_vector_type(4)));
typedef unsigned u32x2 __attribute__((ext_vector_type(2)));
typedef __bf16 bf2_t __attribute__((ext_vector_type(2)));

#define K2_PF 12
#define K2_ROT 1
#define K1_WARM_T 2
#define K1_ROT 1
constexpr int S_ = 2048, D_ = 512, F_ = 1376, FP_ = 1408;

#define LDS_AS __attribute__((address_space(3)))
#define GLDS16(gptr, ldsptr) \
  __builtin_amdgcn_global_load_lds((const unsigned*)(gptr), (unsigned*)(ldsptr), 16, 0, 0)
#define WAIT_V(n) asm volatile("s_waitcnt vmcnt(" #n ")" ::: "memory")
#define BARRIER() asm volatile("s_barrier" ::: "memory")
#define WAIT_L0() asm volatile("s_waitcnt lgkmcnt(0)" ::: "memory")

__device__ __forceinline__ float bf16r(float x) { return __bfloat162float(__float2bfloat16(x)); }
__device__ __forceinline__ unsigned pack2(float lo, float hi) {
  __hip_bfloat16 a = __float2bfloat16(lo), b = __float2bfloat16(hi);
  return (unsigned)__builtin_bit_cast(u16, a) | ((unsigned)__builtin_bit_cast(u16, b) << 16);
}
__device__ __forceinline__ float bflo(unsigned v) { return __builtin_bit_cast(float, v << 16); }
__device__ __forceinline__ float bfhi(unsigned v) { return __builtin_bit_cast(float, v & 0xffff0000u); }

__device__ __forceinline__ float dot2(unsigned a, unsigned b, float c) {
#if defined(__HIP_DEVICE_COMPILE__)
  return __builtin_amdgcn_fdot2_f32_bf16(__builtin_bit_cast(bf2_t, a), __builtin_bit_cast(bf2_t, b), c, false);
#else
  return c;
#endif
}
__device__ __forceinline__ bf16x4 tr_read(const char* p) {
#if defined(__HIP_DEVICE_COMPILE__)
  return __builtin_amdgcn_ds_read_tr16_b64_v4i16((LDS_AS bf16x4*)(p));
#else
  return bf16x4{};
#endif
}
template <int OFF>
__device__ __forceinline__ bf16x8 tr_frag(unsigned addr) {
  bf16x4 a, b;
  asm volatile("ds_read_b64_tr_b16 %0, %2 offset:%3\n\tds_read_b64_tr_b16 %1, %2 offset:%4"
               : "=&v"(a), "=&v"(b)
               : "v"(addr), "i"(OFF), "i"(OFF + 512));
  return __builtin_shufflevector(a, b, 0, 1, 2, 3, 4, 5, 6, 7);
}
template <int OFF>
__device__ __forceinline__ bf16x8 lds_rd128(unsigned addr) {
  bf16x8 a;
  asm volatile("ds_read_b128 %0, %1 offset:%2" : "=&v"(a) : "v"(addr), "i"(OFF));
  return a;
}
__device__ __forceinline__ unsigned lds_addr(const void* p) {
  return (unsigned)(uintptr_t)(const LDS_AS char*)p;
}
__device__ __forceinline__ bf16x8 cat8(bf16x4 a, bf16x4 b) {
  return __builtin_shufflevector(a, b, 0, 1, 2, 3, 4, 5, 6, 7);
}
__device__ __forceinline__ float gelu_tanh(float g) {
  const float c1 = -2.0f * 0.7978845608028654f * 1.4426950408889634f;
  const float c2 = c1 * 0.044715f;
  float w = g * __builtin_fmaf(g * g, c2, c1);
  float e = __builtin_amdgcn_exp2f(w);
  return g * __builtin_amdgcn_rcpf(1.0f + e);
}

__device__ __forceinline__ void touch(const void* p, unsigned& sink) {
  asm volatile("global_load_dword %0, %1, off" : "+v"(sink) : "v"(p) : "memory");
}
#define SCHED0() __builtin_amdgcn_sched_barrier(0)
template <class RD>
__device__ __forceinline__ void mm16(f32x4 (&acc)[4][4], const bf16x8 (&wf)[4], const bf16x8 (&xf)[4], RD rd) {
#pragma unroll
  for (int i = 0; i < 16; ++i) {
    const int ni = i >> 2, mi = i & 3;
    acc[ni][mi] = __builtin_amdgcn_mfma_f32_16x16x32_bf16(wf[ni], xf[mi], acc[ni][mi], 0, 0, 0);
    if (i < 8) {
      SCHED0();
      rd(i);
      SCHED0();
    }
  }
  SCHED0();
}

constexpr int K1_A = 32768, K1_B = 16384, K1_STAGE = K1_A + K1_B;

__global__ __launch_bounds__(768, 3) void k1_kernel(const u16* __restrict__ x, const u16* __restrict__ wd_re,
                                                    const u16* __restrict__ wd_im, const u16* __restrict__ w_gate,
                                                    const u16* __restrict__ w_up, u16* __restrict__ ab,
                                                    u16* __restrict__ h, unsigned* __restrict__ cnt,
                                                    const u16* __restrict__ ws_re, const u16* __restrict__ ws_im,
                                                    const u16* __restrict__ w_down) {
  __shared__ __attribute__((aligned(1024))) char smem[3 * K1_STAGE];
  const int tid = threadIdx.x, wid = __builtin_amdgcn_readfirstlane(tid >> 6), lane = tid & 63;
  const int fr = lane & 15, fq = lane >> 4;
  const int bid = blockIdx.x;
  const int nb = (bid & 7) * 30 + (bid >> 3);
  const int cb = nb >> 3, rb = nb & 7;
  const bool nk = cb < 8;
  const int row0 = rb * 256;
  const int f0 = (cb - 8) * 64;
  if (bid == 0 && tid < 64) cnt[tid * 64] = 0u;
  const int sw = (fr >> 1) & 7;
  const int ch0 = ((fq) ^ sw) * 16, ch1 = ((4 + fq) ^ sw) * 16;
  const int tq = (lane >> 2) & 3, tp = lane & 3;
  const int tswz = ((tq >> 1) & 1) | ((fq & 1) << 1);
  const int tr_row = (8 * fq + tq) * 128 + 8 * tp;
  float* sst = (float*)(smem + 2 * K1_STAGE);
  float* scs = (float*)(smem + 2 * K1_STAGE + 8192);

  if (wid >= 8) {
    const int lw = wid - 8;
    __builtin_amdgcn_s_setprio(2);
    const int l3 = lane >> 3, l7 = lane & 7;
    const int rt_c0 = (l7 ^ (fq & 7)) * 8, rt_c1 = (l7 ^ ((fq + 4) & 7)) * 8;
    const int kt_c0 = (l7 ^ ((fq & 1) << 1)) * 8, kt_c1 = (l7 ^ (((fq & 1) | 2) << 1)) * 8;
    const u16* xa0 = x + (size_t)(row0 + lw * 64 + l3) * D_ + rt_c0;
    const u16* xa1 = x + (size_t)(row0 + lw * 64 + 8 + l3) * D_ + rt_c1;
    const u16 *wb0, *wb1;
    int wstep_j, wstep_t;
    if (nk) {
      const u16* wnk = (cb < 4 ? wd_re : wd_im) + (size_t)((cb & 3) * 128 + lw * 32 + l3) * D_;
      wb0 = wnk + rt_c0;
      wb1 = wnk + 8 * D_ + rt_c1;
      wstep_j = 16 * D_;
      wstep_t = 64;
    } else {
      const u16* w = (lw < 2) ? w_gate : w_up;
      int c0 = f0 + kt_c0, c1 = f0 + kt_c1;
      if (c0 >= F_) c0 -= 32;
      if (c1 >= F_) c1 -= 32;
      const u16* wk = w + (size_t)((lw & 1) * 32 + l3) * F_;
      wb0 = wk + c0;
      wb1 = wk + 8 * F_ + c1;
      wstep_j = 16 * F_;
      wstep_t = 64 * F_;
    }
    auto stage = [&](int tj, int buf) {
      const int t = (tj + K1_ROT * cb) & 7;
      char* sa = smem + buf * K1_STAGE + lw * 8192;
      char* sb = smem + buf * K1_STAGE + K1_A + lw * 4096;
#pragma unroll
      for (int i = 0; i < 4; ++i) {
        GLDS16(xa0 + i * 16 * D_ + t * 64, sa + i * 2048);
        GLDS16(xa1 + i * 16 * D_ + t * 64, sa + i * 2048 + 1024);
      }
#pragma unroll
      for (int i = 0; i < 2; ++i) {
        GLDS16(wb0 + i * wstep_j + t * wstep_t, sb + i * 2048);
        GLDS16(wb1 + i * wstep_j + t * wstep_t, sb + i * 2048 + 1024);
      }
    };
    stage(0, 0);
    stage(1, 1);
    f32x4 accS[4], accG[4], accC[2];
#pragma unroll
    for (int i = 0; i < 4; ++i) {
      accS[i] = f32x4{0.f, 0.f, 0.f, 0.f};
      accG[i] = f32x4{0.f, 0.f, 0.f, 0.f};
    }
    accC[0] = f32x4{0.f, 0.f, 0.f, 0.f};
    accC[1] = f32x4{0.f, 0.f, 0.f, 0.f};
    const short ONE = 0x3f80;
    const bf16x8 ones = {ONE, ONE, ONE, ONE, ONE, ONE, ONE, ONE};
    const unsigned la0 = lds_addr(smem) + (lw * 64 + fr) * 128 + ch0;
    const unsigned la1 = lds_addr(smem) + (lw * 64 + fr) * 128 + ch1;
    const unsigned lb0 = lds_addr(smem) + K1_A + (lw * 32 + fr) * 128 + ch0;
    const unsigned lb1 = lds_addr(smem) + K1_A + (lw * 32 + fr) * 128 + ch1;
    const unsigned lt0 = lds_addr(smem) + K1_A + (lw >> 1) * 8192 + tr_row + 32 * (((lw & 1) * 2) ^ tswz);
    const unsigned lt1 = lds_addr(smem) + K1_A + (lw >> 1) * 8192 + tr_row + 32 * (((lw & 1) * 2 + 1) ^ tswz);
    int ib = 2, buf = 0;
    for (int t = 0; t < 8; ++t) {
      if (t < 7) WAIT_V(12); else WAIT_V(0);
      BARRIER();
      const unsigned so = buf * K1_STAGE;
      bf16x8 xf[8], wf[4];
      xf[0] = lds_rd128<0>(la0 + so); xf[1] = lds_rd128<2048>(la0 + so);
      xf[2] = lds_rd128<4096>(la0 + so); xf[3] = lds_rd128<6144>(la0 + so);
      xf[4] = lds_rd128<0>(la1 + so); xf[5] = lds_rd128<2048>(la1 + so);
      xf[6] = lds_rd128<4096>(la1 + so); xf[7] = lds_rd128<6144>(la1 + so);
      if (nk) {
        wf[0] = lds_rd128<0>(lb0 + so); wf[1] = lds_rd128<2048>(lb0 + so);
        wf[2] = lds_rd128<0>(lb1 + so); wf[3] = lds_rd128<2048>(lb1 + so);
      } else {
        wf[0] = tr_frag<0>(lt0 + so); wf[1] = tr_frag<0>(lt1 + so);
        wf[2] = tr_frag<4096>(lt0 + so); wf[3] = tr_frag<4096>(lt1 + so);
      }
      if (t + 2 < 8) stage(t + 2, ib);
      ib = (ib == 2) ? 0 : ib + 1;
      WAIT_L0();
      __builtin_amdgcn_sched_barrier(0);
#pragma unroll
      for (int i = 0; i < 8; ++i) {
        accS[i & 3] = __builtin_amdgcn_mfma_f32_16x16x32_bf16(ones, xf[i], accS[i & 3], 0, 0, 0);
        accG[i & 3] = __builtin_amdgcn_mfma_f32_16x16x32_bf16(xf[i], xf[i], accG[i & 3], 0, 0, 0);
      }
#pragma unroll
      for (int i = 0; i < 4; ++i)
        accC[i & 1] = __builtin_amdgcn_mfma_f32_16x16x32_bf16(wf[i], ones, accC[i & 1], 0, 0, 0);
      buf = (buf == 2) ? 0 : buf + 1;
    }
    if (fq == (fr >> 2)) {
#pragma unroll
      for (int i = 0; i < 4; ++i) {
        const int r = fr & 3;
        const float q = r == 0 ? accG[i][0] : r == 1 ? accG[i][1] : r == 2 ? accG[i][2] : accG[i][3];
        const float mean = accS[i][0] * (1.0f / D_);
        const float var = fmaxf(q * (1.0f / D_) - mean * mean, 0.f);
        const float sd = bf16r(sqrtf(bf16r(bf16r(var) + 1e-5f)));
        f32x2 o = {bf16r(mean), __builtin_amdgcn_rcpf(sd)};
        *(f32x2*)(sst + (lw * 64 + i * 16 + fr) * 2) = o;
      }
    }
    if (fr == 0) {
      *(f32x4*)(scs + lw * 32 + fq * 4) = accC[0];
      *(f32x4*)(scs + lw * 32 + 16 + fq * 4) = accC[1];
    }
    WAIT_L0();
    BARRIER();
    return;
  }

  const int wm = wid >> 1, wn = wid & 1;
  f32x4 acc[4][4];
#pragma unroll
  for (int i = 0; i < 4; ++i)
#pragma unroll
    for (int j = 0; j < 4; ++j) acc[i][j] = f32x4{0.f, 0.f, 0.f, 0.f};
  const unsigned a_b0 = lds_addr(smem) + (wm * 64 + fr) * 128 + ch0, a_b1 = lds_addr(smem) + (wm * 64 + fr) * 128 + ch1;
  const unsigned n_b0 = lds_addr(smem) + K1_A + (wn * 64 + fr) * 128 + ch0;
  const unsigned n_b1 = lds_addr(smem) + K1_A + (wn * 64 + fr) * 128 + ch1;
  const unsigned t_p0 = lds_addr(smem) + K1_A + tr_row + 32 * ((wn * 2) ^ tswz);
  const unsigned t_p1 = lds_addr(smem) + K1_A + tr_row + 32 * ((wn * 2 + 1) ^ tswz);

  unsigned sink = 0;
  auto warm = [&]() {
    const int g = (bid * 8 + wid) * 64 + lane;
    touch((g < 65536 ? ws_re + (size_t)g * 64 : ws_im + (size_t)(g - 65536) * 64), sink);
    const int g2 = g + 122880;
    if (g2 < 142080) touch((g2 < 131072 ? ws_im + (size_t)(g2 - 65536) * 64 : w_down + (size_t)(g2 - 131072) * 64), sink);
  };
  auto mainloop = [&](auto NKC) {
    constexpr bool NK = decltype(NKC)::value;
    bf16x8 xf0[4], wf0[4], xf1[4], wf1[4];
    auto rdg = [&](auto KSC, bf16x8(&xf)[4], bf16x8(&wf)[4], unsigned so, int g) {
      constexpr int KS = decltype(KSC)::value;
      const unsigned ab_ = (KS ? a_b1 : a_b0) + so;
      switch (g) {
        case 0: xf[0] = lds_rd128<0>(ab_); break;
        case 2: xf[1] = lds_rd128<2048>(ab_); break;
        case 4: xf[2] = lds_rd128<4096>(ab_); break;
        case 6: xf[3] = lds_rd128<6144>(ab_); break;
        default:
          if constexpr (NK) {
            const unsigned nb_ = (KS ? n_b1 : n_b0) + so;
            if (g == 1) wf[0] = lds_rd128<0>(nb_);
            if (g == 3) wf[1] = lds_rd128<2048>(nb_);
            if (g == 5) wf[2] = lds_rd128<4096>(nb_);
            if (g == 7) wf[3] = lds_rd128<6144>(nb_);
          } else {
            if (g == 1) wf[0] = tr_frag<KS * 4096>(t_p0 + so);
            if (g == 3) wf[1] = tr_frag<KS * 4096>(t_p1 + so);
            if (g == 5) wf[2] = tr_frag<KS * 4096 + 8192>(t_p0 + so);
            if (g == 7) wf[3] = tr_frag<KS * 4096 + 8192>(t_p1 + so);
          }
      }
    };
    using C0 = std::integral_constant<int, 0>;
    using C1 = std::integral_constant<int, 1>;
    BARRIER();
#pragma unroll
    for (int g = 0; g < 8; ++g) rdg(C0{}, xf0, wf0, 0u, g);
    WAIT_L0();
    SCHED0();
    unsigned so = 0;
    for (int t = 0; t < 8; ++t) {
      if (t == K1_WARM_T) warm();
      mm16(acc, wf0, xf0, [&](int g) { rdg(C1{}, xf1, wf1, so, g); });
      WAIT_L0();
      SCHED0();
      so = (so == 2 * K1_STAGE) ? 0u : so + K1_STAGE;
      if (t < 7) {
        BARRIER();
        mm16(acc, wf1, xf1, [&](int g) { rdg(C0{}, xf0, wf0, so, g); });
      } else {
        mm16(acc, wf1, xf1, [&](int) {});
      }
      WAIT_L0();
      SCHED0();
    }
  };
  if (nk) mainloop(std::true_type{});
  else mainloop(std::false_type{});
  BARRIER();
  float mb[4], rinv[4];
#pragma unroll
  for (int mi = 0; mi < 4; ++mi) {
    f32x2 v = *(const f32x2*)(sst + (wm * 64 + mi * 16 + fr) * 2);
    mb[mi] = v[0];
    rinv[mi] = v[1];
  }
  f32x4 csv[4];
#pragma unroll
  for (int ni = 0; ni < 4; ++ni) {
    const int ci = nk ? (wn * 64 + ni * 16) : ((ni >> 1) * 64 + wn * 32 + (ni & 1) * 16);
    csv[ni] = *(const f32x4*)(scs + ci + fq * 4);
  }
  if (nk) {
    const float sgn = cb < 4 ? 1.0f : -1.0f;
    u16* dst = ab + (size_t)(cb >> 2) * (S_ * D_) + (size_t)(row0 + wm * 64 + fr) * D_ + (cb & 3) * 128 + wn * 64 + fq * 4;
#pragma unroll
    for (int mi = 0; mi < 4; ++mi) {
      const float r = rinv[mi] * sgn;
#pragma unroll
      for (int ni = 0; ni < 4; ++ni) {
        f32x4 v = (acc[ni][mi] - mb[mi] * csv[ni]) * r;
        u32x2 o = {pack2(v[0], v[1]), pack2(v[2], v[3])};
        *(u32x2*)(dst + mi * 16 * D_ + ni * 16) = o;
      }
    }
  } else {
    const int fc = f0 + wn * 32 + fq * 4;
    u16* dst = h + (size_t)(row0 + wm * 64 + fr) * FP_ + fc;
    const unsigned vmask = (f0 + wn * 32) < F_ ? 0xffffffffu : 0u;
#pragma unroll
    for (int mi = 0; mi < 4; ++mi) {
#pragma unroll
      for (int ni = 0; ni < 2; ++ni) {
        f32x4 g = (acc[ni][mi] - mb[mi] * csv[ni]) * rinv[mi];
        f32x4 u = (acc[ni + 2][mi] - mb[mi] * csv[ni + 2]) * rinv[mi];
        float hv[4];
#pragma unroll
        for (int e = 0; e < 4; ++e) {
          float gl = bf16r(gelu_tanh(bf16r(g[e])));
          hv[e] = gl * bf16r(u[e]);
        }
        u32x2 o = {pack2(hv[0], hv[1]) & vmask, pack2(hv[2], hv[3]) & vmask};
        *(u32x2*)(dst + mi * 16 * FP_ + ni * 16) = o;
      }
    }
  }
  WAIT_V(0);
  asm volatile("" ::"v"(sink));
}

constexpr int K2_STAGE = 32768, K2_NST = 5;
constexpr int CNT_STRIDE = 64;

__global__ __launch_bounds__(512, 2) void k2_kernel(const u16* __restrict__ x, const u16* __restrict__ ws_re,
                                                    const u16* __restrict__ ws_im, const u16* __restrict__ w_down,
                                                    const u16* __restrict__ ab, const u16* __restrict__ h,
                                                    u16* __restrict__ out, float* slab, unsigned* cnt) {
  __shared__ __attribute__((aligned(1024))) char smem[K2_NST * K2_STAGE];
  const int tid = threadIdx.x, wid = __builtin_amdgcn_readfirstlane(tid >> 6), lane = tid & 63;
  const int fr = lane & 15, fq = lane >> 4;
  const int bid = blockIdx.x;
  const int idx = bid >> 3;
  const int q = (bid & 7) >> 1;
  const int rb = (bid & 1) * 8 + (idx >> 2), cbk = idx & 3;
  const int row0 = rb * 128, n0 = cbk * 128, tile = rb * 4 + cbk;
  const int t_begin = q < 2 ? 22 * q : 44 + 21 * (q - 2);
  const int nt = q < 2 ? 22 : 21;

  if (wid >= 4) {
    const int lw = wid - 4;
    const int l3 = lane >> 3, l7 = lane & 7;
    const int rt_c0 = (l7 ^ (fq & 7)) * 8, rt_c1 = (l7 ^ ((fq + 4) & 7)) * 8;
    const int kt_c0 = (l7 ^ ((fq & 1) << 1)) * 8, kt_c1 = (l7 ^ (((fq & 1) | 2) << 1)) * 8;
    const int sub = lw & 1;
    auto stage = [&](int j, int slot) {
      const int t = t_begin + j;
      char* dst = smem + slot * K2_STAGE + lw * 8192;
      if (lw < 2) {
        const u16* A;
        int lda, k0;
        if (t < 64) { A = (t < 32 ? ws_re : ws_im); lda = S_; k0 = (t & 31) * 64; }
        else { A = h; lda = FP_; k0 = (t - 64) * 64; }
        A += (size_t)(row0 + sub * 64) * lda;
#pragma unroll
        for (int i = 0; i < 8; ++i) {
          const u16* src = A + (i * 8 + l3) * lda + k0 + ((i & 1) ? rt_c1 : rt_c0);
          GLDS16(src, dst + i * 1024);
        }
      } else {
        const u16* B;
        int k0, kmax;
        if (t < 64) { B = ab + (size_t)(t >> 5) * (S_ * D_); k0 = (t & 31) * 64; kmax = S_ - 1; }
        else { B = w_down; k0 = (t - 64) * 64; kmax = F_ - 1; }
#pragma unroll
        for (int i = 0; i < 8; ++i) {
          const int krow = min(k0 + i * 8 + l3, kmax);
          const u16* src = B + krow * D_ + n0 + sub * 64 + ((i & 1) ? kt_c1 : kt_c0);
          GLDS16(src, dst + i * 1024);
        }
      }
    };
#pragma unroll
    for (int s = 0; s < K2_NST - 1; ++s) stage(s, s);
    int islot = K2_NST - 1;
    for (int j = 0; j < nt; ++j) {
      const int rem = nt - 1 - j;
      if (rem >= 3) WAIT_V(24);
      else if (rem == 2) WAIT_V(16);
      else if (rem == 1) WAIT_V(8);
      else WAIT_V(0);
      BARRIER();
      if (j + K2_NST - 1 < nt) stage(j + K2_NST - 1, islot);
      islot = (islot == K2_NST - 1) ? 0 : islot + 1;
    }
    return;
  }
  const int wm = wid >> 1, wn = wid & 1;
  f32x4 acc[4][4];
#pragma unroll
  for (int i = 0; i < 4; ++i)
#pragma unroll
    for (int j = 0; j < 4; ++j) acc[i][j] = f32x4{0.f, 0.f, 0.f, 0.f};

  const int sw = (fr >> 1) & 7;
  const unsigned a_base0 = lds_addr(smem) + (wm * 64 + fr) * 128 + ((fq) ^ sw) * 16;
  const unsigned a_base1 = lds_addr(smem) + (wm * 64 + fr) * 128 + ((4 + fq) ^ sw) * 16;
  const int tq = (lane >> 2) & 3, tp = lane & 3;
  const int tswz = ((tq >> 1) & 1) | ((fq & 1) << 1);
  const unsigned b_base = lds_addr(smem) + 16384 + wn * 8192 + (8 * fq + tq) * 128 + 8 * tp;
  unsigned b_off[4];
#pragma unroll
  for (int ni = 0; ni < 4; ++ni) b_off[ni] = b_base + 32 * (ni ^ tswz);

  bf16x8 af0[4], bf0[4], af1[4], bf1[4];
  auto rdg = [&](auto KSC, bf16x8(&af)[4], bf16x8(&bfr)[4], unsigned so, int g) {
    constexpr int KS = decltype(KSC)::value;
    const unsigned ab_ = (KS ? a_base1 : a_base0) + so;
    switch (g) {
      case 0: af[0] = lds_rd128<0>(ab_); break;
      case 2: af[1] = lds_rd128<2048>(ab_); break;
      case 4: af[2] = lds_rd128<4096>(ab_); break;
      case 6: af[3] = lds_rd128<6144>(ab_); break;
      case 1: bfr[0] = tr_frag<KS * 4096>(b_off[0] + so); break;
      case 3: bfr[1] = tr_frag<KS * 4096>(b_off[1] + so); break;
      case 5: bfr[2] = tr_frag<KS * 4096>(b_off[2] + so); break;
      default: bfr[3] = tr_frag<KS * 4096>(b_off[3] + so); break;
    }
  };
  using C0 = std::integral_constant<int, 0>;
  using C1 = std::integral_constant<int, 1>;
  BARRIER();
#pragma unroll
  for (int g = 0; g < 8; ++g) rdg(C0{}, af0, bf0, 0u, g);
  WAIT_L0();
  SCHED0();
  unsigned so = 0;
  for (int j = 0; j < nt; ++j) {
    mm16(acc, bf0, af0, [&](int g) { rdg(C1{}, af1, bf1, so, g); });
    WAIT_L0();
    SCHED0();
    so = (so == (K2_NST - 1) * K2_STAGE) ? 0u : so + K2_STAGE;
    if (j + 1 < nt) {
      BARRIER();
      mm16(acc, bf1, af1, [&](int g) { rdg(C0{}, af0, bf0, so, g); });
    } else {
      mm16(acc, bf1, af1, [&](int) {});
    }
    WAIT_L0();
    SCHED0();
  }
  const __amdgpu_buffer_rsrc_t rs = __builtin_amdgcn_make_buffer_rsrc(slab, 0, 64 * 128 * 1024, 0x00020000);
  f32x4 own[4];
#pragma unroll
  for (int mi = 0; mi < 4; ++mi) {
    if (mi == q) {
#pragma unroll
      for (int ni = 0; ni < 4; ++ni) own[ni] = acc[ni][mi];
    } else {
      const unsigned base = (unsigned)((((tile * 4 + mi) * 4 + q) * 4 + wid) * 2) * 1024u + lane * 16u;
#pragma unroll
      for (int np = 0; np < 2; ++np) {
        const f32x4 a0 = acc[2 * np][mi], a1 = acc[2 * np + 1][mi];
        u32x4 v = {pack2(a0[0], a0[1]), pack2(a0[2], a0[3]), pack2(a1[0], a1[1]), pack2(a1[2], a1[3])};
        __builtin_amdgcn_raw_buffer_store_b128(v, rs, base + np * 1024u, 0, 16);
      }
    }
  }
  const size_t orow = (size_t)(row0 + wm * 64 + q * 16 + fr) * D_ + n0 + wn * 64 + fq * 4;
  u32x2 xv[4];
#pragma unroll
  for (int ni = 0; ni < 4; ++ni) xv[ni] = *(const u32x2*)(x + orow + ni * 16);
  WAIT_V(0);
  BARRIER();
  if (wid == 0) {
    unsigned* c = cnt + tile * CNT_STRIDE;
    if (lane == 0) __hip_atomic_fetch_add(c, 1u, __ATOMIC_RELAXED, __HIP_MEMORY_SCOPE_AGENT);
    for (unsigned spins = 0; spins < (1u << 22); ++spins) {
      const unsigned v = __builtin_amdgcn_readfirstlane(__hip_atomic_load(c, __ATOMIC_RELAXED, __HIP_MEMORY_SCOPE_AGENT));
      if (v >= 4u) break;
      __builtin_amdgcn_s_sleep(1);
    }
  }
  BARRIER();
  u32x4 pv[3][2];
#pragma unroll
  for (int sidx = 0; sidx < 3; ++sidx) {
    const int src = sidx + (sidx >= q ? 1 : 0);
    const unsigned base = (unsigned)((((tile * 4 + q) * 4 + src) * 4 + wid) * 2) * 1024u + lane * 16u;
#pragma unroll
    for (int np = 0; np < 2; ++np) pv[sidx][np] = __builtin_amdgcn_raw_buffer_load_b128(rs, base + np * 1024u, 0, 16);
  }
#pragma unroll
  for (int ni = 0; ni < 4; ++ni) {
    f32x4 v = own[ni];
#pragma unroll
    for (int sidx = 0; sidx < 3; ++sidx) {
      const unsigned w0 = pv[sidx][ni >> 1][(ni & 1) * 2], w1 = pv[sidx][ni >> 1][(ni & 1) * 2 + 1];
      v[0] += bflo(w0); v[1] += bfhi(w0); v[2] += bflo(w1); v[3] += bfhi(w1);
    }
    u32x2 o = {pack2(v[0] + bflo(xv[ni][0]), v[1] + bfhi(xv[ni][0])), pack2(v[2] + bflo(xv[ni][1]), v[3] + bfhi(xv[ni][1]))};
    *(u32x2*)(out + orow + ni * 16) = o;
  }
}

extern "C" void kernel_launch(void* const* d_in, const int* in_sizes, int n_in, void* d_out, int out_size,
                              void* d_ws, size_t ws_size, hipStream_t stream) {
  const u16* x = (const u16*)d_in[0];
  const u16* ws_re = (const u16*)d_in[1];
  const u16* ws_im = (const u16*)d_in[2];
  const u16* wd_re = (const u16*)d_in[3];
  const u16* wd_im = (const u16*)d_in[4];
  const u16* w_gate = (const u16*)d_in[6];
  const u16* w_up = (const u16*)d_in[7];
  const u16* w_down = (const u16*)d_in[8];
  u16* ab = (u16*)d_ws;
  u16* h = ab + (size_t)2 * S_ * D_;
  float* slab = (float*)((char*)d_ws + (16u << 20));
  unsigned* cnt = (unsigned*)((char*)d_ws + (32u << 20));
  k1_kernel<<<240, 768, 0, stream>>>(x, wd_re, wd_im, w_gate, w_up, ab, h, cnt, ws_re, ws_im, w_down);
  k2_kernel<<<256, 512, 0, stream>>>(x, ws_re, ws_im, w_down, ab, h, (u16*)d_out, slab, cnt);
}
```

```cpp
#include <hip/hip_runtime.h>
#include <hip/hip_bf16.h>
#include <stdint.h>
#include <type_traits>

typedef unsigned short u16;
typedef short bf16x8 __attribute__((ext_vector_type(8)));
typedef short bf16x4 __attribute__((ext_vector_type(4)));
typedef float f32x4 __attribute__((ext_vector_type(4)));
typedef float f32x2 __attribute__((ext_vector_type(2)));
typedef unsigned u32x4 __attribute__((ext_vector_type(4)));
typedef unsigned u32x2 __attribute__((ext_vector_type(2)));
typedef __bf16 bf2_t __attribute__((ext_vector_type(2)));

#define K2_PF 12
#define K2_ROT 1
#define K1_WARM_T 2
#define K1_ROT 1
constexpr int S_ = 2048, D_ = 512, F_ = 1376, FP_ = 1408;

#define LDS_AS __attribute__((address_space(3)))
#define GLDS16(gptr, ldsptr) \
  __builtin_amdgcn_global_load_lds((const unsigned*)(gptr), (unsigned*)(ldsptr), 16, 0, 0)
#define WAIT_V(n) asm volatile("s_waitcnt vmcnt(" #n ")" ::: "memory")
#define BARRIER() asm volatile("s_barrier" ::: "memory")
#define WAIT_L0() asm volatile("s_waitcnt lgkmcnt(0)" ::: "memory")

__device__ __forceinline__ float bf16r(float x) { return __bfloat162float(__float2bfloat16(x)); }
__device__ __forceinline__ unsigned pack2(float lo, float hi) {
  __hip_bfloat16 a = __float2bfloat16(lo), b = __float2bfloat16(hi);
  return (unsigned)__builtin_bit_cast(u16, a) | ((unsigned)__builtin_bit_cast(u16, b) << 16);
}
__device__ __forceinline__ float bflo(unsigned v) { return __builtin_bit_cast(float, v << 16); }
__device__ __forceinline__ float bfhi(unsigned v) { return __builtin_bit_cast(float, v & 0xffff0000u); }

__device__ __forceinline__ float dot2(unsigned a, unsigned b, float c) {
#if defined(__HIP_DEVICE_COMPILE__)
  return __builtin_amdgcn_fdot2_f32_bf16(__builtin_bit_cast(bf2_t, a), __builtin_bit_cast(bf2_t, b), c, false);
#else
  return c;
#endif
}
__device__ __forceinline__ bf16x4 tr_read(const char* p) {
#if defined(__HIP_DEVICE_COMPILE__)
  return __builtin_amdgcn_ds_read_tr16_b64_v4i16((LDS_AS bf16x4*)(p));
#else
  return bf16x4{};
#endif
}
template <int OFF>
__device__ __forceinline__ bf16x8 tr_frag(unsigned addr) {
  bf16x4 a, b;
  asm volatile("ds_read_b64_tr_b16 %0, %2 offset:%3\n\tds_read_b64_tr_b16 %1, %2 offset:%4"
               : "=&v"(a), "=&v"(b)
               : "v"(addr), "i"(OFF), "i"(OFF + 512));
  return __builtin_shufflevector(a, b, 0, 1, 2, 3, 4, 5, 6, 7);
}
template <int OFF>
__device__ __forceinline__ bf16x8 lds_rd128(unsigned addr) {
  bf16x8 a;
  asm volatile("ds_read_b128 %0, %1 offset:%2" : "=&v"(a) : "v"(addr), "i"(OFF));
  return a;
}
__device__ __forceinline__ unsigned lds_addr(const void* p) {
  return (unsigned)(uintptr_t)(const LDS_AS char*)p;
}
__device__ __forceinline__ bf16x8 cat8(bf16x4 a, bf16x4 b) {
  return __builtin_shufflevector(a, b, 0, 1, 2, 3, 4, 5, 6, 7);
}
__device__ __forceinline__ float gelu_tanh(float g) {
  const float c1 = -2.0f * 0.7978845608028654f * 1.4426950408889634f;
  const float c2 = c1 * 0.044715f;
  float w = g * __builtin_fmaf(g * g, c2, c1);
  float e = __builtin_amdgcn_exp2f(w);
  return g * __builtin_amdgcn_rcpf(1.0f + e);
}

__device__ __forceinline__ void touch(const void* p, unsigned& sink) {
  asm volatile("global_load_dword %0, %1, off" : "+v"(sink) : "v"(p) : "memory");
}
#define SCHED0() __builtin_amdgcn_sched_barrier(0)
template <class RD>
__device__ __forceinline__ void mm16(f32x4 (&acc)[4][4], const bf16x8 (&wf)[4], const bf16x8 (&xf)[4], RD rd) {
#pragma unroll
  for (int i = 0; i < 16; ++i) {
    const int ni = i >> 2, mi = i & 3;
    acc[ni][mi] = __builtin_amdgcn_mfma_f32_16x16x32_bf16(wf[ni], xf[mi], acc[ni][mi], 0, 0, 0);
    if (i < 8) {
      SCHED0();
      rd(i);
      SCHED0();
    }
  }
  SCHED0();
}

constexpr int K1_A = 32768, K1_B = 16384, K1_STAGE = K1_A + K1_B;

__global__ __launch_bounds__(768, 3) void k1_kernel(const u16* __restrict__ x, const u16* __restrict__ wd_re,
                                                    const u16* __restrict__ wd_im, const u16* __restrict__ w_gate,
                                                    const u16* __restrict__ w_up, u16* __restrict__ ab,
                                                    u16* __restrict__ h, unsigned* __restrict__ cnt,
                                                    const u16* __restrict__ ws_re, const u16* __restrict__ ws_im,
                                                    const u16* __restrict__ w_down) {
  __shared__ __attribute__((aligned(1024))) char smem[3 * K1_STAGE];
  const int tid = threadIdx.x, wid = __builtin_amdgcn_readfirstlane(tid >> 6), lane = tid & 63;
  const int fr = lane & 15, fq = lane >> 4;
  const int bid = blockIdx.x;
  const int nb = (bid & 7) * 30 + (bid >> 3);
  const int cb = nb >> 3, rb = nb & 7;
  const bool nk = cb < 8;
  const int row0 = rb * 256;
  const int f0 = (cb - 8) * 64;
  if (bid == 0 && tid < 64) cnt[tid * 64] = 0u;
  const int sw = (fr >> 1) & 7;
  const int ch0 = ((fq) ^ sw) * 16, ch1 = ((4 + fq) ^ sw) * 16;
  const int tq = (lane >> 2) & 3, tp = lane & 3;
  const int tswz = ((tq >> 1) & 1) | ((fq & 1) << 1);
  const int tr_row = (8 * fq + tq) * 128 + 8 * tp;
  float* sst = (float*)(smem + 2 * K1_STAGE);
  float* scs = (float*)(smem + 2 * K1_STAGE + 8192);

  if (wid >= 8) {
    const int lw = wid - 8;
    __builtin_amdgcn_s_setprio(2);
    const int l3 = lane >> 3, l7 = lane & 7;
    const int rt_c0 = (l7 ^ (fq & 7)) * 8, rt_c1 = (l7 ^ ((fq + 4) & 7)) * 8;
    const int kt_c0 = (l7 ^ ((fq & 1) << 1)) * 8, kt_c1 = (l7 ^ (((fq & 1) | 2) << 1)) * 8;
    const u16* xa0 = x + (size_t)(row0 + lw * 64 + l3) * D_ + rt_c0;
    const u16* xa1 = x + (size_t)(row0 + lw * 64 + 8 + l3) * D_ + rt_c1;
    const u16 *wb0, *wb1;
    int wstep_j, wstep_t;
    if (nk) {
      const u16* wnk = (cb < 4 ? wd_re : wd_im) + (size_t)((cb & 3) * 128 + lw * 32 + l3) * D_;
      wb0 = wnk + rt_c0;
      wb1 = wnk + 8 * D_ + rt_c1;
      wstep_j = 16 * D_;
      wstep_t = 64;
    } else {
      const u16* w = (lw < 2) ? w_gate : w_up;
      int c0 = f0 + kt_c0, c1 = f0 + kt_c1;
      if (c0 >= F_) c0 -= 32;
      if (c1 >= F_) c1 -= 32;
      const u16* wk = w + (size_t)((lw & 1) * 32 + l3) * F_;
      wb0 = wk + c0;
      wb1 = wk + 8 * F_ + c1;
      wstep_j = 16 * F_;
      wstep_t = 64 * F_;
    }
    auto stage = [&](int tj, int buf) {
      const int t = (tj + K1_ROT * cb) & 7;
      char* sa = smem + buf * K1_STAGE + lw * 8192;
      char* sb = smem + buf * K1_STAGE + K1_A + lw * 4096;
#pragma unroll
      for (int i = 0; i < 4; ++i) {
        GLDS16(xa0 + i * 16 * D_ + t * 64, sa + i * 2048);
        GLDS16(xa1 + i * 16 * D_ + t * 64, sa + i * 2048 + 1024);
      }
#pragma unroll
      for (int i = 0; i < 2; ++i) {
        GLDS16(wb0 + i * wstep_j + t * wstep_t, sb + i * 2048);
        GLDS16(wb1 + i * wstep_j + t * wstep_t, sb + i * 2048 + 1024);
      }
    };
    stage(0, 0);
    stage(1, 1);
    f32x4 accS[4], accG[4], accC[2];
#pragma unroll
    for (int i = 0; i < 4; ++i) {
      accS[i] = f32x4{0.f, 0.f, 0.f, 0.f};
      accG[i] = f32x4{0.f, 0.f, 0.f, 0.f};
    }
    accC[0] = f32x4{0.f, 0.f, 0.f, 0.f};
    accC[1] = f32x4{0.f, 0.f, 0.f, 0.f};
    const short ONE = 0x3f80;
    const bf16x8 ones = {ONE, ONE, ONE, ONE, ONE, ONE, ONE, ONE};
    const unsigned la0 = lds_addr(smem) + (lw * 64 + fr) * 128 + ch0;
    const unsigned la1 = lds_addr(smem) + (lw * 64 + fr) * 128 + ch1;
    const unsigned lb0 = lds_addr(smem) + K1_A + (lw * 32 + fr) * 128 + ch0;
    const unsigned lb1 = lds_addr(smem) + K1_A + (lw * 32 + fr) * 128 + ch1;
    const unsigned lt0 = lds_addr(smem) + K1_A + (lw >> 1) * 8192 + tr_row + 32 * (((lw & 1) * 2) ^ tswz);
    const unsigned lt1 = lds_addr(smem) + K1_A + (lw >> 1) * 8192 + tr_row + 32 * (((lw & 1) * 2 + 1) ^ tswz);
    int ib = 2, buf = 0;
    for (int t = 0; t < 8; ++t) {
      if (t < 7) WAIT_V(12); else WAIT_V(0);
      BARRIER();
      const unsigned so = buf * K1_STAGE;
      bf16x8 xf[8], wf[4];
      xf[0] = lds_rd128<0>(la0 + so); xf[1] = lds_rd128<2048>(la0 + so);
      xf[2] = lds_rd128<4096>(la0 + so); xf[3] = lds_rd128<6144>(la0 + so);
      xf[4] = lds_rd128<0>(la1 + so); xf[5] = lds_rd128<2048>(la1 + so);
      xf[6] = lds_rd128<4096>(la1 + so); xf[7] = lds_rd128<6144>(la1 + so);
      if (nk) {
        wf[0] = lds_rd128<0>(lb0 + so); wf[1] = lds_rd128<2048>(lb0 + so);
        wf[2] = lds_rd128<0>(lb1 + so); wf[3] = lds_rd128<2048>(lb1 + so);
      } else {
        wf[0] = tr_frag<0>(lt0 + so); wf[1] = tr_frag<0>(lt1 + so);
        wf[2] = tr_frag<4096>(lt0 + so); wf[3] = tr_frag<4096>(lt1 + so);
      }
      if (t + 2 < 8) stage(t + 2, ib);
      ib = (ib == 2) ? 0 : ib + 1;
      WAIT_L0();
      __builtin_amdgcn_sched_barrier(0);
#pragma unroll
      for (int i = 0; i < 8; ++i) {
        accS[i & 3] = __builtin_amdgcn_mfma_f32_16x16x32_bf16(ones, xf[i], accS[i & 3], 0, 0, 0);
        accG[i & 3] = __builtin_amdgcn_mfma_f32_16x16x32_bf16(xf[i], xf[i], accG[i & 3], 0, 0, 0);
      }
#pragma unroll
      for (int i = 0; i < 4; ++i)
        accC[i & 1] = __builtin_amdgcn_mfma_f32_16x16x32_bf16(wf[i], ones, accC[i & 1], 0, 0, 0);
      buf = (buf == 2) ? 0 : buf + 1;
    }
    if (fq == (fr >> 2)) {
#pragma unroll
      for (int i = 0; i < 4; ++i) {
        const int r = fr & 3;
        const float q = r == 0 ? accG[i][0] : r == 1 ? accG[i][1] : r == 2 ? accG[i][2] : accG[i][3];
        const float mean = accS[i][0] * (1.0f / D_);
        const float var = fmaxf(q * (1.0f / D_) - mean * mean, 0.f);
        const float sd = bf16r(sqrtf(bf16r(bf16r(var) + 1e-5f)));
        f32x2 o = {bf16r(mean), __builtin_amdgcn_rcpf(sd)};
        *(f32x2*)(sst + (lw * 64 + i * 16 + fr) * 2) = o;
      }
    }
    if (fr == 0) {
      *(f32x4*)(scs + lw * 32 + fq * 4) = accC[0];
      *(f32x4*)(scs + lw * 32 + 16 + fq * 4) = accC[1];
    }
    WAIT_L0();
    BARRIER();
    return;
  }

  const int wm = wid >> 1, wn = wid & 1;
  f32x4 acc[4][4];
#pragma unroll
  for (int i = 0; i < 4; ++i)
#pragma unroll
    for (int j = 0; j < 4; ++j) acc[i][j] = f32x4{0.f, 0.f, 0.f, 0.f};
  const unsigned a_b0 = lds_addr(smem) + (wm * 64 + fr) * 128 + ch0, a_b1 = lds_addr(smem) + (wm * 64 + fr) * 128 + ch1;
  const unsigned n_b0 = lds_addr(smem) + K1_A + (wn * 64 + fr) * 128 + ch0;
  const unsigned n_b1 = lds_addr(smem) + K1_A + (wn * 64 + fr) * 128 + ch1;
  const unsigned t_p0 = lds_addr(smem) + K1_A + tr_row + 32 * ((wn * 2) ^ tswz);
  const unsigned t_p1 = lds_addr(smem) + K1_A + tr_row + 32 * ((wn * 2 + 1) ^ tswz);

  unsigned sink = 0;
  auto warm = [&](int t) {
    if ((lane & 7) != t) return;
    const int g = (bid * 8 + wid) * 64 + (lane & 7) * 8 + (lane >> 3);
    touch((g < 65536 ? ws_re + (size_t)g * 64 : ws_im + (size_t)(g - 65536) * 64), sink);
    const int g2 = g + 122880;
    if (g2 < 142080) touch((g2 < 131072 ? ws_im + (size_t)(g2 - 65536) * 64 : w_down + (size_t)(g2 - 131072) * 64), sink);
  };
  auto mainloop = [&](auto NKC) {
    constexpr bool NK = decltype(NKC)::value;
    bf16x8 xf0[4], wf0[4], xf1[4], wf1[4];
    auto rdg = [&](auto KSC, bf16x8(&xf)[4], bf16x8(&wf)[4], unsigned so, int g) {
      constexpr int KS = decltype(KSC)::value;
      const unsigned ab_ = (KS ? a_b1 : a_b0) + so;
      switch (g) {
        case 0: xf[0] = lds_rd128<0>(ab_); break;
        case 2: xf[1] = lds_rd128<2048>(ab_); break;
        case 4: xf[2] = lds_rd128<4096>(ab_); break;
        case 6: xf[3] = lds_rd128<6144>(ab_); break;
        default:
          if constexpr (NK) {
            const unsigned nb_ = (KS ? n_b1 : n_b0) + so;
            if (g == 1) wf[0] = lds_rd128<0>(nb_);
            if (g == 3) wf[1] = lds_rd128<2048>(nb_);
            if (g == 5) wf[2] = lds_rd128<4096>(nb_);
            if (g == 7) wf[3] = lds_rd128<6144>(nb_);
          } else {
            if (g == 1) wf[0] = tr_frag<KS * 4096>(t_p0 + so);
            if (g == 3) wf[1] = tr_frag<KS * 4096>(t_p1 + so);
            if (g == 5) wf[2] = tr_frag<KS * 4096 + 8192>(t_p0 + so);
            if (g == 7) wf[3] = tr_frag<KS * 4096 + 8192>(t_p1 + so);
          }
      }
    };
    using C0 = std::integral_constant<int, 0>;
    using C1 = std::integral_constant<int, 1>;
    BARRIER();
#pragma unroll
    for (int g = 0; g < 8; ++g) rdg(C0{}, xf0, wf0, 0u, g);
    WAIT_L0();
    SCHED0();
    unsigned so = 0;
    for (int t = 0; t < 8; ++t) {
      if (K1_WARM_T < 8) warm(t);
      mm16(acc, wf0, xf0, [&](int g) { rdg(C1{}, xf1, wf1, so, g); });
      WAIT_L0();
      SCHED0();
      so = (so == 2 * K1_STAGE) ? 0u : so + K1_STAGE;
      if (t < 7) {
        BARRIER();
        mm16(acc, wf1, xf1, [&](int g) { rdg(C0{}, xf0, wf0, so, g); });
      } else {
        mm16(acc, wf1, xf1, [&](int) {});
      }
      WAIT_L0();
      SCHED0();
    }
  };
  if (nk) mainloop(std::true_type{});
  else mainloop(std::false_type{});
  BARRIER();
  float mb[4], rinv[4];
#pragma unroll
  for (int mi = 0; mi < 4; ++mi) {
    f32x2 v = *(const f32x2*)(sst + (wm * 64 + mi * 16 + fr) * 2);
    mb[mi] = v[0];
    rinv[mi] = v[1];
  }
  f32x4 csv[4];
#pragma unroll
  for (int ni = 0; ni < 4; ++ni) {
    const int ci = nk ? (wn * 64 + ni * 16) : ((ni >> 1) * 64 + wn * 32 + (ni & 1) * 16);
    csv[ni] = *(const f32x4*)(scs + ci + fq * 4);
  }
  if (nk) {
    const float sgn = cb < 4 ? 1.0f : -1.0f;
    u16* dst = ab + (size_t)(cb >> 2) * (S_ * D_) + (size_t)(row0 + wm * 64 + fr) * D_ + (cb & 3) * 128 + wn * 64 + fq * 4;
#pragma unroll
    for (int mi = 0; mi < 4; ++mi) {
      const float r = rinv[mi] * sgn;
#pragma unroll
      for (int ni = 0; ni < 4; ++ni) {
        f32x4 v = (acc[ni][mi] - mb[mi] * csv[ni]) * r;
        u32x2 o = {pack2(v[0], v[1]), pack2(v[2], v[3])};
        *(u32x2*)(dst + mi * 16 * D_ + ni * 16) = o;
      }
    }
  } else {
    const int fc = f0 + wn * 32 + fq * 4;
    u16* dst = h + (size_t)(row0 + wm * 64 + fr) * FP_ + fc;
    const unsigned vmask = (f0 + wn * 32) < F_ ? 0xffffffffu : 0u;
#pragma unroll
    for (int mi = 0; mi < 4; ++mi) {
#pragma unroll
      for (int ni = 0; ni < 2; ++ni) {
        f32x4 g = (acc[ni][mi] - mb[mi] * csv[ni]) * rinv[mi];
        f32x4 u = (acc[ni + 2][mi] - mb[mi] * csv[ni + 2]) * rinv[mi];
        float hv[4];
#pragma unroll
        for (int e = 0; e < 4; ++e) {
          float gl = bf16r(gelu_tanh(bf16r(g[e])));
          hv[e] = gl * bf16r(u[e]);
        }
        u32x2 o = {pack2(hv[0], hv[1]) & vmask, pack2(hv[2], hv[3]) & vmask};
        *(u32x2*)(dst + mi * 16 * FP_ + ni * 16) = o;
      }
    }
  }
  WAIT_V(0);
  asm volatile("" ::"v"(sink));
}

constexpr int K2_STAGE = 32768, K2_NST = 5;
constexpr int CNT_STRIDE = 64;

__global__ __launch_bounds__(512, 2) void k2_kernel(const u16* __restrict__ x, const u16* __restrict__ ws_re,
                                                    const u16* __restrict__ ws_im, const u16* __restrict__ w_down,
                                                    const u16* __restrict__ ab, const u16* __restrict__ h,
                                                    u16* __restrict__ out, float* slab, unsigned* cnt) {
  __shared__ __attribute__((aligned(1024))) char smem[K2_NST * K2_STAGE];
  const int tid = threadIdx.x, wid = __builtin_amdgcn_readfirstlane(tid >> 6), lane = tid & 63;
  const int fr = lane & 15, fq = lane >> 4;
  const int bid = blockIdx.x;
  const int idx = bid >> 3;
  const int q = (bid & 7) >> 1;
  const int rb = (bid & 1) * 8 + (idx >> 2), cbk = idx & 3;
  const int row0 = rb * 128, n0 = cbk * 128, tile = rb * 4 + cbk;
  const int t_begin = q < 2 ? 22 * q : 44 + 21 * (q - 2);
  const int nt = q < 2 ? 22 : 21;

  if (wid >= 4) {
    const int lw = wid - 4;
    const int l3 = lane >> 3, l7 = lane & 7;
    const int rt_c0 = (l7 ^ (fq & 7)) * 8, rt_c1 = (l7 ^ ((fq + 4) & 7)) * 8;
    const int kt_c0 = (l7 ^ ((fq & 1) << 1)) * 8, kt_c1 = (l7 ^ (((fq & 1) | 2) << 1)) * 8;
    const int sub = lw & 1;
    auto stage = [&](int j, int slot) {
      const int t = t_begin + j;
      char* dst = smem + slot * K2_STAGE + lw * 8192;
      if (lw < 2) {
        const u16* A;
        int lda, k0;
        if (t < 64) { A = (t < 32 ? ws_re : ws_im); lda = S_; k0 = (t & 31) * 64; }
        else { A = h; lda = FP_; k0 = (t - 64) * 64; }
        A += (size_t)(row0 + sub * 64) * lda;
#pragma unroll
        for (int i = 0; i < 8; ++i) {
          const u16* src = A + (i * 8 + l3) * lda + k0 + ((i & 1) ? rt_c1 : rt_c0);
          GLDS16(src, dst + i * 1024);
        }
      } else {
        const u16* B;
        int k0, kmax;
        if (t < 64) { B = ab + (size_t)(t >> 5) * (S_ * D_); k0 = (t & 31) * 64; kmax = S_ - 1; }
        else { B = w_down; k0 = (t - 64) * 64; kmax = F_ - 1; }
#pragma unroll
        for (int i = 0; i < 8; ++i) {
          const int krow = min(k0 + i * 8 + l3, kmax);
          const u16* src = B + krow * D_ + n0 + sub * 64 + ((i & 1) ? kt_c1 : kt_c0);
          GLDS16(src, dst + i * 1024);
        }
      }
    };
#pragma unroll
    for (int s = 0; s < K2_NST - 1; ++s) stage(s, s);
    int islot = K2_NST - 1;
    for (int j = 0; j < nt; ++j) {
      const int rem = nt - 1 - j;
      if (rem >= 3) WAIT_V(24);
      else if (rem == 2) WAIT_V(16);
      else if (rem == 1) WAIT_V(8);
      else WAIT_V(0);
      BARRIER();
      if (j + K2_NST - 1 < nt) stage(j + K2_NST - 1, islot);
      islot = (islot == K2_NST - 1) ? 0 : islot + 1;
    }
    return;
  }
  const int wm = wid >> 1, wn = wid & 1;
  f32x4 acc[4][4];
#pragma unroll
  for (int i = 0; i < 4; ++i)
#pragma unroll
    for (int j = 0; j < 4; ++j) acc[i][j] = f32x4{0.f, 0.f, 0.f, 0.f};

  const int sw = (fr >> 1) & 7;
  const unsigned a_base0 = lds_addr(smem) + (wm * 64 + fr) * 128 + ((fq) ^ sw) * 16;
  const unsigned a_base1 = lds_addr(smem) + (wm * 64 + fr) * 128 + ((4 + fq) ^ sw) * 16;
  const int tq = (lane >> 2) & 3, tp = lane & 3;
  const int tswz = ((tq >> 1) & 1) | ((fq & 1) << 1);
  const unsigned b_base = lds_addr(smem) + 16384 + wn * 8192 + (8 * fq + tq) * 128 + 8 * tp;
  unsigned b_off[4];
#pragma unroll
  for (int ni = 0; ni < 4; ++ni) b_off[ni] = b_base + 32 * (ni ^ tswz);

  bf16x8 af0[4], bf0[4], af1[4], bf1[4];
  auto rdg = [&](auto KSC, bf16x8(&af)[4], bf16x8(&bfr)[4], unsigned so, int g) {
    constexpr int KS = decltype(KSC)::value;
    const unsigned ab_ = (KS ? a_base1 : a_base0) + so;
    switch (g) {
      case 0: af[0] = lds_rd128<0>(ab_); break;
      case 2: af[1] = lds_rd128<2048>(ab_); break;
      case 4: af[2] = lds_rd128<4096>(ab_); break;
      case 6: af[3] = lds_rd128<6144>(ab_); break;
      case 1: bfr[0] = tr_frag<KS * 4096>(b_off[0] + so); break;
      case 3: bfr[1] = tr_frag<KS * 4096>(b_off[1] + so); break;
      case 5: bfr[2] = tr_frag<KS * 4096>(b_off[2] + so); break;
      default: bfr[3] = tr_frag<KS * 4096>(b_off[3] + so); break;
    }
  };
  using C0 = std::integral_constant<int, 0>;
  using C1 = std::integral_constant<int, 1>;
  BARRIER();
#pragma unroll
  for (int g = 0; g < 8; ++g) rdg(C0{}, af0, bf0, 0u, g);
  WAIT_L0();
  SCHED0();
  unsigned so = 0;
  for (int j = 0; j < nt; ++j) {
    mm16(acc, bf0, af0, [&](int g) { rdg(C1{}, af1, bf1, so, g); });
    WAIT_L0();
    SCHED0();
    so = (so == (K2_NST - 1) * K2_STAGE) ? 0u : so + K2_STAGE;
    if (j + 1 < nt) {
      BARRIER();
      mm16(acc, bf1, af1, [&](int g) { rdg(C0{}, af0, bf0, so, g); });
    } else {
      mm16(acc, bf1, af1, [&](int) {});
    }
    WAIT_L0();
    SCHED0();
  }
  const __amdgpu_buffer_rsrc_t rs = __builtin_amdgcn_make_buffer_rsrc(slab, 0, 64 * 128 * 1024, 0x00020000);
  f32x4 own[4];
#pragma unroll
  for (int mi = 0; mi < 4; ++mi) {
    if (mi == q) {
#pragma unroll
      for (int ni = 0; ni < 4; ++ni) own[ni] = acc[ni][mi];
    } else {
      const unsigned base = (unsigned)((((tile * 4 + mi) * 4 + q) * 4 + wid) * 2) * 1024u + lane * 16u;
#pragma unroll
      for (int np = 0; np < 2; ++np) {
        const f32x4 a0 = acc[2 * np][mi], a1 = acc[2 * np + 1][mi];
        u32x4 v = {pack2(a0[0], a0[1]), pack2(a0[2], a0[3]), pack2(a1[0], a1[1]), pack2(a1[2], a1[3])};
        __builtin_amdgcn_raw_buffer_store_b128(v, rs, base + np * 1024u, 0, 16);
      }
    }
  }
  const size_t orow = (size_t)(row0 + wm * 64 + q * 16 + fr) * D_ + n0 + wn * 64 + fq * 4;
  u32x2 xv[4];
#pragma unroll
  for (int ni = 0; ni < 4; ++ni) xv[ni] = *(const u32x2*)(x + orow + ni * 16);
  WAIT_V(0);
  BARRIER();
  if (wid == 0) {
    unsigned* c = cnt + tile * CNT_STRIDE;
    if (lane == 0) __hip_atomic_fetch_add(c, 1u, __ATOMIC_RELAXED, __HIP_MEMORY_SCOPE_AGENT);
    for (unsigned spins = 0; spins < (1u << 22); ++spins) {
      const unsigned v = __builtin_amdgcn_readfirstlane(__hip_atomic_load(c, __ATOMIC_RELAXED, __HIP_MEMORY_SCOPE_AGENT));
      if (v >= 4u) break;
      __builtin_amdgcn_s_sleep(1);
    }
  }
  BARRIER();
  u32x4 pv[3][2];
#pragma unroll
  for (int sidx = 0; sidx < 3; ++sidx) {
    const int src = sidx + (sidx >= q ? 1 : 0);
    const unsigned base = (unsigned)((((tile * 4 + q) * 4 + src) * 4 + wid) * 2) * 1024u + lane * 16u;
#pragma unroll
    for (int np = 0; np < 2; ++np) pv[sidx][np] = __builtin_amdgcn_raw_buffer_load_b128(rs, base + np * 1024u, 0, 16);
  }
#pragma unroll
  for (int ni = 0; ni < 4; ++ni) {
    f32x4 v = own[ni];
#pragma unroll
    for (int sidx = 0; sidx < 3; ++sidx) {
      const unsigned w0 = pv[sidx][ni >> 1][(ni & 1) * 2], w1 = pv[sidx][ni >> 1][(ni & 1) * 2 + 1];
      v[0] += bflo(w0); v[1] += bfhi(w0); v[2] += bflo(w1); v[3] += bfhi(w1);
    }
    u32x2 o = {pack2(v[0] + bflo(xv[ni][0]), v[1] + bfhi(xv[ni][0])), pack2(v[2] + bflo(xv[ni][1]), v[3] + bfhi(xv[ni][1]))};
    *(u32x2*)(out + orow + ni * 16) = o;
  }
}

extern "C" void kernel_launch(void* const* d_in, const int* in_sizes, int n_in, void* d_out, int out_size,
                              void* d_ws, size_t ws_size, hipStream_t stream) {
  const u16* x = (const u16*)d_in[0];
  const u16* ws_re = (const u16*)d_in[1];
  const u16* ws_im = (const u16*)d_in[2];
  const u16* wd_re = (const u16*)d_in[3];
  const u16* wd_im = (const u16*)d_in[4];
  const u16* w_gate = (const u16*)d_in[6];
  const u16* w_up = (const u16*)d_in[7];
  const u16* w_down = (const u16*)d_in[8];
  u16* ab = (u16*)d_ws;
  u16* h = ab + (size_t)2 * S_ * D_;
  float* slab = (float*)((char*)d_ws + (16u << 20));
  unsigned* cnt = (unsigned*)((char*)d_ws + (32u << 20));
  k1_kernel<<<240, 768, 0, stream>>>(x, wd_re, wd_im, w_gate, w_up, ab, h, cnt, ws_re, ws_im, w_down);
  k2_kernel<<<256, 512, 0, stream>>>(x, ws_re, ws_im, w_down, ab, h, (u16*)d_out, slab, cnt);
}
```

```cpp
#include <hip/hip_runtime.h>
#include <hip/hip_bf16.h>
#include <stdint.h>
#include <type_traits>

typedef unsigned short u16;
typedef short bf16x8 __attribute__((ext_vector_type(8)));
typedef short bf16x4 __attribute__((ext_vector_type(4)));
typedef float f32x4 __attribute__((ext_vector_type(4)));
typedef float f32x2 __attribute__((ext_vector_type(2)));
typedef unsigned u32x4 __attribute__((ext_vector_type(4)));
typedef unsigned u32x2 __attribute__((ext_vector_type(2)));
typedef __bf16 bf2_t __attribute__((ext_vector_type(2)));

#define K2_PF 12
#define K2_ROT 1
#define K1_WARM_T 2
#define K1_ROT 1
constexpr int S_ = 2048, D_ = 512, F_ = 1376, FP_ = 1408;

#define LDS_AS __attribute__((address_space(3)))
#define GLDS16(gptr, ldsptr) \
  __builtin_amdgcn_global_load_lds((const unsigned*)(gptr), (unsigned*)(ldsptr), 16, 0, 0)
#define WAIT_V(n) asm volatile("s_waitcnt vmcnt(" #n ")" ::: "memory")
#define BARRIER() asm volatile("s_barrier" ::: "memory")
#define WAIT_L0() asm volatile("s_waitcnt lgkmcnt(0)" ::: "memory")

__device__ __forceinline__ float bf16r(float x) { return __bfloat162float(__float2bfloat16(x)); }
__device__ __forceinline__ unsigned pack2(float lo, float hi) {
  __hip_bfloat16 a = __float2bfloat16(lo), b = __float2bfloat16(hi);
  return (unsigned)__builtin_bit_cast(u16, a) | ((unsigned)__builtin_bit_cast(u16, b) << 16);
}
__device__ __forceinline__ float bflo(unsigned v) { return __builtin_bit_cast(float, v << 16); }
__device__ __forceinline__ float bfhi(unsigned v) { return __builtin_bit_cast(float, v & 0xffff0000u); }

__device__ __forceinline__ float dot2(unsigned a, unsigned b, float c) {
#if defined(__HIP_DEVICE_COMPILE__)
  return __builtin_amdgcn_fdot2_f32_bf16(__builtin_bit_cast(bf2_t, a), __builtin_bit_cast(bf2_t, b), c, false);
#else
  return c;
#endif
}
__device__ __forceinline__ bf16x4 tr_read(const char* p) {
#if defined(__HIP_DEVICE_COMPILE__)
  return __builtin_amdgcn_ds_read_tr16_b64_v4i16((LDS_AS bf16x4*)(p));
#else
  return bf16x4{};
#endif
}
template <int OFF>
__device__ __forceinline__ bf16x8 tr_frag(unsigned addr) {
  bf16x4 a, b;
  asm volatile("ds_read_b64_tr_b16 %0, %2 offset:%3\n\tds_read_b64_tr_b16 %1, %2 offset:%4"
               : "=&v"(a), "=&v"(b)
               : "v"(addr), "i"(OFF), "i"(OFF + 512));
  return __builtin_shufflevector(a, b, 0, 1, 2, 3, 4, 5, 6, 7);
}
template <int OFF>
__device__ __forceinline__ bf16x8 lds_rd128(unsigned addr) {
  bf16x8 a;
  asm volatile("ds_read_b128 %0, %1 offset:%2" : "=&v"(a) : "v"(addr), "i"(OFF));
  return a;
}
__device__ __forceinline__ unsigned lds_addr(const void* p) {
  return (unsigned)(uintptr_t)(const LDS_AS char*)p;
}
__device__ __forceinline__ bf16x8 cat8(bf16x4 a, bf16x4 b) {
  return __builtin_shufflevector(a, b, 0, 1, 2, 3, 4, 5, 6, 7);
}
__device__ __forceinline__ float gelu_tanh(float g) {
  const float c1 = -2.0f * 0.7978845608028654f * 1.4426950408889634f;
  const float c2 = c1 * 0.044715f;
  float w = g * __builtin_fmaf(g * g, c2, c1);
  float e = __builtin_amdgcn_exp2f(w);
  return g * __builtin_amdgcn_rcpf(1.0f + e);
}

__device__ __forceinline__ void touch(const void* p, unsigned& sink) {
  asm volatile("global_load_dword %0, %1, off" : "+v"(sink) : "v"(p) : "memory");
}
#define SCHED0() __builtin_amdgcn_sched_barrier(0)
template <class RD>
__device__ __forceinline__ void mm16(f32x4 (&acc)[4][4], const bf16x8 (&wf)[4], const bf16x8 (&xf)[4], RD rd) {
#pragma unroll
  for (int i = 0; i < 16; ++i) {
    const int ni = i >> 2, mi = i & 3;
    acc[ni][mi] = __builtin_amdgcn_mfma_f32_16x16x32_bf16(wf[ni], xf[mi], acc[ni][mi], 0, 0, 0);
    if (i < 8) {
      SCHED0();
      rd(i);
      SCHED0();
    }
  }
  SCHED0();
}

constexpr int K1_A = 32768, K1_B = 16384, K1_STAGE = K1_A + K1_B;

__global__ __launch_bounds__(768, 3) void k1_kernel(const u16* __restrict__ x, const u16* __restrict__ wd_re,
                                                    const u16* __restrict__ wd_im, const u16* __restrict__ w_gate,
                                                    const u16* __restrict__ w_up, u16* __restrict__ ab,
                                                    u16* __restrict__ h, unsigned* __restrict__ cnt,
                                                    const u16* __restrict__ ws_re, const u16* __restrict__ ws_im,
                                                    const u16* __restrict__ w_down) {
  __shared__ __attribute__((aligned(1024))) char smem[3 * K1_STAGE];
  const int tid = threadIdx.x, wid = __builtin_amdgcn_readfirstlane(tid >> 6), lane = tid & 63;
  const int fr = lane & 15, fq = lane >> 4;
  const int bid = blockIdx.x;
  const int nb = (bid & 7) * 30 + (bid >> 3);
  const int cb = nb >> 3, rb = nb & 7;
  const bool nk = cb < 8;
  const int row0 = rb * 256;
  const int f0 = (cb - 8) * 64;
  if (bid == 0 && tid < 64) cnt[tid * 64] = 0u;
  const int sw = (fr >> 1) & 7;
  const int ch0 = ((fq) ^ sw) * 16, ch1 = ((4 + fq) ^ sw) * 16;
  const int tq = (lane >> 2) & 3, tp = lane & 3;
  const int tswz = ((tq >> 1) & 1) | ((fq & 1) << 1);
  const int tr_row = (8 * fq + tq) * 128 + 8 * tp;
  float* sst = (float*)(smem + 2 * K1_STAGE);
  float* scs = (float*)(smem + 2 * K1_STAGE + 8192);

  if (wid >= 8) {
    const int lw = wid - 8;
    __builtin_amdgcn_s_setprio(2);
    const int l3 = lane >> 3, l7 = lane & 7;
    const int rt_c0 = (l7 ^ (fq & 7)) * 8, rt_c1 = (l7 ^ ((fq + 4) & 7)) * 8;
    const int kt_c0 = (l7 ^ ((fq & 1) << 1)) * 8, kt_c1 = (l7 ^ (((fq & 1) | 2) << 1)) * 8;
    const u16* xa0 = x + (size_t)(row0 + lw * 64 + l3) * D_ + rt_c0;
    const u16* xa1 = x + (size_t)(row0 + lw * 64 + 8 + l3) * D_ + rt_c1;
    const u16 *wb0, *wb1;
    int wstep_j, wstep_t;
    if (nk) {
      const u16* wnk = (cb < 4 ? wd_re : wd_im) + (size_t)((cb & 3) * 128 + lw * 32 + l3) * D_;
      wb0 = wnk + rt_c0;
      wb1 = wnk + 8 * D_ + rt_c1;
      wstep_j = 16 * D_;
      wstep_t = 64;
    } else {
      const u16* w = (lw < 2) ? w_gate : w_up;
      int c0 = f0 + kt_c0, c1 = f0 + kt_c1;
      if (c0 >= F_) c0 -= 32;
      if (c1 >= F_) c1 -= 32;
      const u16* wk = w + (size_t)((lw & 1) * 32 + l3) * F_;
      wb0 = wk + c0;
      wb1 = wk + 8 * F_ + c1;
      wstep_j = 16 * F_;
      wstep_t = 64 * F_;
    }
    auto stage = [&](int tj, int buf) {
      const int t = (tj + K1_ROT * cb) & 7;
      char* sa = smem + buf * K1_STAGE + lw * 8192;
      char* sb = smem + buf * K1_STAGE + K1_A + lw * 4096;
#pragma unroll
      for (int i = 0; i < 4; ++i) {
        GLDS16(xa0 + i * 16 * D_ + t * 64, sa + i * 2048);
        GLDS16(xa1 + i * 16 * D_ + t * 64, sa + i * 2048 + 1024);
      }
#pragma unroll
      for (int i = 0; i < 2; ++i) {
        GLDS16(wb0 + i * wstep_j + t * wstep_t, sb + i * 2048);
        GLDS16(wb1 + i * wstep_j + t * wstep_t, sb + i * 2048 + 1024);
      }
    };
#pragma unroll 1
    for (int t = 0; t < 2; ++t) stage(t, t);
    f32x4 accS[4], accG[4], accC[2];
#pragma unroll
    for (int i = 0; i < 4; ++i) {
      accS[i] = f32x4{0.f, 0.f, 0.f, 0.f};
      accG[i] = f32x4{0.f, 0.f, 0.f, 0.f};
    }
    accC[0] = f32x4{0.f, 0.f, 0.f, 0.f};
    accC[1] = f32x4{0.f, 0.f, 0.f, 0.f};
    const short ONE = 0x3f80;
    const bf16x8 ones = {ONE, ONE, ONE, ONE, ONE, ONE, ONE, ONE};
    const unsigned la0 = lds_addr(smem) + (lw * 64 + fr) * 128 + ch0;
    const unsigned la1 = lds_addr(smem) + (lw * 64 + fr) * 128 + ch1;
    const unsigned lb0 = lds_addr(smem) + K1_A + (lw * 32 + fr) * 128 + ch0;
    const unsigned lb1 = lds_addr(smem) + K1_A + (lw * 32 + fr) * 128 + ch1;
    const unsigned lt0 = lds_addr(smem) + K1_A + (lw >> 1) * 8192 + tr_row + 32 * (((lw & 1) * 2) ^ tswz);
    const unsigned lt1 = lds_addr(smem) + K1_A + (lw >> 1) * 8192 + tr_row + 32 * (((lw & 1) * 2 + 1) ^ tswz);
    int ib = 2, buf = 0;
#pragma unroll 1
    for (int t = 0; t < 8; ++t) {
      if (t < 7) WAIT_V(12); else WAIT_V(0);
      BARRIER();
      const unsigned so = buf * K1_STAGE;
      bf16x8 xf[8], wf[4];
      xf[0] = lds_rd128<0>(la0 + so); xf[1] = lds_rd128<2048>(la0 + so);
      xf[2] = lds_rd128<4096>(la0 + so); xf[3] = lds_rd128<6144>(la0 + so);
      xf[4] = lds_rd128<0>(la1 + so); xf[5] = lds_rd128<2048>(la1 + so);
      xf[6] = lds_rd128<4096>(la1 + so); xf[7] = lds_rd128<6144>(la1 + so);
      if (nk) {
        wf[0] = lds_rd128<0>(lb0 + so); wf[1] = lds_rd128<2048>(lb0 + so);
        wf[2] = lds_rd128<0>(lb1 + so); wf[3] = lds_rd128<2048>(lb1 + so);
      } else {
        wf[0] = tr_frag<0>(lt0 + so); wf[1] = tr_frag<0>(lt1 + so);
        wf[2] = tr_frag<4096>(lt0 + so); wf[3] = tr_frag<4096>(lt1 + so);
      }
      if (t + 2 < 8) stage(t + 2, ib);
      ib = (ib == 2) ? 0 : ib + 1;
      WAIT_L0();
      __builtin_amdgcn_sched_barrier(0);
#pragma unroll
      for (int i = 0; i < 8; ++i) {
        accS[i & 3] = __builtin_amdgcn_mfma_f32_16x16x32_bf16(ones, xf[i], accS[i & 3], 0, 0, 0);
        accG[i & 3] = __builtin_amdgcn_mfma_f32_16x16x32_bf16(xf[i], xf[i], accG[i & 3], 0, 0, 0);
      }
#pragma unroll
      for (int i = 0; i < 4; ++i)
        accC[i & 1] = __builtin_amdgcn_mfma_f32_16x16x32_bf16(wf[i], ones, accC[i & 1], 0, 0, 0);
      buf = (buf == 2) ? 0 : buf + 1;
    }
    if (fq == (fr >> 2)) {
#pragma unroll
      for (int i = 0; i < 4; ++i) {
        const int r = fr & 3;
        const float q = r == 0 ? accG[i][0] : r == 1 ? accG[i][1] : r == 2 ? accG[i][2] : accG[i][3];
        const float mean = accS[i][0] * (1.0f / D_);
        const float var = fmaxf(q * (1.0f / D_) - mean * mean, 0.f);
        const float sd = bf16r(sqrtf(bf16r(bf16r(var) + 1e-5f)));
        f32x2 o = {bf16r(mean), __builtin_amdgcn_rcpf(sd)};
        *(f32x2*)(sst + (lw * 64 + i * 16 + fr) * 2) = o;
      }
    }
    if (fr == 0) {
      *(f32x4*)(scs + lw * 32 + fq * 4) = accC[0];
      *(f32x4*)(scs + lw * 32 + 16 + fq * 4) = accC[1];
    }
    WAIT_L0();
    BARRIER();
    return;
  }

  const int wm = wid >> 1, wn = wid & 1;
  f32x4 acc[4][4];
#pragma unroll
  for (int i = 0; i < 4; ++i)
#pragma unroll
    for (int j = 0; j < 4; ++j) acc[i][j] = f32x4{0.f, 0.f, 0.f, 0.f};
  const unsigned a_b0 = lds_addr(smem) + (wm * 64 + fr) * 128 + ch0, a_b1 = lds_addr(smem) + (wm * 64 + fr) * 128 + ch1;
  const unsigned n_b0 = lds_addr(smem) + K1_A + (wn * 64 + fr) * 128 + ch0;
  const unsigned n_b1 = lds_addr(smem) + K1_A + (wn * 64 + fr) * 128 + ch1;
  const unsigned t_p0 = lds_addr(smem) + K1_A + tr_row + 32 * ((wn * 2) ^ tswz);
  const unsigned t_p1 = lds_addr(smem) + K1_A + tr_row + 32 * ((wn * 2 + 1) ^ tswz);

  unsigned sink = 0;
  auto warm = [&](int t) {
    if ((lane & 7) != t) return;
    const int g = (bid * 8 + wid) * 64 + (lane & 7) * 8 + (lane >> 3);
    touch((g < 65536 ? ws_re + (size_t)g * 64 : ws_im + (size_t)(g - 65536) * 64), sink);
    const int g2 = g + 122880;
    if (g2 < 142080) touch((g2 < 131072 ? ws_im + (size_t)(g2 - 65536) * 64 : w_down + (size_t)(g2 - 131072) * 64), sink);
  };
  auto mainloop = [&](auto NKC) {
    constexpr bool NK = decltype(NKC)::value;
    bf16x8 xf0[4], wf0[4], xf1[4], wf1[4];
    auto rdg = [&](auto KSC, bf16x8(&xf)[4], bf16x8(&wf)[4], unsigned so, int g) {
      constexpr int KS = decltype(KSC)::value;
      const unsigned ab_ = (KS ? a_b1 : a_b0) + so;
      switch (g) {
        case 0: xf[0] = lds_rd128<0>(ab_); break;
        case 2: xf[1] = lds_rd128<2048>(ab_); break;
        case 4: xf[2] = lds_rd128<4096>(ab_); break;
        case 6: xf[3] = lds_rd128<6144>(ab_); break;
        default:
          if constexpr (NK) {
            const unsigned nb_ = (KS ? n_b1 : n_b0) + so;
            if (g == 1) wf[0] = lds_rd128<0>(nb_);
            if (g == 3) wf[1] = lds_rd128<2048>(nb_);
            if (g == 5) wf[2] = lds_rd128<4096>(nb_);
            if (g == 7) wf[3] = lds_rd128<6144>(nb_);
          } else {
            if (g == 1) wf[0] = tr_frag<KS * 4096>(t_p0 + so);
            if (g == 3) wf[1] = tr_frag<KS * 4096>(t_p1 + so);
            if (g == 5) wf[2] = tr_frag<KS * 4096 + 8192>(t_p0 + so);
            if (g == 7) wf[3] = tr_frag<KS * 4096 + 8192>(t_p1 + so);
          }
      }
    };
    using C0 = std::integral_constant<int, 0>;
    using C1 = std::integral_constant<int, 1>;
    BARRIER();
#pragma unroll
    for (int g = 0; g < 8; ++g) rdg(C0{}, xf0, wf0, 0u, g);
    WAIT_L0();
    SCHED0();
    unsigned so = 0;
#pragma unroll 1
    for (int t = 0; t < 7; ++t) {
      if (K1_WARM_T < 8) warm(t);
      mm16(acc, wf0, xf0, [&](int g) { rdg(C1{}, xf1, wf1, so, g); });
      WAIT_L0();
      SCHED0();
      so = (so == 2 * K1_STAGE) ? 0u : so + K1_STAGE;
      BARRIER();
      mm16(acc, wf1, xf1, [&](int g) { rdg(C0{}, xf0, wf0, so, g); });
      WAIT_L0();
      SCHED0();
    }
    if (K1_WARM_T < 8) warm(7);
    mm16(acc, wf0, xf0, [&](int g) { rdg(C1{}, xf1, wf1, so, g); });
    WAIT_L0();
    SCHED0();
    mm16(acc, wf1, xf1, [&](int) {});
  };
  if (nk) mainloop(std::true_type{});
  else mainloop(std::false_type{});
  BARRIER();
  float mb[4], rinv[4];
#pragma unroll
  for (int mi = 0; mi < 4; ++mi) {
    f32x2 v = *(const f32x2*)(sst + (wm * 64 + mi * 16 + fr) * 2);
    mb[mi] = v[0];
    rinv[mi] = v[1];
  }
  f32x4 csv[4];
#pragma unroll
  for (int ni = 0; ni < 4; ++ni) {
    const int ci = nk ? (wn * 64 + ni * 16) : ((ni >> 1) * 64 + wn * 32 + (ni & 1) * 16);
    csv[ni] = *(const f32x4*)(scs + ci + fq * 4);
  }
  if (nk) {
    const float sgn = cb < 4 ? 1.0f : -1.0f;
    u16* dst = ab + (size_t)(cb >> 2) * (S_ * D_) + (size_t)(row0 + wm * 64 + fr) * D_ + (cb & 3) * 128 + wn * 64 + fq * 4;
#pragma unroll
    for (int mi = 0; mi < 4; ++mi) {
      const float r = rinv[mi] * sgn;
#pragma unroll
      for (int ni = 0; ni < 4; ++ni) {
        f32x4 v = (acc[ni][mi] - mb[mi] * csv[ni]) * r;
        u32x2 o = {pack2(v[0], v[1]), pack2(v[2], v[3])};
        *(u32x2*)(dst + mi * 16 * D_ + ni * 16) = o;
      }
    }
  } else {
    const int fc = f0 + wn * 32 + fq * 4;
    u16* dst = h + (size_t)(row0 + wm * 64 + fr) * FP_ + fc;
    const unsigned vmask = (f0 + wn * 32) < F_ ? 0xffffffffu : 0u;
#pragma unroll
    for (int mi = 0; mi < 4; ++mi) {
#pragma unroll
      for (int ni = 0; ni < 2; ++ni) {
        f32x4 g = (acc[ni][mi] - mb[mi] * csv[ni]) * rinv[mi];
        f32x4 u = (acc[ni + 2][mi] - mb[mi] * csv[ni + 2]) * rinv[mi];
        float hv[4];
#pragma unroll
        for (int e = 0; e < 4; ++e) {
          float gl = bf16r(gelu_tanh(bf16r(g[e])));
          hv[e] = gl * bf16r(u[e]);
        }
        u32x2 o = {pack2(hv[0], hv[1]) & vmask, pack2(hv[2], hv[3]) & vmask};
        *(u32x2*)(dst + mi * 16 * FP_ + ni * 16) = o;
      }
    }
  }
  WAIT_V(0);
  asm volatile("" ::"v"(sink));
}

constexpr int K2_STAGE = 32768, K2_NST = 5;
constexpr int CNT_STRIDE = 64;

__global__ __launch_bounds__(512, 2) void k2_kernel(const u16* __restrict__ x, const u16* __restrict__ ws_re,
                                                    const u16* __restrict__ ws_im, const u16* __restrict__ w_down,
                                                    const u16* __restrict__ ab, const u16* __restrict__ h,
                                                    u16* __restrict__ out, float* slab, unsigned* cnt) {
  __shared__ __attribute__((aligned(1024))) char smem[K2_NST * K2_STAGE];
  const int tid = threadIdx.x, wid = __builtin_amdgcn_readfirstlane(tid >> 6), lane = tid & 63;
  const int fr = lane & 15, fq = lane >> 4;
  const int bid = blockIdx.x;
  const int idx = bid >> 3;
  const int q = (bid & 7) >> 1;
  const int rb = (bid & 1) * 8 + (idx >> 2), cbk = idx & 3;
  const int row0 = rb * 128, n0 = cbk * 128, tile = rb * 4 + cbk;
  const int t_begin = q < 2 ? 22 * q : 44 + 21 * (q - 2);
  const int nt = q < 2 ? 22 : 21;

  if (wid >= 4) {
    const int lw = wid - 4;
    const int l3 = lane >> 3, l7 = lane & 7;
    const int rt_c0 = (l7 ^ (fq & 7)) * 8, rt_c1 = (l7 ^ ((fq + 4) & 7)) * 8;
    const int kt_c0 = (l7 ^ ((fq & 1) << 1)) * 8, kt_c1 = (l7 ^ (((fq & 1) | 2) << 1)) * 8;
    const int sub = lw & 1;
    auto stage = [&](int j, int slot) {
      const int t = t_begin + j;
      char* dst = smem + slot * K2_STAGE + lw * 8192;
      if (lw < 2) {
        const u16* A;
        int lda, k0;
        if (t < 64) { A = (t < 32 ? ws_re : ws_im); lda = S_; k0 = (t & 31) * 64; }
        else { A = h; lda = FP_; k0 = (t - 64) * 64; }
        A += (size_t)(row0 + sub * 64) * lda;
#pragma unroll
        for (int i = 0; i < 8; ++i) {
          const u16* src = A + (i * 8 + l3) * lda + k0 + ((i & 1) ? rt_c1 : rt_c0);
          GLDS16(src, dst + i * 1024);
        }
      } else {
        const u16* B;
        int k0, kmax;
        if (t < 64) { B = ab + (size_t)(t >> 5) * (S_ * D_); k0 = (t & 31) * 64; kmax = S_ - 1; }
        else { B = w_down; k0 = (t - 64) * 64; kmax = F_ - 1; }
#pragma unroll
        for (int i = 0; i < 8; ++i) {
          const int krow = min(k0 + i * 8 + l3, kmax);
          const u16* src = B + krow * D_ + n0 + sub * 64 + ((i & 1) ? kt_c1 : kt_c0);
          GLDS16(src, dst + i * 1024);
        }
      }
    };
#pragma unroll 1
    for (int s = 0; s < K2_NST - 1; ++s) stage(s, s);
    int islot = K2_NST - 1;
#pragma unroll 1
    for (int j = 0; j < nt; ++j) {
      const int rem = nt - 1 - j;
      if (rem >= 3) WAIT_V(24);
      else if (rem == 2) WAIT_V(16);
      else if (rem == 1) WAIT_V(8);
      else WAIT_V(0);
      BARRIER();
      if (j + K2_NST - 1 < nt) stage(j + K2_NST - 1, islot);
      islot = (islot == K2_NST - 1) ? 0 : islot + 1;
    }
    return;
  }
  const int wm = wid >> 1, wn = wid & 1;
  f32x4 acc[4][4];
#pragma unroll
  for (int i = 0; i < 4; ++i)
#pragma unroll
    for (int j = 0; j < 4; ++j) acc[i][j] = f32x4{0.f, 0.f, 0.f, 0.f};

  const int sw = (fr >> 1) & 7;
  const unsigned a_base0 = lds_addr(smem) + (wm * 64 + fr) * 128 + ((fq) ^ sw) * 16;
  const unsigned a_base1 = lds_addr(smem) + (wm * 64 + fr) * 128 + ((4 + fq) ^ sw) * 16;
  const int tq = (lane >> 2) & 3, tp = lane & 3;
  const int tswz = ((tq >> 1) & 1) | ((fq & 1) << 1);
  const unsigned b_base = lds_addr(smem) + 16384 + wn * 8192 + (8 * fq + tq) * 128 + 8 * tp;
  unsigned b_off[4];
#pragma unroll
  for (int ni = 0; ni < 4; ++ni) b_off[ni] = b_base + 32 * (ni ^ tswz);

  bf16x8 af0[4], bf0[4], af1[4], bf1[4];
  auto rdg = [&](auto KSC, bf16x8(&af)[4], bf16x8(&bfr)[4], unsigned so, int g) {
    constexpr int KS = decltype(KSC)::value;
    const unsigned ab_ = (KS ? a_base1 : a_base0) + so;
    switch (g) {
      case 0: af[0] = lds_rd128<0>(ab_); break;
      case 2: af[1] = lds_rd128<2048>(ab_); break;
      case 4: af[2] = lds_rd128<4096>(ab_); break;
      case 6: af[3] = lds_rd128<6144>(ab_); break;
      case 1: bfr[0] = tr_frag<KS * 4096>(b_off[0] + so); break;
      case 3: bfr[1] = tr_frag<KS * 4096>(b_off[1] + so); break;
      case 5: bfr[2] = tr_frag<KS * 4096>(b_off[2] + so); break;
      default: bfr[3] = tr_frag<KS * 4096>(b_off[3] + so); break;
    }
  };
  using C0 = std::integral_constant<int, 0>;
  using C1 = std::integral_constant<int, 1>;
  BARRIER();
#pragma unroll
  for (int g = 0; g < 8; ++g) rdg(C0{}, af0, bf0, 0u, g);
  WAIT_L0();
  SCHED0();
  unsigned so = 0;
#pragma unroll 1
  for (int j = 0; j < nt - 1; ++j) {
    mm16(acc, bf0, af0, [&](int g) { rdg(C1{}, af1, bf1, so, g); });
    WAIT_L0();
    SCHED0();
    so = (so == (K2_NST - 1) * K2_STAGE) ? 0u : so + K2_STAGE;
    BARRIER();
    mm16(acc, bf1, af1, [&](int g) { rdg(C0{}, af0, bf0, so, g); });
    WAIT_L0();
    SCHED0();
  }
  mm16(acc, bf0, af0, [&](int g) { rdg(C1{}, af1, bf1, so, g); });
  WAIT_L0();
  SCHED0();
  mm16(acc, bf1, af1, [&](int) {});
  const __amdgpu_buffer_rsrc_t rs = __builtin_amdgcn_make_buffer_rsrc(slab, 0, 64 * 128 * 1024, 0x00020000);
  f32x4 own[4];
#pragma unroll
  for (int mi = 0; mi < 4; ++mi) {
    if (mi == q) {
#pragma unroll
      for (int ni = 0; ni < 4; ++ni) own[ni] = acc[ni][mi];
    } else {
      const unsigned base = (unsigned)((((tile * 4 + mi) * 4 + q) * 4 + wid) * 2) * 1024u + lane * 16u;
#pragma unroll
      for (int np = 0; np < 2; ++np) {
        const f32x4 a0 = acc[2 * np][mi], a1 = acc[2 * np + 1][mi];
        u32x4 v = {pack2(a0[0], a0[1]), pack2(a0[2], a0[3]), pack2(a1[0], a1[1]), pack2(a1[2], a1[3])};
        __builtin_amdgcn_raw_buffer_store_b128(v, rs, base + np * 1024u, 0, 16);
      }
    }
  }
  const size_t orow = (size_t)(row0 + wm * 64 + q * 16 + fr) * D_ + n0 + wn * 64 + fq * 4;
  u32x2 xv[4];
#pragma unroll
  for (int ni = 0; ni < 4; ++ni) xv[ni] = *(const u32x2*)(x + orow + ni * 16);
  WAIT_V(0);
  BARRIER();
  if (wid == 0) {
    unsigned* c = cnt + tile * CNT_STRIDE;
    if (lane == 0) __hip_atomic_fetch_add(c, 1u, __ATOMIC_RELAXED, __HIP_MEMORY_SCOPE_AGENT);
#pragma unroll 1
    for (unsigned spins = 0; spins < (1u << 22); ++spins) {
      const unsigned v = __builtin_amdgcn_readfirstlane(__hip_atomic_load(c, __ATOMIC_RELAXED, __HIP_MEMORY_SCOPE_AGENT));
      if (v >= 4u) break;
      __builtin_amdgcn_s_sleep(1);
    }
  }
  BARRIER();
  u32x4 pv[3][2];
#pragma unroll
  for (int sidx = 0; sidx < 3; ++sidx) {
    const int src = sidx + (sidx >= q ? 1 : 0);
    const unsigned base = (unsigned)((((tile * 4 + q) * 4 + src) * 4 + wid) * 2) * 1024u + lane * 16u;
#pragma unroll
    for (int np = 0; np < 2; ++np) pv[sidx][np] = __builtin_amdgcn_raw_buffer_load_b128(rs, base + np * 1024u, 0, 16);
  }
#pragma unroll
  for (int ni = 0; ni < 4; ++ni) {
    f32x4 v = own[ni];
#pragma unroll
    for (int sidx = 0; sidx < 3; ++sidx) {
      const unsigned w0 = pv[sidx][ni >> 1][(ni & 1) * 2], w1 = pv[sidx][ni >> 1][(ni & 1) * 2 + 1];
      v[0] += bflo(w0); v[1] += bfhi(w0); v[2] += bflo(w1); v[3] += bfhi(w1);
    }
    u32x2 o = {pack2(v[0] + bflo(xv[ni][0]), v[1] + bfhi(xv[ni][0])), pack2(v[2] + bflo(xv[ni][1]), v[3] + bfhi(xv[ni][1]))};
    *(u32x2*)(out + orow + ni * 16) = o;
  }
}

extern "C" void kernel_launch(void* const* d_in, const int* in_sizes, int n_in, void* d_out, int out_size,
                              void* d_ws, size_t ws_size, hipStream_t stream) {
  const u16* x = (const u16*)d_in[0];
  const u16* ws_re = (const u16*)d_in[1];
  const u16* ws_im = (const u16*)d_in[2];
  const u16* wd_re = (const u16*)d_in[3];
  const u16* wd_im = (const u16*)d_in[4];
  const u16* w_gate = (const u16*)d_in[6];
  const u16* w_up = (const u16*)d_in[7];
  const u16* w_down = (const u16*)d_in[8];
  u16* ab = (u16*)d_ws;
  u16* h = ab + (size_t)2 * S_ * D_;
  float* slab = (float*)((char*)d_ws + (16u << 20));
  unsigned* cnt = (unsigned*)((char*)d_ws + (32u << 20));
  k1_kernel<<<240, 768, 0, stream>>>(x, wd_re, wd_im, w_gate, w_up, ab, h, cnt, ws_re, ws_im, w_down);
  k2_kernel<<<256, 512, 0, stream>>>(x, ws_re, ws_im, w_down, ab, h, (u16*)d_out, slab, cnt);
}
```

```cpp
#include <hip/hip_runtime.h>
#include <hip/hip_bf16.h>
#include <stdint.h>
#include <type_traits>

typedef unsigned short u16;
typedef unsigned long long u64;
typedef short bf16x8 __attribute__((ext_vector_type(8)));
typedef short bf16x4 __attribute__((ext_vector_type(4)));
typedef float f32x4 __attribute__((ext_vector_type(4)));
typedef float f32x2 __attribute__((ext_vector_type(2)));
typedef unsigned u32x4 __attribute__((ext_vector_type(4)));
typedef unsigned u32x2 __attribute__((ext_vector_type(2)));

constexpr int S_ = 2048, D_ = 512, F_ = 1376, FP_ = 1408;

#define LDS_AS __attribute__((address_space(3)))
#define GLDS16(gptr, ldsptr, aux) \
  __builtin_amdgcn_global_load_lds((const unsigned*)(gptr), (unsigned*)(ldsptr), 16, 0, aux)
#define WAIT_V(n) asm volatile("s_waitcnt vmcnt(" #n ")" ::: "memory")
#define BARRIER() asm volatile("s_barrier" ::: "memory")
#define WAIT_L0() asm volatile("s_waitcnt lgkmcnt(0)" ::: "memory")
#define SCHED0() __builtin_amdgcn_sched_barrier(0)
#define RLX_AGENT __ATOMIC_RELAXED, __HIP_MEMORY_SCOPE_AGENT

__device__ __forceinline__ float bf16r(float x) { return __bfloat162float(__float2bfloat16(x)); }
__device__ __forceinline__ unsigned pack2(float lo, float hi) {
  __hip_bfloat16 a = __float2bfloat16(lo), b = __float2bfloat16(hi);
  return (unsigned)__builtin_bit_cast(u16, a) | ((unsigned)__builtin_bit_cast(u16, b) << 16);
}
__device__ __forceinline__ float bflo(unsigned v) { return __builtin_bit_cast(float, v << 16); }
__device__ __forceinline__ float bfhi(unsigned v) { return __builtin_bit_cast(float, v & 0xffff0000u); }

template <int OFF>
__device__ __forceinline__ bf16x8 tr_frag(unsigned addr) {
  bf16x4 a, b;
  asm volatile("ds_read_b64_tr_b16 %0, %2 offset:%3\n\tds_read_b64_tr_b16 %1, %2 offset:%4"
               : "=&v"(a), "=&v"(b)
               : "v"(addr), "i"(OFF), "i"(OFF + 512));
  return __builtin_shufflevector(a, b, 0, 1, 2, 3, 4, 5, 6, 7);
}
template <int OFF>
__device__ __forceinline__ bf16x8 lds_rd128(unsigned addr) {
  bf16x8 a;
  asm volatile("ds_read_b128 %0, %1 offset:%2" : "=&v"(a) : "v"(addr), "i"(OFF));
  return a;
}
__device__ __forceinline__ unsigned lds_addr(const void* p) { return (unsigned)(uintptr_t)(const LDS_AS char*)p; }
__device__ __forceinline__ float gelu_tanh(float g) {
  const float c1 = -2.0f * 0.7978845608028654f * 1.4426950408889634f;
  const float c2 = c1 * 0.044715f;
  float w = g * __builtin_fmaf(g * g, c2, c1);
  float e = __builtin_amdgcn_exp2f(w);
  return g * __builtin_amdgcn_rcpf(1.0f + e);
}
template <class RD>
__device__ __forceinline__ void mm16(f32x4 (&acc)[4][4], const bf16x8 (&wf)[4], const bf16x8 (&xf)[4], RD rd) {
#pragma unroll
  for (int i = 0; i < 16; ++i) {
    const int ni = i >> 2, mi = i & 3;
    acc[ni][mi] = __builtin_amdgcn_mfma_f32_16x16x32_bf16(wf[ni], xf[mi], acc[ni][mi], 0, 0, 0);
    if (i < 8) {
      SCHED0();
      rd(i);
      SCHED0();
    }
  }
  SCHED0();
}

constexpr size_t WS_AB = 0;
constexpr size_t WS_H = 4u << 20;
constexpr size_t WS_SLAB = 16u << 20;
constexpr size_t WS_SYNC = 32u << 20;
constexpr int FL_ARRIVE = 0, FL_READY1 = 256, FL_READY2 = 512;
__device__ __forceinline__ u64* flag_ptr(char* ws, int idx) { return (u64*)(ws + WS_SYNC + 4096 + (size_t)idx * 64); }
__device__ __forceinline__ u64 flag_tag(u64 nonce, int idx) { return nonce + (u64)(idx + 1) * 0x9E3779B97F4A7C15ull; }
constexpr unsigned SPIN_CAP = 1u << 22;

constexpr int K1_A = 32768, K1_B = 16384, K1_STAGE = K1_A + K1_B;
constexpr int K2_STAGE = 32768, K2_NST = 5;
constexpr int L_SCR = 131072;
constexpr int L_SST = 147456, L_SCS = L_SST + 2048, L_NONCE = L_SCS + 512;

__global__ __launch_bounds__(768, 3) void fused_kernel(const u16* __restrict__ x, const u16* __restrict__ ws_re,
                                                       const u16* __restrict__ ws_im, const u16* __restrict__ wd_re,
                                                       const u16* __restrict__ wd_im, const u16* __restrict__ w_gate,
                                                       const u16* __restrict__ w_up, const u16* __restrict__ w_down,
                                                       u16* __restrict__ out, char* ws) {
  __shared__ __attribute__((aligned(1024))) char smem[K2_NST * K2_STAGE];
  const int tid = threadIdx.x, wid = __builtin_amdgcn_readfirstlane(tid >> 6), lane = tid & 63;
  const int fr = lane & 15, fq = lane >> 4;
  const int bid = blockIdx.x;
  u16* ab = (u16*)(ws + WS_AB);
  u16* h = (u16*)(ws + WS_H);

  const bool has_a = bid < 240;
  const int nb = (bid & 7) * 30 + (bid >> 3);
  const int cb = nb >> 3, rbA = nb & 7;
  const bool nk = cb < 8;
  const int rowA = rbA * 256;
  const int f0 = (cb - 8) * 64;
  const int idx = bid >> 3;
  const int q = (bid & 7) >> 1;
  const int rbB = (bid & 1) * 8 + (idx >> 2), cbk = idx & 3;
  const int rowB = rbB * 128, n0 = cbk * 128, tile = rbB * 4 + cbk;
  const int t_begin = q < 2 ? 22 * q : 44 + 21 * (q - 2);
  const int nt = q < 2 ? 22 : 21;

  const int sw = (fr >> 1) & 7;
  const int ch0 = ((fq) ^ sw) * 16, ch1 = ((4 + fq) ^ sw) * 16;
  const int tq = (lane >> 2) & 3, tp = lane & 3;
  const int tswz = ((tq >> 1) & 1) | ((fq & 1) << 1);
  const int tr_row = (8 * fq + tq) * 128 + 8 * tp;
  float* sst = (float*)(smem + L_SST);
  float* scs = (float*)(smem + L_SCS);
  u64* snonce = (u64*)(smem + L_NONCE);
  const unsigned sbase = lds_addr(smem);

  if (wid >= 8) {
    const int lw = wid - 8;
    const int l3 = lane >> 3, l7 = lane & 7;
    const int rt_c0 = (l7 ^ (fq & 7)) * 8, rt_c1 = (l7 ^ ((fq + 4) & 7)) * 8;
    const int kt_c0 = (l7 ^ ((fq & 1) << 1)) * 8, kt_c1 = (l7 ^ (((fq & 1) | 2) << 1)) * 8;
    if (has_a) {
      __builtin_amdgcn_s_setprio(2);
      const u16* xa0 = x + (size_t)(rowA + lw * 64 + l3) * D_ + rt_c0;
      const u16* xa1 = x + (size_t)(rowA + lw * 64 + 8 + l3) * D_ + rt_c1;
      const u16 *wb0, *wb1;
      int wstep_j, wstep_t;
      if (nk) {
        const u16* wnk = (cb < 4 ? wd_re : wd_im) + (size_t)((cb & 3) * 128 + lw * 32 + l3) * D_;
        wb0 = wnk + rt_c0;
        wb1 = wnk + 8 * D_ + rt_c1;
        wstep_j = 16 * D_;
        wstep_t = 64;
      } else {
        const u16* w = (lw < 2) ? w_gate : w_up;
        int c0 = f0 + kt_c0, c1 = f0 + kt_c1;
        if (c0 >= F_) c0 -= 32;
        if (c1 >= F_) c1 -= 32;
        const u16* wk = w + (size_t)((lw & 1) * 32 + l3) * F_;
        wb0 = wk + c0;
        wb1 = wk + 8 * F_ + c1;
        wstep_j = 16 * F_;
        wstep_t = 64 * F_;
      }
      auto stage = [&](int tj, int buf) {
        const int t = (tj + cb) & 7;
        char* sa = smem + buf * K1_STAGE + lw * 8192;
        char* sb = smem + buf * K1_STAGE + K1_A + lw * 4096;
#pragma unroll
        for (int i = 0; i < 4; ++i) {
          GLDS16(xa0 + i * 16 * D_ + t * 64, sa + i * 2048, 0);
          GLDS16(xa1 + i * 16 * D_ + t * 64, sa + i * 2048 + 1024, 0);
        }
#pragma unroll
        for (int i = 0; i < 2; ++i) {
          GLDS16(wb0 + i * wstep_j + t * wstep_t, sb + i * 2048, 0);
          GLDS16(wb1 + i * wstep_j + t * wstep_t, sb + i * 2048 + 1024, 0);
        }
      };
#pragma unroll 1
      for (int t = 0; t < 2; ++t) stage(t, t);
      f32x4 accS[4], accG[4], accC[2];
#pragma unroll
      for (int i = 0; i < 4; ++i) {
        accS[i] = f32x4{0.f, 0.f, 0.f, 0.f};
        accG[i] = f32x4{0.f, 0.f, 0.f, 0.f};
      }
      accC[0] = f32x4{0.f, 0.f, 0.f, 0.f};
      accC[1] = f32x4{0.f, 0.f, 0.f, 0.f};
      const short ONE = 0x3f80;
      const bf16x8 ones = {ONE, ONE, ONE, ONE, ONE, ONE, ONE, ONE};
      const unsigned la0 = sbase + (lw * 64 + fr) * 128 + ch0;
      const unsigned la1 = sbase + (lw * 64 + fr) * 128 + ch1;
      const unsigned lb0 = sbase + K1_A + (lw * 32 + fr) * 128 + ch0;
      const unsigned lb1 = sbase + K1_A + (lw * 32 + fr) * 128 + ch1;
      const unsigned lt0 = sbase + K1_A + (lw >> 1) * 8192 + tr_row + 32 * (((lw & 1) * 2) ^ tswz);
      const unsigned lt1 = sbase + K1_A + (lw >> 1) * 8192 + tr_row + 32 * (((lw & 1) * 2 + 1) ^ tswz);
      int ib = 2, buf = 0;
#pragma unroll 1
      for (int t = 0; t < 8; ++t) {
        if (t < 7) WAIT_V(12); else WAIT_V(0);
        BARRIER();
        const unsigned so = buf * K1_STAGE;
        bf16x8 xf[8], wf[4];
        xf[0] = lds_rd128<0>(la0 + so); xf[1] = lds_rd128<2048>(la0 + so);
        xf[2] = lds_rd128<4096>(la0 + so); xf[3] = lds_rd128<6144>(la0 + so);
        xf[4] = lds_rd128<0>(la1 + so); xf[5] = lds_rd128<2048>(la1 + so);
        xf[6] = lds_rd128<4096>(la1 + so); xf[7] = lds_rd128<6144>(la1 + so);
        if (nk) {
          wf[0] = lds_rd128<0>(lb0 + so); wf[1] = lds_rd128<2048>(lb0 + so);
          wf[2] = lds_rd128<0>(lb1 + so); wf[3] = lds_rd128<2048>(lb1 + so);
        } else {
          wf[0] = tr_frag<0>(lt0 + so); wf[1] = tr_frag<0>(lt1 + so);
          wf[2] = tr_frag<4096>(lt0 + so); wf[3] = tr_frag<4096>(lt1 + so);
        }
        if (t + 2 < 8) stage(t + 2, ib);
        ib = (ib == 2) ? 0 : ib + 1;
        WAIT_L0();
        SCHED0();
#pragma unroll
        for (int i = 0; i < 8; ++i) {
          accS[i & 3] = __builtin_amdgcn_mfma_f32_16x16x32_bf16(ones, xf[i], accS[i & 3], 0, 0, 0);
          accG[i & 3] = __builtin_amdgcn_mfma_f32_16x16x32_bf16(xf[i], xf[i], accG[i & 3], 0, 0, 0);
        }
#pragma unroll
        for (int i = 0; i < 4; ++i)
          accC[i & 1] = __builtin_amdgcn_mfma_f32_16x16x32_bf16(wf[i], ones, accC[i & 1], 0, 0, 0);
        buf = (buf == 2) ? 0 : buf + 1;
      }
      if (fq == (fr >> 2)) {
#pragma unroll
        for (int i = 0; i < 4; ++i) {
          const int r = fr & 3;
          const float qq = r == 0 ? accG[i][0] : r == 1 ? accG[i][1] : r == 2 ? accG[i][2] : accG[i][3];
          const float mean = accS[i][0] * (1.0f / D_);
          const float var = fmaxf(qq * (1.0f / D_) - mean * mean, 0.f);
          const float sd = bf16r(sqrtf(bf16r(bf16r(var) + 1e-5f)));
          f32x2 o = {bf16r(mean), __builtin_amdgcn_rcpf(sd)};
          *(f32x2*)(sst + (lw * 64 + i * 16 + fr) * 2) = o;
        }
      }
      if (fr == 0) {
        *(f32x4*)(scs + lw * 32 + fq * 4) = accC[0];
        *(f32x4*)(scs + lw * 32 + 16 + fq * 4) = accC[1];
      }
      WAIT_L0();
      BARRIER();
      __builtin_amdgcn_s_setprio(0);
    }
    const int sub = lw & 1;
    auto stageB = [&](int j, int slot) {
      const int t = t_begin + j;
      char* dst = smem + slot * K2_STAGE + lw * 8192;
      if (lw < 2) {
        if (t < 64) {
          const u16* A = (t < 32 ? ws_re : ws_im) + (size_t)(rowB + sub * 64 + l3) * S_ + (t & 31) * 64;
#pragma unroll
          for (int i = 0; i < 8; ++i) GLDS16(A + i * 8 * S_ + ((i & 1) ? rt_c1 : rt_c0), dst + i * 1024, 0);
        } else {
          const u16* A = h + (size_t)(rowB + sub * 64 + l3) * FP_ + (t - 64) * 64;
#pragma unroll
          for (int i = 0; i < 8; ++i) GLDS16(A + i * 8 * FP_ + ((i & 1) ? rt_c1 : rt_c0), dst + i * 1024, 16);
        }
      } else {
        if (t < 64) {
          const u16* B = ab + (size_t)(t * 64 + l3) * D_ + n0 + sub * 64;
#pragma unroll
          for (int i = 0; i < 8; ++i) GLDS16(B + i * 8 * D_ + ((i & 1) ? kt_c1 : kt_c0), dst + i * 1024, 16);
        } else {
          const int k0 = (t - 64) * 64;
#pragma unroll
          for (int i = 0; i < 8; ++i) {
            const int krow = min(k0 + i * 8 + l3, F_ - 1);
            GLDS16(w_down + krow * D_ + n0 + sub * 64 + ((i & 1) ? kt_c1 : kt_c0), dst + i * 1024, 0);
          }
        }
      }
    };
    const bool early = (lw < 2) ? (t_begin + K2_NST - 2 < 64) : (t_begin >= 64);
    if (early) {
#pragma unroll 1
      for (int s = 0; s < K2_NST - 1; ++s) stageB(s, s);
    }
    BARRIER();
    BARRIER();
    if (!early) {
#pragma unroll 1
      for (int s = 0; s < K2_NST - 1; ++s) stageB(s, s);
    }
    int islot = K2_NST - 1;
#pragma unroll 1
    for (int j = 0; j < nt; ++j) {
      const int rem = nt - 1 - j;
      if (rem >= 3) WAIT_V(24);
      else if (rem == 2) WAIT_V(16);
      else if (rem == 1) WAIT_V(8);
      else WAIT_V(0);
      BARRIER();
      if (j + K2_NST - 1 < nt) stageB(j + K2_NST - 1, islot);
      islot = (islot == K2_NST - 1) ? 0 : islot + 1;
    }
    return;
  }

  u64 nonce = 0;
  if (wid == 4) {
    nonce = __hip_atomic_load((u64*)(ws + WS_SYNC), RLX_AGENT);
    nonce = ((u64)__builtin_amdgcn_readfirstlane((unsigned)(nonce >> 32)) << 32) |
            (u64)__builtin_amdgcn_readfirstlane((unsigned)nonce);
    if (lane == 0) {
      *snonce = nonce;
      __hip_atomic_store(flag_ptr(ws, FL_ARRIVE + bid), flag_tag(nonce, FL_ARRIVE + bid), RLX_AGENT);
    }
  }
  const int wm = wid >> 1, wn = wid & 1;
  using C0 = std::integral_constant<int, 0>;
  using C1 = std::integral_constant<int, 1>;

  if (has_a) {
    f32x4 acc[4][4];
#pragma unroll
    for (int i = 0; i < 4; ++i)
#pragma unroll
      for (int j = 0; j < 4; ++j) acc[i][j] = f32x4{0.f, 0.f, 0.f, 0.f};
    const unsigned a_b0 = sbase + (wm * 64 + fr) * 128 + ch0, a_b1 = sbase + (wm * 64 + fr) * 128 + ch1;
    const unsigned n_b0 = sbase + K1_A + (wn * 64 + fr) * 128 + ch0;
    const unsigned n_b1 = sbase + K1_A + (wn * 64 + fr) * 128 + ch1;
    const unsigned t_p0 = sbase + K1_A + tr_row + 32 * ((wn * 2) ^ tswz);
    const unsigned t_p1 = sbase + K1_A + tr_row + 32 * ((wn * 2 + 1) ^ tswz);

    auto mainloop = [&](auto NKC) {
      constexpr bool NK = decltype(NKC)::value;
      bf16x8 xf0[4], wf0[4], xf1[4], wf1[4];
      auto rdg = [&](auto KSC, bf16x8(&xf)[4], bf16x8(&wf)[4], unsigned so, int g) {
        constexpr int KS = decltype(KSC)::value;
        const unsigned ab_ = (KS ? a_b1 : a_b0) + so;
        switch (g) {
          case 0: xf[0] = lds_rd128<0>(ab_); break;
          case 2: xf[1] = lds_rd128<2048>(ab_); break;
          case 4: xf[2] = lds_rd128<4096>(ab_); break;
          case 6: xf[3] = lds_rd128<6144>(ab_); break;
          default:
            if constexpr (NK) {
              const unsigned nb_ = (KS ? n_b1 : n_b0) + so;
              if (g == 1) wf[0] = lds_rd128<0>(nb_);
              if (g == 3) wf[1] = lds_rd128<2048>(nb_);
              if (g == 5) wf[2] = lds_rd128<4096>(nb_);
              if (g == 7) wf[3] = lds_rd128<6144>(nb_);
            } else {
              if (g == 1) wf[0] = tr_frag<KS * 4096>(t_p0 + so);
              if (g == 3) wf[1] = tr_frag<KS * 4096>(t_p1 + so);
              if (g == 5) wf[2] = tr_frag<KS * 4096 + 8192>(t_p0 + so);
              if (g == 7) wf[3] = tr_frag<KS * 4096 + 8192>(t_p1 + so);
            }
        }
      };
      BARRIER();
#pragma unroll
      for (int g = 0; g < 8; ++g) rdg(C0{}, xf0, wf0, 0u, g);
      WAIT_L0();
      SCHED0();
      unsigned so = 0;
#pragma unroll 1
      for (int t = 0; t < 7; ++t) {
        mm16(acc, wf0, xf0, [&](int g) { rdg(C1{}, xf1, wf1, so, g); });
        WAIT_L0();
        SCHED0();
        so = (so == 2 * K1_STAGE) ? 0u : so + K1_STAGE;
        BARRIER();
        mm16(acc, wf1, xf1, [&](int g) { rdg(C0{}, xf0, wf0, so, g); });
        WAIT_L0();
        SCHED0();
      }
      mm16(acc, wf0, xf0, [&](int g) { rdg(C1{}, xf1, wf1, so, g); });
      WAIT_L0();
      SCHED0();
      mm16(acc, wf1, xf1, [&](int) {});
    };
    if (nk) mainloop(std::true_type{});
    else mainloop(std::false_type{});
    BARRIER();
    float mb[4], rinv[4];
#pragma unroll
    for (int mi = 0; mi < 4; ++mi) {
      f32x2 v = *(const f32x2*)(sst + (wm * 64 + mi * 16 + fr) * 2);
      mb[mi] = v[0];
      rinv[mi] = v[1];
    }
    f32x4 csv[4];
#pragma unroll
    for (int ni = 0; ni < 4; ++ni) {
      const int ci = nk ? (wn * 64 + ni * 16) : ((ni >> 1) * 64 + wn * 32 + (ni & 1) * 16);
      csv[ni] = *(const f32x4*)(scs + ci + fq * 4);
    }
    char* scr = smem + L_SCR + wid * 2048;
    const __amdgpu_buffer_rsrc_t rsw = __builtin_amdgcn_make_buffer_rsrc(ws, 0, 16u << 20, 0x00020000);
    if (nk) {
      const float sgn = cb < 4 ? 1.0f : -1.0f;
      const unsigned gbase = (unsigned)WS_AB +
          (unsigned)(((cb >> 2) * S_ + rowA + wm * 64 + (lane >> 3)) * D_ + (cb & 3) * 128 + wn * 64 + (lane & 7) * 8) * 2u;
#pragma unroll
      for (int mi = 0; mi < 4; ++mi) {
        const float r = rinv[mi] * sgn;
#pragma unroll
        for (int ni = 0; ni < 4; ++ni) {
          f32x4 v = (acc[ni][mi] - mb[mi] * csv[ni]) * r;
          u32x2 o = {pack2(v[0], v[1]), pack2(v[2], v[3])};
          *(u32x2*)(scr + fr * 128 + (((ni * 2 + (fq >> 1)) ^ (fr & 7)) * 16) + (fq & 1) * 8) = o;
        }
#pragma unroll
        for (int hf = 0; hf < 2; ++hf) {
          const int r2 = hf * 8 + (lane >> 3);
          u32x4 v = *(const u32x4*)(scr + r2 * 128 + (((lane & 7) ^ (r2 & 7)) * 16));
          __builtin_amdgcn_raw_buffer_store_b128(v, rsw, gbase + (unsigned)((mi * 16 + hf * 8) * D_ * 2), 0, 16);
        }
      }
    } else {
      const unsigned vmask = (f0 + wn * 32) < F_ ? 0xffffffffu : 0u;
      const unsigned gbase = (unsigned)WS_H +
          (unsigned)((rowA + wm * 64 + (lane >> 2)) * FP_ + f0 + wn * 32 + (lane & 3) * 8) * 2u;
#pragma unroll
      for (int mi = 0; mi < 4; ++mi) {
#pragma unroll
        for (int ni = 0; ni < 2; ++ni) {
          f32x4 g = (acc[ni][mi] - mb[mi] * csv[ni]) * rinv[mi];
          f32x4 u = (acc[ni + 2][mi] - mb[mi] * csv[ni + 2]) * rinv[mi];
          float hv[4];
#pragma unroll
          for (int e = 0; e < 4; ++e) {
            float gl = bf16r(gelu_tanh(bf16r(g[e])));
            hv[e] = gl * bf16r(u[e]);
          }
          u32x2 o = {pack2(hv[0], hv[1]) & vmask, pack2(hv[2], hv[3]) & vmask};
          *(u32x2*)(scr + fr * 64 + (((ni * 2 + (fq >> 1)) ^ ((fr >> 1) & 3)) * 16) + (fq & 1) * 8) = o;
        }
        const int r2 = lane >> 2;
        u32x4 v = *(const u32x4*)(scr + r2 * 64 + (((lane & 3) ^ ((r2 >> 1) & 3)) * 16));
        __builtin_amdgcn_raw_buffer_store_b128(v, rsw, gbase + (unsigned)(mi * 16 * FP_ * 2), 0, 16);
      }
    }
    WAIT_V(0);
  }
  WAIT_L0();
  BARRIER();
  if (wid > 4) return;

  if (wid == 4) {
    int last = -1;
    auto wait_dep = [&](int j) {
      const int t = t_begin + j;
      const int fi = FL_READY1 + (t < 64 ? (((t >> 5) * 4 + cbk) * 8 + ((t & 31) >> 2)) : ((8 + t - 64) * 8 + (rbB >> 1)));
      if (fi == last) return;
      last = fi;
      const u64 want = flag_tag(nonce, fi);
      u64* p = flag_ptr(ws, fi);
#pragma unroll 1
      for (unsigned spins = 0; spins < SPIN_CAP; ++spins) {
        const u64 v = __hip_atomic_load(p, RLX_AGENT);
        const bool ok = __builtin_amdgcn_readfirstlane((unsigned)(v == want)) != 0;
        if (ok) break;
        __builtin_amdgcn_s_sleep(1);
      }
    };
#pragma unroll 1
    for (int j = 0; j < K2_NST - 1; ++j) wait_dep(j);
    BARRIER();
#pragma unroll 1
    for (int j = 0; j < nt; ++j) {
      if (j + K2_NST - 1 < nt) wait_dep(j + K2_NST - 1);
      BARRIER();
    }
    return;
  }

  if (wid == 0) {
    nonce = *snonce;
    nonce = ((u64)__builtin_amdgcn_readfirstlane((unsigned)(nonce >> 32)) << 32) |
            (u64)__builtin_amdgcn_readfirstlane((unsigned)nonce);
    if (has_a && lane == 0) __hip_atomic_store(flag_ptr(ws, FL_READY1 + nb), flag_tag(nonce, FL_READY1 + nb), RLX_AGENT);
  }
  BARRIER();
  {
    f32x4 acc[4][4];
#pragma unroll
    for (int i = 0; i < 4; ++i)
#pragma unroll
      for (int j = 0; j < 4; ++j) acc[i][j] = f32x4{0.f, 0.f, 0.f, 0.f};
    const unsigned a_base0 = sbase + (wm * 64 + fr) * 128 + ch0;
    const unsigned a_base1 = sbase + (wm * 64 + fr) * 128 + ch1;
    const unsigned b_base = sbase + 16384 + wn * 8192 + tr_row;
    unsigned b_off[4];
#pragma unroll
    for (int ni = 0; ni < 4; ++ni) b_off[ni] = b_base + 32 * (ni ^ tswz);
    bf16x8 af0[4], bf0[4], af1[4], bf1[4];
    auto rdg = [&](auto KSC, bf16x8(&af)[4], bf16x8(&bfr)[4], unsigned so, int g) {
      constexpr int KS = decltype(KSC)::value;
      const unsigned ab_ = (KS ? a_base1 : a_base0) + so;
      switch (g) {
        case 0: af[0] = lds_rd128<0>(ab_); break;
        case 2: af[1] = lds_rd128<2048>(ab_); break;
        case 4: af[2] = lds_rd128<4096>(ab_); break;
        case 6: af[3] = lds_rd128<6144>(ab_); break;
        case 1: bfr[0] = tr_frag<KS * 4096>(b_off[0] + so); break;
        case 3: bfr[1] = tr_frag<KS * 4096>(b_off[1] + so); break;
        case 5: bfr[2] = tr_frag<KS * 4096>(b_off[2] + so); break;
        default: bfr[3] = tr_frag<KS * 4096>(b_off[3] + so); break;
      }
    };
    BARRIER();
#pragma unroll
    for (int g = 0; g < 8; ++g) rdg(C0{}, af0, bf0, 0u, g);
    WAIT_L0();
    SCHED0();
    unsigned so = 0;
#pragma unroll 1
    for (int j = 0; j < nt - 1; ++j) {
      mm16(acc, bf0, af0, [&](int g) { rdg(C1{}, af1, bf1, so, g); });
      WAIT_L0();
      SCHED0();
      so = (so == (K2_NST - 1) * K2_STAGE) ? 0u : so + K2_STAGE;
      BARRIER();
      mm16(acc, bf1, af1, [&](int g) { rdg(C0{}, af0, bf0, so, g); });
      WAIT_L0();
      SCHED0();
    }
    mm16(acc, bf0, af0, [&](int g) { rdg(C1{}, af1, bf1, so, g); });
    WAIT_L0();
    SCHED0();
    mm16(acc, bf1, af1, [&](int) {});
    const __amdgpu_buffer_rsrc_t rs = __builtin_amdgcn_make_buffer_rsrc(ws + WS_SLAB, 0, 8u << 20, 0x00020000);
    f32x4 own[4];
#pragma unroll
    for (int mi = 0; mi < 4; ++mi) {
      if (mi == q) {
#pragma unroll
        for (int ni = 0; ni < 4; ++ni) own[ni] = acc[ni][mi];
      } else {
        const unsigned base = (unsigned)((((tile * 4 + mi) * 4 + q) * 4 + wid) * 2) * 1024u + lane * 16u;
#pragma unroll
        for (int np = 0; np < 2; ++np) {
          const f32x4 a0 = acc[2 * np][mi], a1 = acc[2 * np + 1][mi];
          u32x4 v = {pack2(a0[0], a0[1]), pack2(a0[2], a0[3]), pack2(a1[0], a1[1]), pack2(a1[2], a1[3])};
          __builtin_amdgcn_raw_buffer_store_b128(v, rs, base + np * 1024u, 0, 16);
        }
      }
    }
    const size_t orow = (size_t)(rowB + wm * 64 + q * 16 + fr) * D_ + n0 + wn * 64 + fq * 4;
    u32x2 xv[4];
#pragma unroll
    for (int ni = 0; ni < 4; ++ni) xv[ni] = *(const u32x2*)(x + orow + ni * 16);
    WAIT_V(0);
    BARRIER();
    if (wid == 0) {
      if (lane == 0)
        __hip_atomic_store(flag_ptr(ws, FL_READY2 + tile * 4 + q), flag_tag(nonce, FL_READY2 + tile * 4 + q), RLX_AGENT);
      const int fi = FL_READY2 + tile * 4 + (lane & 3);
      const u64 want = flag_tag(nonce, fi);
      u64* p = flag_ptr(ws, fi);
#pragma unroll 1
      for (unsigned spins = 0; spins < SPIN_CAP; ++spins) {
        const u64 v = __hip_atomic_load(p, RLX_AGENT);
        if (__all(v == want || (lane & 3) == q)) break;
        __builtin_amdgcn_s_sleep(1);
      }
    }
    BARRIER();
    u32x4 pv[3][2];
#pragma unroll
    for (int sidx = 0; sidx < 3; ++sidx) {
      const int src = sidx + (sidx >= q ? 1 : 0);
      const unsigned base = (unsigned)((((tile * 4 + q) * 4 + src) * 4 + wid) * 2) * 1024u + lane * 16u;
#pragma unroll
      for (int np = 0; np < 2; ++np) pv[sidx][np] = __builtin_amdgcn_raw_buffer_load_b128(rs, base + np * 1024u, 0, 16);
    }
#pragma unroll
    for (int ni = 0; ni < 4; ++ni) {
      f32x4 v = own[ni];
#pragma unroll
      for (int sidx = 0; sidx < 3; ++sidx) {
        const unsigned w0 = pv[sidx][ni >> 1][(ni & 1) * 2], w1 = pv[sidx][ni >> 1][(ni & 1) * 2 + 1];
        v[0] += bflo(w0); v[1] += bfhi(w0); v[2] += bflo(w1); v[3] += bfhi(w1);
      }
      u32x2 o = {pack2(v[0] + bflo(xv[ni][0]), v[1] + bfhi(xv[ni][0])), pack2(v[2] + bflo(xv[ni][1]), v[3] + bfhi(xv[ni][1]))};
      *(u32x2*)(out + orow + ni * 16) = o;
    }
  }
  if (bid == 0 && wid == 0) {
#pragma unroll 1
    for (unsigned spins = 0; spins < SPIN_CAP; ++spins) {
      bool ok = true;
#pragma unroll
      for (int i = 0; i < 4; ++i) {
        const int fi = FL_ARRIVE + i * 64 + lane;
        ok &= __hip_atomic_load(flag_ptr(ws, fi), RLX_AGENT) == flag_tag(nonce, fi);
      }
      if (__all(ok)) break;
      __builtin_amdgcn_s_sleep(1);
    }
    if (lane == 0) __hip_atomic_store((u64*)(ws + WS_SYNC), nonce + 1, RLX_AGENT);
  }
}

extern "C" void kernel_launch(void* const* d_in, const int* in_sizes, int n_in, void* d_out, int out_size,
                              void* d_ws, size_t ws_size, hipStream_t stream) {
  fused_kernel<<<256, 768, 0, stream>>>((const u16*)d_in[0], (const u16*)d_in[1], (const u16*)d_in[2],
                                        (const u16*)d_in[3], (const u16*)d_in[4], (const u16*)d_in[6],
                                        (const u16*)d_in[7], (const u16*)d_in[8], (u16*)d_out, (char*)d_ws);
}
```

```cpp
#include <hip/hip_runtime.h>
#include <hip/hip_bf16.h>
#include <stdint.h>
#include <type_traits>

typedef unsigned short u16;
typedef unsigned long long u64;
typedef short bf16x8 __attribute__((ext_vector_type(8)));
typedef short bf16x4 __attribute__((ext_vector_type(4)));
typedef float f32x4 __attribute__((ext_vector_type(4)));
typedef float f32x2 __attribute__((ext_vector_type(2)));
typedef unsigned u32x4 __attribute__((ext_vector_type(4)));
typedef unsigned u32x2 __attribute__((ext_vector_type(2)));

constexpr int S_ = 2048, D_ = 512, F_ = 1376, FP_ = 1408;

#define LDS_AS __attribute__((address_space(3)))
#define GLDS16(gptr, ldsptr, aux) \
  __builtin_amdgcn_global_load_lds((const unsigned*)(gptr), (unsigned*)(ldsptr), 16, 0, aux)
#define WAIT_V(n) asm volatile("s_waitcnt vmcnt(" #n ")" ::: "memory")
#define BARRIER() asm volatile("s_barrier" ::: "memory")
#define WAIT_L0() asm volatile("s_waitcnt lgkmcnt(0)" ::: "memory")
#define SCHED0() __builtin_amdgcn_sched_barrier(0)
#define RLX_AGENT __ATOMIC_RELAXED, __HIP_MEMORY_SCOPE_AGENT

__device__ __forceinline__ float bf16r(float x) { return __bfloat162float(__float2bfloat16(x)); }
__device__ __forceinline__ unsigned pack2(float lo, float hi) {
  __hip_bfloat16 a = __float2bfloat16(lo), b = __float2bfloat16(hi);
  return (unsigned)__builtin_bit_cast(u16, a) | ((unsigned)__builtin_bit_cast(u16, b) << 16);
}
__device__ __forceinline__ float bflo(unsigned v) { return __builtin_bit_cast(float, v << 16); }
__device__ __forceinline__ float bfhi(unsigned v) { return __builtin_bit_cast(float, v & 0xffff0000u); }

template <int OFF>
__device__ __forceinline__ bf16x8 tr_frag(unsigned addr) {
  bf16x4 a, b;
  asm volatile("ds_read_b64_tr_b16 %0, %2 offset:%3\n\tds_read_b64_tr_b16 %1, %2 offset:%4"
               : "=&v"(a), "=&v"(b)
               : "v"(addr), "i"(OFF), "i"(OFF + 512));
  return __builtin_shufflevector(a, b, 0, 1, 2, 3, 4, 5, 6, 7);
}
template <int OFF>
__device__ __forceinline__ bf16x8 lds_rd128(unsigned addr) {
  bf16x8 a;
  asm volatile("ds_read_b128 %0, %1 offset:%2" : "=&v"(a) : "v"(addr), "i"(OFF));
  return a;
}
__device__ __forceinline__ unsigned lds_addr(const void* p) { return (unsigned)(uintptr_t)(const LDS_AS char*)p; }
__device__ __forceinline__ float gelu_tanh(float g) {
  const float c1 = -2.0f * 0.7978845608028654f * 1.4426950408889634f;
  const float c2 = c1 * 0.044715f;
  float w = g * __builtin_fmaf(g * g, c2, c1);
  float e = __builtin_amdgcn_exp2f(w);
  return g * __builtin_amdgcn_rcpf(1.0f + e);
}
#define TOUCH(gptr, ldsptr) __builtin_amdgcn_global_load_lds((const unsigned*)(gptr), (unsigned*)(ldsptr), 4, 0, 0)
template <class RD>
__device__ __forceinline__ void mm16(f32x4 (&acc)[4][4], const bf16x8 (&wf)[4], const bf16x8 (&xf)[4], RD rd) {
#pragma unroll
  for (int i = 0; i < 16; ++i) {
    const int ni = i >> 2, mi = i & 3;
    acc[ni][mi] = __builtin_amdgcn_mfma_f32_16x16x32_bf16(wf[ni], xf[mi], acc[ni][mi], 0, 0, 0);
    if (i < 8) {
      SCHED0();
      rd(i);
      SCHED0();
    }
  }
  SCHED0();
}

constexpr size_t WS_AB = 0;
constexpr size_t WS_H = 4u << 20;
constexpr size_t WS_SLAB = 16u << 20;
constexpr size_t WS_SYNC = 32u << 20;
constexpr int FL_ARRIVE = 0, FL_READY1 = 256, FL_READY2 = 512;
__device__ __forceinline__ u64* flag_ptr(char* ws, int idx) { return (u64*)(ws + WS_SYNC + 4096 + (size_t)idx * 64); }
__device__ __forceinline__ u64 flag_tag(u64 nonce, int idx) { return nonce + (u64)(idx + 1) * 0x9E3779B97F4A7C15ull; }
constexpr unsigned SPIN_CAP = 1u << 22;

constexpr int K1_A = 32768, K1_B = 16384, K1_STAGE = K1_A + K1_B;
constexpr int K2_STAGE = 32768, K2_NST = 5;
constexpr int L_SCR = 131072;
constexpr int L_SST = 147456, L_SCS = L_SST + 2048, L_NONCE = L_SCS + 512, L_DUMMY = L_NONCE + 256;

__global__ __launch_bounds__(768, 3) void fused_kernel(const u16* __restrict__ x, const u16* __restrict__ ws_re,
                                                       const u16* __restrict__ ws_im, const u16* __restrict__ wd_re,
                                                       const u16* __restrict__ wd_im, const u16* __restrict__ w_gate,
                                                       const u16* __restrict__ w_up, const u16* __restrict__ w_down,
                                                       u16* __restrict__ out, char* ws) {
  __shared__ __attribute__((aligned(1024))) char smem[K2_NST * K2_STAGE];
  const int tid = threadIdx.x, wid = __builtin_amdgcn_readfirstlane(tid >> 6), lane = tid & 63;
  const int fr = lane & 15, fq = lane >> 4;
  const int bid = blockIdx.x;
  u16* ab = (u16*)(ws + WS_AB);
  u16* h = (u16*)(ws + WS_H);

  const bool has_a = bid < 240;
  const int nb = (bid & 7) * 30 + (bid >> 3);
  const int cb = nb >> 3, rbA = nb & 7;
  const bool nk = cb < 8;
  const int rowA = rbA * 256;
  const int f0 = (cb - 8) * 64;
  const int idx = bid >> 3;
  const int q = (bid & 7) >> 1;
  const int rbB = (bid & 1) * 8 + (idx >> 2), cbk = idx & 3;
  const int rowB = rbB * 128, n0 = cbk * 128, tile = rbB * 4 + cbk;
  const int t_begin = q < 2 ? 22 * q : 44 + 21 * (q - 2);
  const int nt = q < 2 ? 22 : 21;

  const int sw = (fr >> 1) & 7;
  const int ch0 = ((fq) ^ sw) * 16, ch1 = ((4 + fq) ^ sw) * 16;
  const int tq = (lane >> 2) & 3, tp = lane & 3;
  const int tswz = ((tq >> 1) & 1) | ((fq & 1) << 1);
  const int tr_row = (8 * fq + tq) * 128 + 8 * tp;
  float* sst = (float*)(smem + L_SST);
  float* scs = (float*)(smem + L_SCS);
  u64* snonce = (u64*)(smem + L_NONCE);
  const unsigned sbase = lds_addr(smem);

  if (wid >= 8) {
    const int lw = wid - 8;
    const int l3 = lane >> 3, l7 = lane & 7;
    const int rt_c0 = (l7 ^ (fq & 7)) * 8, rt_c1 = (l7 ^ ((fq + 4) & 7)) * 8;
    const int kt_c0 = (l7 ^ ((fq & 1) << 1)) * 8, kt_c1 = (l7 ^ (((fq & 1) | 2) << 1)) * 8;
    if (has_a) {
      __builtin_amdgcn_s_setprio(2);
      const u16* xa0 = x + (size_t)(rowA + lw * 64 + l3) * D_ + rt_c0;
      const u16* xa1 = x + (size_t)(rowA + lw * 64 + 8 + l3) * D_ + rt_c1;
      const u16 *wb0, *wb1;
      int wstep_j, wstep_t;
      if (nk) {
        const u16* wnk = (cb < 4 ? wd_re : wd_im) + (size_t)((cb & 3) * 128 + lw * 32 + l3) * D_;
        wb0 = wnk + rt_c0;
        wb1 = wnk + 8 * D_ + rt_c1;
        wstep_j = 16 * D_;
        wstep_t = 64;
      } else {
        const u16* w = (lw < 2) ? w_gate : w_up;
        int c0 = f0 + kt_c0, c1 = f0 + kt_c1;
        if (c0 >= F_) c0 -= 32;
        if (c1 >= F_) c1 -= 32;
        const u16* wk = w + (size_t)((lw & 1) * 32 + l3) * F_;
        wb0 = wk + c0;
        wb1 = wk + 8 * F_ + c1;
        wstep_j = 16 * F_;
        wstep_t = 64 * F_;
      }
      auto stage = [&](int tj, int buf) {
        const int t = (tj + cb) & 7;
        char* sa = smem + buf * K1_STAGE + lw * 8192;
        char* sb = smem + buf * K1_STAGE + K1_A + lw * 4096;
#pragma unroll
        for (int i = 0; i < 4; ++i) {
          GLDS16(xa0 + i * 16 * D_ + t * 64, sa + i * 2048, 0);
          GLDS16(xa1 + i * 16 * D_ + t * 64, sa + i * 2048 + 1024, 0);
        }
#pragma unroll
        for (int i = 0; i < 2; ++i) {
          GLDS16(wb0 + i * wstep_j + t * wstep_t, sb + i * 2048, 0);
          GLDS16(wb1 + i * wstep_j + t * wstep_t, sb + i * 2048 + 1024, 0);
        }
      };
#pragma unroll 1
      for (int t = 0; t < 2; ++t) stage(t, t);
      f32x4 accS[4], accG[4], accC[2];
#pragma unroll
      for (int i = 0; i < 4; ++i) {
        accS[i] = f32x4{0.f, 0.f, 0.f, 0.f};
        accG[i] = f32x4{0.f, 0.f, 0.f, 0.f};
      }
      accC[0] = f32x4{0.f, 0.f, 0.f, 0.f};
      accC[1] = f32x4{0.f, 0.f, 0.f, 0.f};
      const short ONE = 0x3f80;
      const bf16x8 ones = {ONE, ONE, ONE, ONE, ONE, ONE, ONE, ONE};
      const unsigned la0 = sbase + (lw * 64 + fr) * 128 + ch0;
      const unsigned la1 = sbase + (lw * 64 + fr) * 128 + ch1;
      const unsigned lb0 = sbase + K1_A + (lw * 32 + fr) * 128 + ch0;
      const unsigned lb1 = sbase + K1_A + (lw * 32 + fr) * 128 + ch1;
      const unsigned lt0 = sbase + K1_A + (lw >> 1) * 8192 + tr_row + 32 * (((lw & 1) * 2) ^ tswz);
      const unsigned lt1 = sbase + K1_A + (lw >> 1) * 8192 + tr_row + 32 * (((lw & 1) * 2 + 1) ^ tswz);
      int ib = 2, buf = 0;
#pragma unroll 1
      for (int t = 0; t < 8; ++t) {
        if (t < 7) WAIT_V(12); else WAIT_V(0);
        BARRIER();
        const unsigned so = buf * K1_STAGE;
        bf16x8 xf[8], wf[4];
        xf[0] = lds_rd128<0>(la0 + so); xf[1] = lds_rd128<2048>(la0 + so);
        xf[2] = lds_rd128<4096>(la0 + so); xf[3] = lds_rd128<6144>(la0 + so);
        xf[4] = lds_rd128<0>(la1 + so); xf[5] = lds_rd128<2048>(la1 + so);
        xf[6] = lds_rd128<4096>(la1 + so); xf[7] = lds_rd128<6144>(la1 + so);
        if (nk) {
          wf[0] = lds_rd128<0>(lb0 + so); wf[1] = lds_rd128<2048>(lb0 + so);
          wf[2] = lds_rd128<0>(lb1 + so); wf[3] = lds_rd128<2048>(lb1 + so);
        } else {
          wf[0] = tr_frag<0>(lt0 + so); wf[1] = tr_frag<0>(lt1 + so);
          wf[2] = tr_frag<4096>(lt0 + so); wf[3] = tr_frag<4096>(lt1 + so);
        }
        if (t + 2 < 8) stage(t + 2, ib);
        ib = (ib == 2) ? 0 : ib + 1;
        WAIT_L0();
        SCHED0();
#pragma unroll
        for (int i = 0; i < 8; ++i) {
          accS[i & 3] = __builtin_amdgcn_mfma_f32_16x16x32_bf16(ones, xf[i], accS[i & 3], 0, 0, 0);
          accG[i & 3] = __builtin_amdgcn_mfma_f32_16x16x32_bf16(xf[i], xf[i], accG[i & 3], 0, 0, 0);
        }
#pragma unroll
        for (int i = 0; i < 4; ++i)
          accC[i & 1] = __builtin_amdgcn_mfma_f32_16x16x32_bf16(wf[i], ones, accC[i & 1], 0, 0, 0);
        buf = (buf == 2) ? 0 : buf + 1;
      }
      if (fq == (fr >> 2)) {
#pragma unroll
        for (int i = 0; i < 4; ++i) {
          const int r = fr & 3;
          const float qq = r == 0 ? accG[i][0] : r == 1 ? accG[i][1] : r == 2 ? accG[i][2] : accG[i][3];
          const float mean = accS[i][0] * (1.0f / D_);
          const float var = fmaxf(qq * (1.0f / D_) - mean * mean, 0.f);
          const float sd = bf16r(sqrtf(bf16r(bf16r(var) + 1e-5f)));
          f32x2 o = {bf16r(mean), __builtin_amdgcn_rcpf(sd)};
          *(f32x2*)(sst + (lw * 64 + i * 16 + fr) * 2) = o;
        }
      }
      if (fr == 0) {
        *(f32x4*)(scs + lw * 32 + fq * 4) = accC[0];
        *(f32x4*)(scs + lw * 32 + 16 + fq * 4) = accC[1];
      }
      WAIT_L0();
      BARRIER();
      __builtin_amdgcn_s_setprio(0);
    }
    const int sub = lw & 1;
    auto stageB = [&](int j, int slot) {
      const int t = t_begin + j;
      char* dst = smem + slot * K2_STAGE + lw * 8192;
      if (lw < 2) {
        if (t < 64) {
          const u16* A = (t < 32 ? ws_re : ws_im) + (size_t)(rowB + sub * 64 + l3) * S_ + (t & 31) * 64;
#pragma unroll
          for (int i = 0; i < 8; ++i) GLDS16(A + i * 8 * S_ + ((i & 1) ? rt_c1 : rt_c0), dst + i * 1024, 0);
        } else {
          const u16* A = h + (size_t)(rowB + sub * 64 + l3) * FP_ + (t - 64) * 64;
#pragma unroll
          for (int i = 0; i < 8; ++i) GLDS16(A + i * 8 * FP_ + ((i & 1) ? rt_c1 : rt_c0), dst + i * 1024, 16);
        }
      } else {
        if (t < 64) {
          const u16* B = ab + (size_t)(t * 64 + l3) * D_ + n0 + sub * 64;
#pragma unroll
          for (int i = 0; i < 8; ++i) GLDS16(B + i * 8 * D_ + ((i & 1) ? kt_c1 : kt_c0), dst + i * 1024, 16);
        } else {
          const int k0 = (t - 64) * 64;
#pragma unroll
          for (int i = 0; i < 8; ++i) {
            const int krow = min(k0 + i * 8 + l3, F_ - 1);
            GLDS16(w_down + krow * D_ + n0 + sub * 64 + ((i & 1) ? kt_c1 : kt_c0), dst + i * 1024, 0);
          }
        }
      }
    };
    const bool early = (lw < 2) ? (t_begin + K2_NST - 2 < 64) : (t_begin >= 64);
    if (early) {
#pragma unroll 1
      for (int s = 0; s < K2_NST - 1; ++s) stageB(s, s);
    }
    BARRIER();
    BARRIER();
    if (!early) {
#pragma unroll 1
      for (int s = 0; s < K2_NST - 1; ++s) stageB(s, s);
    }
    int islot = K2_NST - 1;
#pragma unroll 1
    for (int j = 0; j < nt; ++j) {
      const int rem = nt - 1 - j;
      if (rem >= 3) WAIT_V(24);
      else if (rem == 2) WAIT_V(16);
      else if (rem == 1) WAIT_V(8);
      else WAIT_V(0);
      BARRIER();
      if (j + K2_NST - 1 < nt) stageB(j + K2_NST - 1, islot);
      islot = (islot == K2_NST - 1) ? 0 : islot + 1;
    }
    return;
  }

  u64 nonce = 0;
  if (wid == 4) {
    nonce = __hip_atomic_load((u64*)(ws + WS_SYNC), RLX_AGENT);
    nonce = ((u64)__builtin_amdgcn_readfirstlane((unsigned)(nonce >> 32)) << 32) |
            (u64)__builtin_amdgcn_readfirstlane((unsigned)nonce);
    if (lane == 0) {
      *snonce = nonce;
      __hip_atomic_store(flag_ptr(ws, FL_ARRIVE + bid), flag_tag(nonce, FL_ARRIVE + bid), RLX_AGENT);
    }
  }
  const int wm = wid >> 1, wn = wid & 1;
  using C0 = std::integral_constant<int, 0>;
  using C1 = std::integral_constant<int, 1>;
  char* dummy = smem + L_DUMMY;
  auto warm = [&](int slot) {
    if (lane >= 16) return;
    const int L = slot * 16 + lane;
    if (q < 3) {
      const int ns = min(nt, 64 - t_begin);
      const int row = L / ns, t = t_begin + (L - row * ns);
      if (row < 32) TOUCH((t < 32 ? ws_re : ws_im) + (size_t)(rowB + cbk * 32 + row) * S_ + (t & 31) * 64, dummy);
    } else {
      const int krow = 64 + (idx >> 2) * 168 + (L >> 1);
      if (L < 336 && krow < F_) TOUCH(w_down + (size_t)krow * D_ + n0 + (L & 1) * 64, dummy);
    }
  };
  if (!has_a) {
#pragma unroll 1
    for (int i = 0; i < 6; ++i) warm(i * 8 + wid);
  }

  if (has_a) {
    f32x4 acc[4][4];
#pragma unroll
    for (int i = 0; i < 4; ++i)
#pragma unroll
      for (int j = 0; j < 4; ++j) acc[i][j] = f32x4{0.f, 0.f, 0.f, 0.f};
    const unsigned a_b0 = sbase + (wm * 64 + fr) * 128 + ch0, a_b1 = sbase + (wm * 64 + fr) * 128 + ch1;
    const unsigned n_b0 = sbase + K1_A + (wn * 64 + fr) * 128 + ch0;
    const unsigned n_b1 = sbase + K1_A + (wn * 64 + fr) * 128 + ch1;
    const unsigned t_p0 = sbase + K1_A + tr_row + 32 * ((wn * 2) ^ tswz);
    const unsigned t_p1 = sbase + K1_A + tr_row + 32 * ((wn * 2 + 1) ^ tswz);

    auto mainloop = [&](auto NKC) {
      constexpr bool NK = decltype(NKC)::value;
      bf16x8 xf0[4], wf0[4], xf1[4], wf1[4];
      auto rdg = [&](auto KSC, bf16x8(&xf)[4], bf16x8(&wf)[4], unsigned so, int g) {
        constexpr int KS = decltype(KSC)::value;
        const unsigned ab_ = (KS ? a_b1 : a_b0) + so;
        switch (g) {
          case 0: xf[0] = lds_rd128<0>(ab_); break;
          case 2: xf[1] = lds_rd128<2048>(ab_); break;
          case 4: xf[2] = lds_rd128<4096>(ab_); break;
          case 6: xf[3] = lds_rd128<6144>(ab_); break;
          default:
            if constexpr (NK) {
              const unsigned nb_ = (KS ? n_b1 : n_b0) + so;
              if (g == 1) wf[0] = lds_rd128<0>(nb_);
              if (g == 3) wf[1] = lds_rd128<2048>(nb_);
              if (g == 5) wf[2] = lds_rd128<4096>(nb_);
              if (g == 7) wf[3] = lds_rd128<6144>(nb_);
            } else {
              if (g == 1) wf[0] = tr_frag<KS * 4096>(t_p0 + so);
              if (g == 3) wf[1] = tr_frag<KS * 4096>(t_p1 + so);
              if (g == 5) wf[2] = tr_frag<KS * 4096 + 8192>(t_p0 + so);
              if (g == 7) wf[3] = tr_frag<KS * 4096 + 8192>(t_p1 + so);
            }
        }
      };
      BARRIER();
#pragma unroll
      for (int g = 0; g < 8; ++g) rdg(C0{}, xf0, wf0, 0u, g);
      WAIT_L0();
      SCHED0();
      unsigned so = 0;
#pragma unroll 1
      for (int t = 0; t < 7; ++t) {
        if (t > 0) warm((t - 1) * 8 + wid);
        mm16(acc, wf0, xf0, [&](int g) { rdg(C1{}, xf1, wf1, so, g); });
        WAIT_L0();
        SCHED0();
        so = (so == 2 * K1_STAGE) ? 0u : so + K1_STAGE;
        BARRIER();
        mm16(acc, wf1, xf1, [&](int g) { rdg(C0{}, xf0, wf0, so, g); });
        WAIT_L0();
        SCHED0();
      }
      mm16(acc, wf0, xf0, [&](int g) { rdg(C1{}, xf1, wf1, so, g); });
      WAIT_L0();
      SCHED0();
      mm16(acc, wf1, xf1, [&](int) {});
    };
    if (nk) mainloop(std::true_type{});
    else mainloop(std::false_type{});
    BARRIER();
    float mb[4], rinv[4];
#pragma unroll
    for (int mi = 0; mi < 4; ++mi) {
      f32x2 v = *(const f32x2*)(sst + (wm * 64 + mi * 16 + fr) * 2);
      mb[mi] = v[0];
      rinv[mi] = v[1];
    }
    f32x4 csv[4];
#pragma unroll
    for (int ni = 0; ni < 4; ++ni) {
      const int ci = nk ? (wn * 64 + ni * 16) : ((ni >> 1) * 64 + wn * 32 + (ni & 1) * 16);
      csv[ni] = *(const f32x4*)(scs + ci + fq * 4);
    }
    char* scr = smem + L_SCR + wid * 2048;
    const __amdgpu_buffer_rsrc_t rsw = __builtin_amdgcn_make_buffer_rsrc(ws, 0, 16u << 20, 0x00020000);
    if (nk) {
      const float sgn = cb < 4 ? 1.0f : -1.0f;
      const unsigned gbase = (unsigned)WS_AB +
          (unsigned)(((cb >> 2) * S_ + rowA + wm * 64 + (lane >> 3)) * D_ + (cb & 3) * 128 + wn * 64 + (lane & 7) * 8) * 2u;
#pragma unroll
      for (int mi = 0; mi < 4; ++mi) {
        const float r = rinv[mi] * sgn;
#pragma unroll
        for (int ni = 0; ni < 4; ++ni) {
          f32x4 v = (acc[ni][mi] - mb[mi] * csv[ni]) * r;
          u32x2 o = {pack2(v[0], v[1]), pack2(v[2], v[3])};
          *(u32x2*)(scr + fr * 128 + (((ni * 2 + (fq >> 1)) ^ (fr & 7)) * 16) + (fq & 1) * 8) = o;
        }
#pragma unroll
        for (int hf = 0; hf < 2; ++hf) {
          const int r2 = hf * 8 + (lane >> 3);
          u32x4 v = *(const u32x4*)(scr + r2 * 128 + (((lane & 7) ^ (r2 & 7)) * 16));
          __builtin_amdgcn_raw_buffer_store_b128(v, rsw, gbase + (unsigned)((mi * 16 + hf * 8) * D_ * 2), 0, 16);
        }
      }
    } else {
      const unsigned vmask = (f0 + wn * 32) < F_ ? 0xffffffffu : 0u;
      const unsigned gbase = (unsigned)WS_H +
          (unsigned)((rowA + wm * 64 + (lane >> 2)) * FP_ + f0 + wn * 32 + (lane & 3) * 8) * 2u;
#pragma unroll
      for (int mi = 0; mi < 4; ++mi) {
#pragma unroll
        for (int ni = 0; ni < 2; ++ni) {
          f32x4 g = (acc[ni][mi] - mb[mi] * csv[ni]) * rinv[mi];
          f32x4 u = (acc[ni + 2][mi] - mb[mi] * csv[ni + 2]) * rinv[mi];
          float hv[4];
#pragma unroll
          for (int e = 0; e < 4; ++e) {
            float gl = bf16r(gelu_tanh(bf16r(g[e])));
            hv[e] = gl * bf16r(u[e]);
          }
          u32x2 o = {pack2(hv[0], hv[1]) & vmask, pack2(hv[2], hv[3]) & vmask};
          *(u32x2*)(scr + fr * 64 + (((ni * 2 + (fq >> 1)) ^ ((fr >> 1) & 3)) * 16) + (fq & 1) * 8) = o;
        }
        const int r2 = lane >> 2;
        u32x4 v = *(const u32x4*)(scr + r2 * 64 + (((lane & 3) ^ ((r2 >> 1) & 3)) * 16));
        __builtin_amdgcn_raw_buffer_store_b128(v, rsw, gbase + (unsigned)(mi * 16 * FP_ * 2), 0, 16);
      }
    }
  }
  WAIT_V(0);
  WAIT_L0();
  BARRIER();
  if (wid > 4) return;

  if (wid == 4) {
    int last = -1;
    auto wait_dep = [&](int j) {
      const int t = t_begin + j;
      const int fi = FL_READY1 + (t < 64 ? (((t >> 5) * 4 + cbk) * 8 + ((t & 31) >> 2)) : ((8 + t - 64) * 8 + (rbB >> 1)));
      if (fi == last) return;
      last = fi;
      const u64 want = flag_tag(nonce, fi);
      u64* p = flag_ptr(ws, fi);
#pragma unroll 1
      for (unsigned spins = 0; spins < SPIN_CAP; ++spins) {
        const u64 v = __hip_atomic_load(p, RLX_AGENT);
        const bool ok = __builtin_amdgcn_readfirstlane((unsigned)(v == want)) != 0;
        if (ok) break;
        __builtin_amdgcn_s_sleep(1);
      }
    };
#pragma unroll 1
    for (int j = 0; j < K2_NST - 1; ++j) wait_dep(j);
    BARRIER();
#pragma unroll 1
    for (int j = 0; j < nt; ++j) {
      if (j + K2_NST - 1 < nt) wait_dep(j + K2_NST - 1);
      BARRIER();
    }
    return;
  }

  if (wid == 0) {
    nonce = *snonce;
    nonce = ((u64)__builtin_amdgcn_readfirstlane((unsigned)(nonce >> 32)) << 32) |
            (u64)__builtin_amdgcn_readfirstlane((unsigned)nonce);
    if (has_a && lane == 0) __hip_atomic_store(flag_ptr(ws, FL_READY1 + nb), flag_tag(nonce, FL_READY1 + nb), RLX_AGENT);
  }
  BARRIER();
  {
    f32x4 acc[4][4];
#pragma unroll
    for (int i = 0; i < 4; ++i)
#pragma unroll
      for (int j = 0; j < 4; ++j) acc[i][j] = f32x4{0.f, 0.f, 0.f, 0.f};
    const unsigned a_base0 = sbase + (wm * 64 + fr) * 128 + ch0;
    const unsigned a_base1 = sbase + (wm * 64 + fr) * 128 + ch1;
    const unsigned b_base = sbase + 16384 + wn * 8192 + tr_row;
    unsigned b_off[4];
#pragma unroll
    for (int ni = 0; ni < 4; ++ni) b_off[ni] = b_base + 32 * (ni ^ tswz);
    bf16x8 af0[4], bf0[4], af1[4], bf1[4];
    auto rdg = [&](auto KSC, bf16x8(&af)[4], bf16x8(&bfr)[4], unsigned so, int g) {
      constexpr int KS = decltype(KSC)::value;
      const unsigned ab_ = (KS ? a_base1 : a_base0) + so;
      switch (g) {
        case 0: af[0] = lds_rd128<0>(ab_); break;
        case 2: af[1] = lds_rd128<2048>(ab_); break;
        case 4: af[2] = lds_rd128<4096>(ab_); break;
        case 6: af[3] = lds_rd128<6144>(ab_); break;
        case 1: bfr[0] = tr_frag<KS * 4096>(b_off[0] + so); break;
        case 3: bfr[1] = tr_frag<KS * 4096>(b_off[1] + so); break;
        case 5: bfr[2] = tr_frag<KS * 4096>(b_off[2] + so); break;
        default: bfr[3] = tr_frag<KS * 4096>(b_off[3] + so); break;
      }
    };
    BARRIER();
#pragma unroll
    for (int g = 0; g < 8; ++g) rdg(C0{}, af0, bf0, 0u, g);
    WAIT_L0();
    SCHED0();
    unsigned so = 0;
#pragma unroll 1
    for (int j = 0; j < nt - 1; ++j) {
      mm16(acc, bf0, af0, [&](int g) { rdg(C1{}, af1, bf1, so, g); });
      WAIT_L0();
      SCHED0();
      so = (so == (K2_NST - 1) * K2_STAGE) ? 0u : so + K2_STAGE;
      BARRIER();
      mm16(acc, bf1, af1, [&](int g) { rdg(C0{}, af0, bf0, so, g); });
      WAIT_L0();
      SCHED0();
    }
    mm16(acc, bf0, af0, [&](int g) { rdg(C1{}, af1, bf1, so, g); });
    WAIT_L0();
    SCHED0();
    mm16(acc, bf1, af1, [&](int) {});
    const __amdgpu_buffer_rsrc_t rs = __builtin_amdgcn_make_buffer_rsrc(ws + WS_SLAB, 0, 8u << 20, 0x00020000);
    f32x4 own[4];
#pragma unroll
    for (int mi = 0; mi < 4; ++mi) {
      if (mi == q) {
#pragma unroll
        for (int ni = 0; ni < 4; ++ni) own[ni] = acc[ni][mi];
      } else {
        const unsigned base = (unsigned)((((tile * 4 + mi) * 4 + q) * 4 + wid) * 2) * 1024u + lane * 16u;
#pragma unroll
        for (int np = 0; np < 2; ++np) {
          const f32x4 a0 = acc[2 * np][mi], a1 = acc[2 * np + 1][mi];
          u32x4 v = {pack2(a0[0], a0[1]), pack2(a0[2], a0[3]), pack2(a1[0], a1[1]), pack2(a1[2], a1[3])};
          __builtin_amdgcn_raw_buffer_store_b128(v, rs, base + np * 1024u, 0, 16);
        }
      }
    }
    const size_t orow = (size_t)(rowB + wm * 64 + q * 16 + fr) * D_ + n0 + wn * 64 + fq * 4;
    u32x2 xv[4];
#pragma unroll
    for (int ni = 0; ni < 4; ++ni) xv[ni] = *(const u32x2*)(x + orow + ni * 16);
    WAIT_V(0);
    BARRIER();
    if (wid == 0) {
      if (lane == 0)
        __hip_atomic_store(flag_ptr(ws, FL_READY2 + tile * 4 + q), flag_tag(nonce, FL_READY2 + tile * 4 + q), RLX_AGENT);
      const int fi = FL_READY2 + tile * 4 + (lane & 3);
      const u64 want = flag_tag(nonce, fi);
      u64* p = flag_ptr(ws, fi);
#pragma unroll 1
      for (unsigned spins = 0; spins < SPIN_CAP; ++spins) {
        const u64 v = __hip_atomic_load(p, RLX_AGENT);
        if (__all(v == want || (lane & 3) == q)) break;
        __builtin_amdgcn_s_sleep(1);
      }
    }
    BARRIER();
    u32x4 pv[3][2];
#pragma unroll
    for (int sidx = 0; sidx < 3; ++sidx) {
      const int src = sidx + (sidx >= q ? 1 : 0);
      const unsigned base = (unsigned)((((tile * 4 + q) * 4 + src) * 4 + wid) * 2) * 1024u + lane * 16u;
#pragma unroll
      for (int np = 0; np < 2; ++np) pv[sidx][np] = __builtin_amdgcn_raw_buffer_load_b128(rs, base + np * 1024u, 0, 16);
    }
#pragma unroll
    for (int ni = 0; ni < 4; ++ni) {
      f32x4 v = own[ni];
#pragma unroll
      for (int sidx = 0; sidx < 3; ++sidx) {
        const unsigned w0 = pv[sidx][ni >> 1][(ni & 1) * 2], w1 = pv[sidx][ni >> 1][(ni & 1) * 2 + 1];
        v[0] += bflo(w0); v[1] += bfhi(w0); v[2] += bflo(w1); v[3] += bfhi(w1);
      }
      u32x2 o = {pack2(v[0] + bflo(xv[ni][0]), v[1] + bfhi(xv[ni][0])), pack2(v[2] + bflo(xv[ni][1]), v[3] + bfhi(xv[ni][1]))};
      *(u32x2*)(out + orow + ni * 16) = o;
    }
  }
  if (bid == 0 && wid == 0) {
#pragma unroll 1
    for (unsigned spins = 0; spins < SPIN_CAP; ++spins) {
      bool ok = true;
#pragma unroll
      for (int i = 0; i < 4; ++i) {
        const int fi = FL_ARRIVE + i * 64 + lane;
        ok &= __hip_atomic_load(flag_ptr(ws, fi), RLX_AGENT) == flag_tag(nonce, fi);
      }
      if (__all(ok)) break;
      __builtin_amdgcn_s_sleep(1);
    }
    if (lane == 0) __hip_atomic_store((u64*)(ws + WS_SYNC), nonce + 1, RLX_AGENT);
  }
}

extern "C" void kernel_launch(void* const* d_in, const int* in_sizes, int n_in, void* d_out, int out_size,
                              void* d_ws, size_t ws_size, hipStream_t stream) {
  fused_kernel<<<256, 768, 0, stream>>>((const u16*)d_in[0], (const u16*)d_in[1], (const u16*)d_in[2],
                                        (const u16*)d_in[3], (const u16*)d_in[4], (const u16*)d_in[6],
                                        (const u16*)d_in[7], (const u16*)d_in[8], (u16*)d_out, (char*)d_ws);
}
```

```cpp
#include <hip/hip_runtime.h>
#include <hip/hip_bf16.h>
#include <stdint.h>
#include <type_traits>

typedef unsigned short u16;
typedef unsigned long long u64;
typedef short bf16x8 __attribute__((ext_vector_type(8)));
typedef short bf16x4 __attribute__((ext_vector_type(4)));
typedef float f32x4 __attribute__((ext_vector_type(4)));
typedef float f32x2 __attribute__((ext_vector_type(2)));
typedef unsigned u32x4 __attribute__((ext_vector_type(4)));
typedef unsigned u32x2 __attribute__((ext_vector_type(2)));

#define TB1 23
#define TB2 45
#define TB3 67
constexpr int S_ = 2048, D_ = 512, F_ = 1376, FP_ = 1408;

#define LDS_AS __attribute__((address_space(3)))
#define GLDS16(gptr, ldsptr, aux) \
  __builtin_amdgcn_global_load_lds((const unsigned*)(gptr), (unsigned*)(ldsptr), 16, 0, aux)
#define WAIT_V(n) asm volatile("s_waitcnt vmcnt(" #n ")" ::: "memory")
#define BARRIER() asm volatile("s_barrier" ::: "memory")
#define WAIT_L0() asm volatile("s_waitcnt lgkmcnt(0)" ::: "memory")
#define SCHED0() __builtin_amdgcn_sched_barrier(0)
#define RLX_AGENT __ATOMIC_RELAXED, __HIP_MEMORY_SCOPE_AGENT

__device__ __forceinline__ float bf16r(float x) { return __bfloat162float(__float2bfloat16(x)); }
__device__ __forceinline__ unsigned pack2(float lo, float hi) {
  __hip_bfloat16 a = __float2bfloat16(lo), b = __float2bfloat16(hi);
  return (unsigned)__builtin_bit_cast(u16, a) | ((unsigned)__builtin_bit_cast(u16, b) << 16);
}
__device__ __forceinline__ float bflo(unsigned v) { return __builtin_bit_cast(float, v << 16); }
__device__ __forceinline__ float bfhi(unsigned v) { return __builtin_bit_cast(float, v & 0xffff0000u); }

template <int OFF>
__device__ __forceinline__ bf16x8 tr_frag(unsigned addr) {
  bf16x4 a, b;
  asm volatile("ds_read_b64_tr_b16 %0, %2 offset:%3\n\tds_read_b64_tr_b16 %1, %2 offset:%4"
               : "=&v"(a), "=&v"(b)
               : "v"(addr), "i"(OFF), "i"(OFF + 512));
  return __builtin_shufflevector(a, b, 0, 1, 2, 3, 4, 5, 6, 7);
}
template <int OFF>
__device__ __forceinline__ bf16x8 lds_rd128(unsigned addr) {
  bf16x8 a;
  asm volatile("ds_read_b128 %0, %1 offset:%2" : "=&v"(a) : "v"(addr), "i"(OFF));
  return a;
}
__device__ __forceinline__ unsigned lds_addr(const void* p) { return (unsigned)(uintptr_t)(const LDS_AS char*)p; }
__device__ __forceinline__ float gelu_tanh(float g) {
  const float c1 = -2.0f * 0.7978845608028654f * 1.4426950408889634f;
  const float c2 = c1 * 0.044715f;
  float w = g * __builtin_fmaf(g * g, c2, c1);
  float e = __builtin_amdgcn_exp2f(w);
  return g * __builtin_amdgcn_rcpf(1.0f + e);
}
#define TOUCH(gptr, ldsptr) __builtin_amdgcn_global_load_lds((const unsigned*)(gptr), (unsigned*)(ldsptr), 4, 0, 0)
template <class RD>
__device__ __forceinline__ void mm16(f32x4 (&acc)[4][4], const bf16x8 (&wf)[4], const bf16x8 (&xf)[4], RD rd) {
#pragma unroll
  for (int i = 0; i < 16; ++i) {
    const int ni = i >> 2, mi = i & 3;
    acc[ni][mi] = __builtin_amdgcn_mfma_f32_16x16x32_bf16(wf[ni], xf[mi], acc[ni][mi], 0, 0, 0);
    if (i < 8) {
      SCHED0();
      rd(i);
      SCHED0();
    }
  }
  SCHED0();
}

constexpr size_t WS_AB = 0;
constexpr size_t WS_H = 4u << 20;
constexpr size_t WS_SLAB = 16u << 20;
constexpr size_t WS_SYNC = 32u << 20;
constexpr int FL_ARRIVE = 0, FL_READY1 = 256, FL_READY2 = 512;
__device__ __forceinline__ u64* flag_ptr(char* ws, int idx) { return (u64*)(ws + WS_SYNC + 4096 + (size_t)idx * 64); }
__device__ __forceinline__ u64 flag_tag(u64 nonce, int idx) { return nonce + (u64)(idx + 1) * 0x9E3779B97F4A7C15ull; }
constexpr unsigned SPIN_CAP = 1u << 22;

constexpr int K1_A = 32768, K1_B = 16384, K1_STAGE = K1_A + K1_B;
constexpr int K2_STAGE = 32768, K2_NST = 5;
constexpr int L_SCR = 131072;
constexpr int L_SST = 147456, L_SCS = L_SST + 2048, L_NONCE = L_SCS + 512, L_DUMMY = L_NONCE + 256;

__global__ __launch_bounds__(768, 3) void fused_kernel(const u16* __restrict__ x, const u16* __restrict__ ws_re,
                                                       const u16* __restrict__ ws_im, const u16* __restrict__ wd_re,
                                                       const u16* __restrict__ wd_im, const u16* __restrict__ w_gate,
                                                       const u16* __restrict__ w_up, const u16* __restrict__ w_down,
                                                       u16* __restrict__ out, char* ws) {
  __shared__ __attribute__((aligned(1024))) char smem[K2_NST * K2_STAGE];
  const int tid = threadIdx.x, wid = __builtin_amdgcn_readfirstlane(tid >> 6), lane = tid & 63;
  const int fr = lane & 15, fq = lane >> 4;
  const int bid = blockIdx.x;
  u16* ab = (u16*)(ws + WS_AB);
  u16* h = (u16*)(ws + WS_H);

  const bool has_a = bid < 240;
  const int nb = (bid & 7) * 30 + (bid >> 3);
  const int cb = nb >> 3, rbA = nb & 7;
  const bool nk = cb < 8;
  const int rowA = rbA * 256;
  const int f0 = (cb - 8) * 64;
  const int idx = bid >> 3;
  const int q = (bid & 7) >> 1;
  const int rbB = (bid & 1) * 8 + (idx >> 2), cbk = idx & 3;
  const int rowB = rbB * 128, n0 = cbk * 128, tile = rbB * 4 + cbk;
  const int t_begin = q == 0 ? 0 : q == 1 ? TB1 : q == 2 ? TB2 : TB3;
  const int nt = (q == 0 ? TB1 : q == 1 ? TB2 : q == 2 ? TB3 : 86) - t_begin;

  const int sw = (fr >> 1) & 7;
  const int ch0 = ((fq) ^ sw) * 16, ch1 = ((4 + fq) ^ sw) * 16;
  const int tq = (lane >> 2) & 3, tp = lane & 3;
  const int tswz = ((tq >> 1) & 1) | ((fq & 1) << 1);
  const int tr_row = (8 * fq + tq) * 128 + 8 * tp;
  float* sst = (float*)(smem + L_SST);
  float* scs = (float*)(smem + L_SCS);
  u64* snonce = (u64*)(smem + L_NONCE);
  const unsigned sbase = lds_addr(smem);

  if (wid >= 8) {
    const int lw = wid - 8;
    const int l3 = lane >> 3, l7 = lane & 7;
    const int rt_c0 = (l7 ^ (fq & 7)) * 8, rt_c1 = (l7 ^ ((fq + 4) & 7)) * 8;
    const int kt_c0 = (l7 ^ ((fq & 1) << 1)) * 8, kt_c1 = (l7 ^ (((fq & 1) | 2) << 1)) * 8;
    if (has_a) {
      __builtin_amdgcn_s_setprio(2);
      const u16* xa0 = x + (size_t)(rowA + lw * 64 + l3) * D_ + rt_c0;
      const u16* xa1 = x + (size_t)(rowA + lw * 64 + 8 + l3) * D_ + rt_c1;
      const u16 *wb0, *wb1;
      int wstep_j, wstep_t;
      if (nk) {
        const u16* wnk = (cb < 4 ? wd_re : wd_im) + (size_t)((cb & 3) * 128 + lw * 32 + l3) * D_;
        wb0 = wnk + rt_c0;
        wb1 = wnk + 8 * D_ + rt_c1;
        wstep_j = 16 * D_;
        wstep_t = 64;
      } else {
        const u16* w = (lw < 2) ? w_gate : w_up;
        int c0 = f0 + kt_c0, c1 = f0 + kt_c1;
        if (c0 >= F_) c0 -= 32;
        if (c1 >= F_) c1 -= 32;
        const u16* wk = w + (size_t)((lw & 1) * 32 + l3) * F_;
        wb0 = wk + c0;
        wb1 = wk + 8 * F_ + c1;
        wstep_j = 16 * F_;
        wstep_t = 64 * F_;
      }
      auto stage = [&](int tj, int buf) {
        const int t = (tj + cb) & 7;
        char* sa = smem + buf * K1_STAGE + lw * 8192;
        char* sb = smem + buf * K1_STAGE + K1_A + lw * 4096;
#pragma unroll
        for (int i = 0; i < 4; ++i) {
          GLDS16(xa0 + i * 16 * D_ + t * 64, sa + i * 2048, 0);
          GLDS16(xa1 + i * 16 * D_ + t * 64, sa + i * 2048 + 1024, 0);
        }
#pragma unroll
        for (int i = 0; i < 2; ++i) {
          GLDS16(wb0 + i * wstep_j + t * wstep_t, sb + i * 2048, 0);
          GLDS16(wb1 + i * wstep_j + t * wstep_t, sb + i * 2048 + 1024, 0);
        }
      };
#pragma unroll 1
      for (int t = 0; t < 2; ++t) stage(t, t);
      f32x4 accS[4], accG[4], accC[2];
#pragma unroll
      for (int i = 0; i < 4; ++i) {
        accS[i] = f32x4{0.f, 0.f, 0.f, 0.f};
        accG[i] = f32x4{0.f, 0.f, 0.f, 0.f};
      }
      accC[0] = f32x4{0.f, 0.f, 0.f, 0.f};
      accC[1] = f32x4{0.f, 0.f, 0.f, 0.f};
      const short ONE = 0x3f80;
      const bf16x8 ones = {ONE, ONE, ONE, ONE, ONE, ONE, ONE, ONE};
      const unsigned la0 = sbase + (lw * 64 + fr) * 128 + ch0;
      const unsigned la1 = sbase + (lw * 64 + fr) * 128 + ch1;
      const unsigned lb0 = sbase + K1_A + (lw * 32 + fr) * 128 + ch0;
      const unsigned lb1 = sbase + K1_A + (lw * 32 + fr) * 128 + ch1;
      const unsigned lt0 = sbase + K1_A + (lw >> 1) * 8192 + tr_row + 32 * (((lw & 1) * 2) ^ tswz);
      const unsigned lt1 = sbase + K1_A + (lw >> 1) * 8192 + tr_row + 32 * (((lw & 1) * 2 + 1) ^ tswz);
      int ib = 2, buf = 0;
#pragma unroll 1
      for (int t = 0; t < 8; ++t) {
        if (t < 7) WAIT_V(12); else WAIT_V(0);
        BARRIER();
        const unsigned so = buf * K1_STAGE;
        bf16x8 xf[8], wf[4];
        xf[0] = lds_rd128<0>(la0 + so); xf[1] = lds_rd128<2048>(la0 + so);
        xf[2] = lds_rd128<4096>(la0 + so); xf[3] = lds_rd128<6144>(la0 + so);
        xf[4] = lds_rd128<0>(la1 + so); xf[5] = lds_rd128<2048>(la1 + so);
        xf[6] = lds_rd128<4096>(la1 + so); xf[7] = lds_rd128<6144>(la1 + so);
        if (nk) {
          wf[0] = lds_rd128<0>(lb0 + so); wf[1] = lds_rd128<2048>(lb0 + so);
          wf[2] = lds_rd128<0>(lb1 + so); wf[3] = lds_rd128<2048>(lb1 + so);
        } else {
          wf[0] = tr_frag<0>(lt0 + so); wf[1] = tr_frag<0>(lt1 + so);
          wf[2] = tr_frag<4096>(lt0 + so); wf[3] = tr_frag<4096>(lt1 + so);
        }
        if (t + 2 < 8) stage(t + 2, ib);
        ib = (ib == 2) ? 0 : ib + 1;
        WAIT_L0();
        SCHED0();
#pragma unroll
        for (int i = 0; i < 8; ++i) {
          accS[i & 3] = __builtin_amdgcn_mfma_f32_16x16x32_bf16(ones, xf[i], accS[i & 3], 0, 0, 0);
          accG[i & 3] = __builtin_amdgcn_mfma_f32_16x16x32_bf16(xf[i], xf[i], accG[i & 3], 0, 0, 0);
        }
#pragma unroll
        for (int i = 0; i < 4; ++i)
          accC[i & 1] = __builtin_amdgcn_mfma_f32_16x16x32_bf16(wf[i], ones, accC[i & 1], 0, 0, 0);
        buf = (buf == 2) ? 0 : buf + 1;
      }
      if (fq == (fr >> 2)) {
#pragma unroll
        for (int i = 0; i < 4; ++i) {
          const int r = fr & 3;
          const float qq = r == 0 ? accG[i][0] : r == 1 ? accG[i][1] : r == 2 ? accG[i][2] : accG[i][3];
          const float mean = accS[i][0] * (1.0f / D_);
          const float var = fmaxf(qq * (1.0f / D_) - mean * mean, 0.f);
          const float sd = bf16r(sqrtf(bf16r(bf16r(var) + 1e-5f)));
          f32x2 o = {bf16r(mean), __builtin_amdgcn_rcpf(sd)};
          *(f32x2*)(sst + (lw * 64 + i * 16 + fr) * 2) = o;
        }
      }
      if (fr == 0) {
        *(f32x4*)(scs + lw * 32 + fq * 4) = accC[0];
        *(f32x4*)(scs + lw * 32 + 16 + fq * 4) = accC[1];
      }
      WAIT_L0();
      BARRIER();
      __builtin_amdgcn_s_setprio(0);
    }
    const int sub = lw & 1;
    auto stageB = [&](int j, int slot) {
      const int t = t_begin + j;
      char* dst = smem + slot * K2_STAGE + lw * 8192;
      if (lw < 2) {
        if (t < 64) {
          const u16* A = (t < 32 ? ws_re : ws_im) + (size_t)(rowB + sub * 64 + l3) * S_ + (t & 31) * 64;
#pragma unroll
          for (int i = 0; i < 8; ++i) GLDS16(A + i * 8 * S_ + ((i & 1) ? rt_c1 : rt_c0), dst + i * 1024, 0);
        } else {
          const u16* A = h + (size_t)(rowB + sub * 64 + l3) * FP_ + (t - 64) * 64;
#pragma unroll
          for (int i = 0; i < 8; ++i) GLDS16(A + i * 8 * FP_ + ((i & 1) ? rt_c1 : rt_c0), dst + i * 1024, 16);
        }
      } else {
        if (t < 64) {
          const u16* B = ab + (size_t)(t * 64 + l3) * D_ + n0 + sub * 64;
#pragma unroll
          for (int i = 0; i < 8; ++i) GLDS16(B + i * 8 * D_ + ((i & 1) ? kt_c1 : kt_c0), dst + i * 1024, 16);
        } else {
          const int k0 = (t - 64) * 64;
#pragma unroll
          for (int i = 0; i < 8; ++i) {
            const int krow = min(k0 + i * 8 + l3, F_ - 1);
            GLDS16(w_down + krow * D_ + n0 + sub * 64 + ((i & 1) ? kt_c1 : kt_c0), dst + i * 1024, 0);
          }
        }
      }
    };
    const bool early = (lw < 2) ? (t_begin + K2_NST - 2 < 64) : (t_begin >= 64);
    if (early) {
#pragma unroll 1
      for (int s = 0; s < K2_NST - 1; ++s) stageB(s, s);
    }
    BARRIER();
    BARRIER();
    if (!early) {
#pragma unroll 1
      for (int s = 0; s < K2_NST - 1; ++s) stageB(s, s);
    }
    int islot = K2_NST - 1;
#pragma unroll 1
    for (int j = 0; j < nt; ++j) {
      const int rem = nt - 1 - j;
      if (rem >= 3) WAIT_V(24);
      else if (rem == 2) WAIT_V(16);
      else if (rem == 1) WAIT_V(8);
      else WAIT_V(0);
      BARRIER();
      if (j + K2_NST - 1 < nt) stageB(j + K2_NST - 1, islot);
      islot = (islot == K2_NST - 1) ? 0 : islot + 1;
    }
    return;
  }

  u64 nonce = 0;
  if (wid == 4) {
    nonce = __hip_atomic_load((u64*)(ws + WS_SYNC), RLX_AGENT);
    nonce = ((u64)__builtin_amdgcn_readfirstlane((unsigned)(nonce >> 32)) << 32) |
            (u64)__builtin_amdgcn_readfirstlane((unsigned)nonce);
    if (lane == 0) {
      *snonce = nonce;
      __hip_atomic_store(flag_ptr(ws, FL_ARRIVE + bid), flag_tag(nonce, FL_ARRIVE + bid), RLX_AGENT);
    }
  }
  const int wm = wid >> 1, wn = wid & 1;
  using C0 = std::integral_constant<int, 0>;
  using C1 = std::integral_constant<int, 1>;
  char* dummy = smem + L_DUMMY;
  auto warm = [&](int slot) {
    if (lane >= 16) return;
    const int L = slot * 16 + lane;
    if (q < 3) {
      const int ns = min(nt, 64 - t_begin);
      const int row = L / ns, t = t_begin + (L - row * ns);
      if (row < 32) TOUCH((t < 32 ? ws_re : ws_im) + (size_t)(rowB + cbk * 32 + row) * S_ + (t & 31) * 64, dummy);
    } else {
      const int per = (86 - TB3) * 8;
      const int krow = (TB3 - 64) * 64 + (idx >> 2) * per + (L >> 1);
      if (L < 2 * per && krow < F_) TOUCH(w_down + (size_t)krow * D_ + n0 + (L & 1) * 64, dummy);
    }
  };
  if (!has_a) {
#pragma unroll 1
    for (int i = 0; i < 6; ++i) warm(i * 8 + wid);
  }

  if (has_a) {
    f32x4 acc[4][4];
#pragma unroll
    for (int i = 0; i < 4; ++i)
#pragma unroll
      for (int j = 0; j < 4; ++j) acc[i][j] = f32x4{0.f, 0.f, 0.f, 0.f};
    const unsigned a_b0 = sbase + (wm * 64 + fr) * 128 + ch0, a_b1 = sbase + (wm * 64 + fr) * 128 + ch1;
    const unsigned n_b0 = sbase + K1_A + (wn * 64 + fr) * 128 + ch0;
    const unsigned n_b1 = sbase + K1_A + (wn * 64 + fr) * 128 + ch1;
    const unsigned t_p0 = sbase + K1_A + tr_row + 32 * ((wn * 2) ^ tswz);
    const unsigned t_p1 = sbase + K1_A + tr_row + 32 * ((wn * 2 + 1) ^ tswz);

    auto mainloop = [&](auto NKC) {
      constexpr bool NK = decltype(NKC)::value;
      bf16x8 xf0[4], wf0[4], xf1[4], wf1[4];
      auto rdg = [&](auto KSC, bf16x8(&xf)[4], bf16x8(&wf)[4], unsigned so, int g) {
        constexpr int KS = decltype(KSC)::value;
        const unsigned ab_ = (KS ? a_b1 : a_b0) + so;
        switch (g) {
          case 0: xf[0] = lds_rd128<0>(ab_); break;
          case 2: xf[1] = lds_rd128<2048>(ab_); break;
          case 4: xf[2] = lds_rd128<4096>(ab_); break;
          case 6: xf[3] = lds_rd128<6144>(ab_); break;
          default:
            if constexpr (NK) {
              const unsigned nb_ = (KS ? n_b1 : n_b0) + so;
              if (g == 1) wf[0] = lds_rd128<0>(nb_);
              if (g == 3) wf[1] = lds_rd128<2048>(nb_);
              if (g == 5) wf[2] = lds_rd128<4096>(nb_);
              if (g == 7) wf[3] = lds_rd128<6144>(nb_);
            } else {
              if (g == 1) wf[0] = tr_frag<KS * 4096>(t_p0 + so);
              if (g == 3) wf[1] = tr_frag<KS * 4096>(t_p1 + so);
              if (g == 5) wf[2] = tr_frag<KS * 4096 + 8192>(t_p0 + so);
              if (g == 7) wf[3] = tr_frag<KS * 4096 + 8192>(t_p1 + so);
            }
        }
      };
      BARRIER();
#pragma unroll
      for (int g = 0; g < 8; ++g) rdg(C0{}, xf0, wf0, 0u, g);
      WAIT_L0();
      SCHED0();
      unsigned so = 0;
#pragma unroll 1
      for (int t = 0; t < 7; ++t) {
        if (t > 0) warm((t - 1) * 8 + wid);
        mm16(acc, wf0, xf0, [&](int g) { rdg(C1{}, xf1, wf1, so, g); });
        WAIT_L0();
        SCHED0();
        so = (so == 2 * K1_STAGE) ? 0u : so + K1_STAGE;
        BARRIER();
        mm16(acc, wf1, xf1, [&](int g) { rdg(C0{}, xf0, wf0, so, g); });
        WAIT_L0();
        SCHED0();
      }
      mm16(acc, wf0, xf0, [&](int g) { rdg(C1{}, xf1, wf1, so, g); });
      WAIT_L0();
      SCHED0();
      mm16(acc, wf1, xf1, [&](int) {});
    };
    if (nk) mainloop(std::true_type{});
    else mainloop(std::false_type{});
    BARRIER();
    float mb[4], rinv[4];
#pragma unroll
    for (int mi = 0; mi < 4; ++mi) {
      f32x2 v = *(const f32x2*)(sst + (wm * 64 + mi * 16 + fr) * 2);
      mb[mi] = v[0];
      rinv[mi] = v[1];
    }
    f32x4 csv[4];
#pragma unroll
    for (int ni = 0; ni < 4; ++ni) {
      const int ci = nk ? (wn * 64 + ni * 16) : ((ni >> 1) * 64 + wn * 32 + (ni & 1) * 16);
      csv[ni] = *(const f32x4*)(scs + ci + fq * 4);
    }
    char* scr = smem + L_SCR + wid * 2048;
    const __amdgpu_buffer_rsrc_t rsw = __builtin_amdgcn_make_buffer_rsrc(ws, 0, 16u << 20, 0x00020000);
    if (nk) {
      const float sgn = cb < 4 ? 1.0f : -1.0f;
      const unsigned gbase = (unsigned)WS_AB +
          (unsigned)(((cb >> 2) * S_ + rowA + wm * 64 + (lane >> 3)) * D_ + (cb & 3) * 128 + wn * 64 + (lane & 7) * 8) * 2u;
      u32x2 o[4][4];
#pragma unroll
      for (int mi = 0; mi < 4; ++mi) {
        const float r = rinv[mi] * sgn;
#pragma unroll
        for (int ni = 0; ni < 4; ++ni) {
          f32x4 v = (acc[ni][mi] - mb[mi] * csv[ni]) * r;
          o[mi][ni] = u32x2{pack2(v[0], v[1]), pack2(v[2], v[3])};
        }
      }
#pragma unroll
      for (int mi = 0; mi < 4; ++mi) {
#pragma unroll
        for (int ni = 0; ni < 4; ++ni)
          *(u32x2*)(scr + fr * 128 + (((ni * 2 + (fq >> 1)) ^ (fr & 7)) * 16) + (fq & 1) * 8) = o[mi][ni];
#pragma unroll
        for (int hf = 0; hf < 2; ++hf) {
          const int r2 = hf * 8 + (lane >> 3);
          u32x4 v = *(const u32x4*)(scr + r2 * 128 + (((lane & 7) ^ (r2 & 7)) * 16));
          __builtin_amdgcn_raw_buffer_store_b128(v, rsw, gbase + (unsigned)((mi * 16 + hf * 8) * D_ * 2), 0, 16);
        }
      }
    } else {
      const unsigned vmask = (f0 + wn * 32) < F_ ? 0xffffffffu : 0u;
      const unsigned gbase = (unsigned)WS_H +
          (unsigned)((rowA + wm * 64 + (lane >> 2)) * FP_ + f0 + wn * 32 + (lane & 3) * 8) * 2u;
#pragma unroll
      for (int mp = 0; mp < 2; ++mp) {
#pragma unroll
        for (int mj = 0; mj < 2; ++mj) {
          const int mi = mp * 2 + mj;
#pragma unroll
          for (int ni = 0; ni < 2; ++ni) {
            f32x4 g = (acc[ni][mi] - mb[mi] * csv[ni]) * rinv[mi];
            f32x4 u = (acc[ni + 2][mi] - mb[mi] * csv[ni + 2]) * rinv[mi];
            float hv[4];
#pragma unroll
            for (int e = 0; e < 4; ++e) {
              float gl = bf16r(gelu_tanh(bf16r(g[e])));
              hv[e] = gl * bf16r(u[e]);
            }
            u32x2 o = {pack2(hv[0], hv[1]) & vmask, pack2(hv[2], hv[3]) & vmask};
            *(u32x2*)(scr + mj * 1024 + fr * 64 + (((ni * 2 + (fq >> 1)) ^ ((fr >> 1) & 3)) * 16) + (fq & 1) * 8) = o;
          }
        }
        const int r2 = lane >> 2;
#pragma unroll
        for (int mj = 0; mj < 2; ++mj) {
          u32x4 v = *(const u32x4*)(scr + mj * 1024 + r2 * 64 + (((lane & 3) ^ ((r2 >> 1) & 3)) * 16));
          __builtin_amdgcn_raw_buffer_store_b128(v, rsw, gbase + (unsigned)((mp * 2 + mj) * 16 * FP_ * 2), 0, 16);
        }
      }
    }
  }
  WAIT_V(0);
  WAIT_L0();
  BARRIER();
  if (wid > 4) return;

  if (wid == 4) {
    int last = -1;
    auto wait_dep = [&](int j) {
      const int t = t_begin + j;
      const int fi = FL_READY1 + (t < 64 ? (((t >> 5) * 4 + cbk) * 8 + ((t & 31) >> 2)) : ((8 + t - 64) * 8 + (rbB >> 1)));
      if (fi == last) return;
      last = fi;
      const u64 want = flag_tag(nonce, fi);
      u64* p = flag_ptr(ws, fi);
#pragma unroll 1
      for (unsigned spins = 0; spins < SPIN_CAP; ++spins) {
        const u64 v = __hip_atomic_load(p, RLX_AGENT);
        const bool ok = __builtin_amdgcn_readfirstlane((unsigned)(v == want)) != 0;
        if (ok) break;
        __builtin_amdgcn_s_sleep(1);
      }
    };
#pragma unroll 1
    for (int j = 0; j < K2_NST - 1; ++j) wait_dep(j);
    BARRIER();
#pragma unroll 1
    for (int j = 0; j < nt; ++j) {
      if (j + K2_NST - 1 < nt) wait_dep(j + K2_NST - 1);
      BARRIER();
    }
    return;
  }

  if (wid == 0) {
    nonce = *snonce;
    nonce = ((u64)__builtin_amdgcn_readfirstlane((unsigned)(nonce >> 32)) << 32) |
            (u64)__builtin_amdgcn_readfirstlane((unsigned)nonce);
    if (has_a && lane == 0) __hip_atomic_store(flag_ptr(ws, FL_READY1 + nb), flag_tag(nonce, FL_READY1 + nb), RLX_AGENT);
  }
  BARRIER();
  {
    f32x4 acc[4][4];
#pragma unroll
    for (int i = 0; i < 4; ++i)
#pragma unroll
      for (int j = 0; j < 4; ++j) acc[i][j] = f32x4{0.f, 0.f, 0.f, 0.f};
    const unsigned a_base0 = sbase + (wm * 64 + fr) * 128 + ch0;
    const unsigned a_base1 = sbase + (wm * 64 + fr) * 128 + ch1;
    const unsigned b_base = sbase + 16384 + wn * 8192 + tr_row;
    unsigned b_off[4];
#pragma unroll
    for (int ni = 0; ni < 4; ++ni) b_off[ni] = b_base + 32 * (ni ^ tswz);
    bf16x8 af0[4], bf0[4], af1[4], bf1[4];
    auto rdg = [&](auto KSC, bf16x8(&af)[4], bf16x8(&bfr)[4], unsigned so, int g) {
      constexpr int KS = decltype(KSC)::value;
      const unsigned ab_ = (KS ? a_base1 : a_base0) + so;
      switch (g) {
        case 0: af[0] = lds_rd128<0>(ab_); break;
        case 2: af[1] = lds_rd128<2048>(ab_); break;
        case 4: af[2] = lds_rd128<4096>(ab_); break;
        case 6: af[3] = lds_rd128<6144>(ab_); break;
        case 1: bfr[0] = tr_frag<KS * 4096>(b_off[0] + so); break;
        case 3: bfr[1] = tr_frag<KS * 4096>(b_off[1] + so); break;
        case 5: bfr[2] = tr_frag<KS * 4096>(b_off[2] + so); break;
        default: bfr[3] = tr_frag<KS * 4096>(b_off[3] + so); break;
      }
    };
    BARRIER();
#pragma unroll
    for (int g = 0; g < 8; ++g) rdg(C0{}, af0, bf0, 0u, g);
    WAIT_L0();
    SCHED0();
    unsigned so = 0;
#pragma unroll 1
    for (int j = 0; j < nt - 1; ++j) {
      mm16(acc, bf0, af0, [&](int g) { rdg(C1{}, af1, bf1, so, g); });
      WAIT_L0();
      SCHED0();
      so = (so == (K2_NST - 1) * K2_STAGE) ? 0u : so + K2_STAGE;
      BARRIER();
      mm16(acc, bf1, af1, [&](int g) { rdg(C0{}, af0, bf0, so, g); });
      WAIT_L0();
      SCHED0();
    }
    mm16(acc, bf0, af0, [&](int g) { rdg(C1{}, af1, bf1, so, g); });
    WAIT_L0();
    SCHED0();
    mm16(acc, bf1, af1, [&](int) {});
    const __amdgpu_buffer_rsrc_t rs = __builtin_amdgcn_make_buffer_rsrc(ws + WS_SLAB, 0, 8u << 20, 0x00020000);
    f32x4 own[4];
#pragma unroll
    for (int mi = 0; mi < 4; ++mi) {
      if (mi == q) {
#pragma unroll
        for (int ni = 0; ni < 4; ++ni) own[ni] = acc[ni][mi];
      } else {
        const unsigned base = (unsigned)((((tile * 4 + mi) * 4 + q) * 4 + wid) * 2) * 1024u + lane * 16u;
#pragma unroll
        for (int np = 0; np < 2; ++np) {
          const f32x4 a0 = acc[2 * np][mi], a1 = acc[2 * np + 1][mi];
          u32x4 v = {pack2(a0[0], a0[1]), pack2(a0[2], a0[3]), pack2(a1[0], a1[1]), pack2(a1[2], a1[3])};
          __builtin_amdgcn_raw_buffer_store_b128(v, rs, base + np * 1024u, 0, 16);
        }
      }
    }
    const size_t orow = (size_t)(rowB + wm * 64 + q * 16 + fr) * D_ + n0 + wn * 64 + fq * 4;
    u32x2 xv[4];
#pragma unroll
    for (int ni = 0; ni < 4; ++ni) xv[ni] = *(const u32x2*)(x + orow + ni * 16);
    WAIT_V(0);
    BARRIER();
    if (wid == 0) {
      if (lane == 0)
        __hip_atomic_store(flag_ptr(ws, FL_READY2 + tile * 4 + q), flag_tag(nonce, FL_READY2 + tile * 4 + q), RLX_AGENT);
      const int fi = FL_READY2 + tile * 4 + (lane & 3);
      const u64 want = flag_tag(nonce, fi);
      u64* p = flag_ptr(ws, fi);
#pragma unroll 1
      for (unsigned spins = 0; spins < SPIN_CAP; ++spins) {
        const u64 v = __hip_atomic_load(p, RLX_AGENT);
        if (__all(v == want || (lane & 3) == q)) break;
        __builtin_amdgcn_s_sleep(1);
      }
    }
    BARRIER();
    u32x4 pv[3][2];
#pragma unroll
    for (int sidx = 0; sidx < 3; ++sidx) {
      const int src = sidx + (sidx >= q ? 1 : 0);
      const unsigned base = (unsigned)((((tile * 4 + q) * 4 + src) * 4 + wid) * 2) * 1024u + lane * 16u;
#pragma unroll
      for (int np = 0; np < 2; ++np) pv[sidx][np] = __builtin_amdgcn_raw_buffer_load_b128(rs, base + np * 1024u, 0, 16);
    }
#pragma unroll
    for (int ni = 0; ni < 4; ++ni) {
      f32x4 v = own[ni];
#pragma unroll
      for (int sidx = 0; sidx < 3; ++sidx) {
        const unsigned w0 = pv[sidx][ni >> 1][(ni & 1) * 2], w1 = pv[sidx][ni >> 1][(ni & 1) * 2 + 1];
        v[0] += bflo(w0); v[1] += bfhi(w0); v[2] += bflo(w1); v[3] += bfhi(w1);
      }
      u32x2 o = {pack2(v[0] + bflo(xv[ni][0]), v[1] + bfhi(xv[ni][0])), pack2(v[2] + bflo(xv[ni][1]), v[3] + bfhi(xv[ni][1]))};
      *(u32x2*)(out + orow + ni * 16) = o;
    }
  }
  if (bid == 0 && wid == 0) {
#pragma unroll 1
    for (unsigned spins = 0; spins < SPIN_CAP; ++spins) {
      bool ok = true;
#pragma unroll
      for (int i = 0; i < 4; ++i) {
        const int fi = FL_ARRIVE + i * 64 + lane;
        ok &= __hip_atomic_load(flag_ptr(ws, fi), RLX_AGENT) == flag_tag(nonce, fi);
      }
      if (__all(ok)) break;
      __builtin_amdgcn_s_sleep(1);
    }
    if (lane == 0) __hip_atomic_store((u64*)(ws + WS_SYNC), nonce + 1, RLX_AGENT);
  }
}

extern "C" void kernel_launch(void* const* d_in, const int* in_sizes, int n_in, void* d_out, int out_size,
                              void* d_ws, size_t ws_size, hipStream_t stream) {
  fused_kernel<<<256, 768, 0, stream>>>((const u16*)d_in[0], (const u16*)d_in[1], (const u16*)d_in[2],
                                        (const u16*)d_in[3], (const u16*)d_in[4], (const u16*)d_in[6],
                                        (const u16*)d_in[7], (const u16*)d_in[8], (u16*)d_out, (char*)d_ws);
}
```

```cpp
#include <hip/hip_runtime.h>
#include <hip/hip_bf16.h>
#include <stdint.h>
#include <type_traits>

typedef unsigned short u16;
typedef unsigned long long u64;
typedef short bf16x8 __attribute__((ext_vector_type(8)));
typedef short bf16x4 __attribute__((ext_vector_type(4)));
typedef float f32x4 __attribute__((ext_vector_type(4)));
typedef float f32x2 __attribute__((ext_vector_type(2)));
typedef unsigned u32x4 __attribute__((ext_vector_type(4)));
typedef unsigned u32x2 __attribute__((ext_vector_type(2)));

#define TB1 23
#define TB2 45
#define TB3 67
constexpr int S_ = 2048, D_ = 512, F_ = 1376, FP_ = 1408;

#define LDS_AS __attribute__((address_space(3)))
#define GLDS16(gptr, ldsptr, aux) \
  __builtin_amdgcn_global_load_lds((const unsigned*)(gptr), (unsigned*)(ldsptr), 16, 0, aux)
#define WAIT_V(n) asm volatile("s_waitcnt vmcnt(" #n ")" ::: "memory")
#define BARRIER() asm volatile("s_barrier" ::: "memory")
#define WAIT_L0() asm volatile("s_waitcnt lgkmcnt(0)" ::: "memory")
#define SCHED0() __builtin_amdgcn_sched_barrier(0)
#define RLX_AGENT __ATOMIC_RELAXED, __HIP_MEMORY_SCOPE_AGENT

__device__ __forceinline__ float bf16r(float x) { return __bfloat162float(__float2bfloat16(x)); }
typedef __bf16 bf2_t __attribute__((ext_vector_type(2)));
__device__ __forceinline__ unsigned pack2(float lo, float hi) {
  f32x2 v = {lo, hi};
  return __builtin_bit_cast(unsigned, __builtin_convertvector(v, bf2_t));
}
__device__ __forceinline__ float bflo(unsigned v) { return __builtin_bit_cast(float, v << 16); }
__device__ __forceinline__ float bfhi(unsigned v) { return __builtin_bit_cast(float, v & 0xffff0000u); }

template <int OFF>
__device__ __forceinline__ bf16x8 tr_frag(unsigned addr) {
  bf16x4 a, b;
  asm volatile("ds_read_b64_tr_b16 %0, %2 offset:%3\n\tds_read_b64_tr_b16 %1, %2 offset:%4"
               : "=&v"(a), "=&v"(b)
               : "v"(addr), "i"(OFF), "i"(OFF + 512));
  return __builtin_shufflevector(a, b, 0, 1, 2, 3, 4, 5, 6, 7);
}
template <int OFF>
__device__ __forceinline__ bf16x8 lds_rd128(unsigned addr) {
  bf16x8 a;
  asm volatile("ds_read_b128 %0, %1 offset:%2" : "=&v"(a) : "v"(addr), "i"(OFF));
  return a;
}
__device__ __forceinline__ unsigned lds_addr(const void* p) { return (unsigned)(uintptr_t)(const LDS_AS char*)p; }
__device__ __forceinline__ float gelu_tanh(float g) {
  const float c1 = -2.0f * 0.7978845608028654f * 1.4426950408889634f;
  const float c2 = c1 * 0.044715f;
  float w = g * __builtin_fmaf(g * g, c2, c1);
  float e = __builtin_amdgcn_exp2f(w);
  return g * __builtin_amdgcn_rcpf(1.0f + e);
}
#define TOUCH(gptr, ldsptr) __builtin_amdgcn_global_load_lds((const unsigned*)(gptr), (unsigned*)(ldsptr), 4, 0, 0)
template <class RD>
__device__ __forceinline__ void mm16(f32x4 (&acc)[4][4], const bf16x8 (&wf)[4], const bf16x8 (&xf)[4], RD rd) {
#pragma unroll
  for (int i = 0; i < 16; ++i) {
    const int ni = i >> 2, mi = i & 3;
    acc[ni][mi] = __builtin_amdgcn_mfma_f32_16x16x32_bf16(wf[ni], xf[mi], acc[ni][mi], 0, 0, 0);
    if (i < 8) {
      SCHED0();
      rd(i);
      SCHED0();
    }
  }
  SCHED0();
}

constexpr size_t WS_AB = 0;
constexpr size_t WS_H = 4u << 20;
constexpr size_t WS_SLAB = 16u << 20;
constexpr size_t WS_SYNC = 32u << 20;
constexpr int FL_ARRIVE = 0, FL_READY1 = 256, FL_READY2 = 512;
__device__ __forceinline__ u64* flag_ptr(char* ws, int idx) { return (u64*)(ws + WS_SYNC + 4096 + (size_t)idx * 64); }
__device__ __forceinline__ u64 flag_tag(u64 nonce, int idx) { return nonce + (u64)(idx + 1) * 0x9E3779B97F4A7C15ull; }
constexpr unsigned SPIN_CAP = 1u << 22;

constexpr int K1_A = 32768, K1_B = 16384, K1_STAGE = K1_A + K1_B;
constexpr int K2_STAGE = 32768, K2_NST = 5;
constexpr int L_SCR = 131072;
constexpr int L_SST = 147456, L_SCS = L_SST + 2048, L_NONCE = L_SCS + 512, L_DUMMY = L_NONCE + 256;

__global__ __launch_bounds__(768, 3) void fused_kernel(const u16* __restrict__ x, const u16* __restrict__ ws_re,
                                                       const u16* __restrict__ ws_im, const u16* __restrict__ wd_re,
                                                       const u16* __restrict__ wd_im, const u16* __restrict__ w_gate,
                                                       const u16* __restrict__ w_up, const u16* __restrict__ w_down,
                                                       u16* __restrict__ out, char* ws) {
  __shared__ __attribute__((aligned(1024))) char smem[K2_NST * K2_STAGE];
  const int tid = threadIdx.x, wid = __builtin_amdgcn_readfirstlane(tid >> 6), lane = tid & 63;
  const int fr = lane & 15, fq = lane >> 4;
  const int bid = blockIdx.x;
  u16* ab = (u16*)(ws + WS_AB);
  u16* h = (u16*)(ws + WS_H);

  const bool has_a = bid < 240;
  const int nb = (bid & 7) * 30 + (bid >> 3);
  const int cb = nb >> 3, rbA = nb & 7;
  const bool nk = cb < 8;
  const int rowA = rbA * 256;
  const int f0 = (cb - 8) * 64;
  const int idx = bid >> 3;
  const int q = (bid & 7) >> 1;
  const int rbB = (bid & 1) * 8 + (idx >> 2), cbk = idx & 3;
  const int rowB = rbB * 128, n0 = cbk * 128, tile = rbB * 4 + cbk;
  const int t_begin = q == 0 ? 0 : q == 1 ? TB1 : q == 2 ? TB2 : TB3;
  const int nt = (q == 0 ? TB1 : q == 1 ? TB2 : q == 2 ? TB3 : 86) - t_begin;

  const int sw = (fr >> 1) & 7;
  const int ch0 = ((fq) ^ sw) * 16, ch1 = ((4 + fq) ^ sw) * 16;
  const int tq = (lane >> 2) & 3, tp = lane & 3;
  const int tswz = ((tq >> 1) & 1) | ((fq & 1) << 1);
  const int tr_row = (8 * fq + tq) * 128 + 8 * tp;
  float* sst = (float*)(smem + L_SST);
  float* scs = (float*)(smem + L_SCS);
  u64* snonce = (u64*)(smem + L_NONCE);
  const unsigned sbase = lds_addr(smem);

  if (wid >= 8) {
    const int lw = wid - 8;
    const int l3 = lane >> 3, l7 = lane & 7;
    const int rt_c0 = (l7 ^ (fq & 7)) * 8, rt_c1 = (l7 ^ ((fq + 4) & 7)) * 8;
    const int kt_c0 = (l7 ^ ((fq & 1) << 1)) * 8, kt_c1 = (l7 ^ (((fq & 1) | 2) << 1)) * 8;
    if (has_a) {
      __builtin_amdgcn_s_setprio(2);
      const u16* xa0 = x + (size_t)(rowA + lw * 64 + l3) * D_ + rt_c0;
      const u16* xa1 = x + (size_t)(rowA + lw * 64 + 8 + l3) * D_ + rt_c1;
      const u16 *wb0, *wb1;
      int wstep_j, wstep_t;
      if (nk) {
        const u16* wnk = (cb < 4 ? wd_re : wd_im) + (size_t)((cb & 3) * 128 + lw * 32 + l3) * D_;
        wb0 = wnk + rt_c0;
        wb1 = wnk + 8 * D_ + rt_c1;
        wstep_j = 16 * D_;
        wstep_t = 64;
      } else {
        const u16* w = (lw < 2) ? w_gate : w_up;
        int c0 = f0 + kt_c0, c1 = f0 + kt_c1;
        if (c0 >= F_) c0 -= 32;
        if (c1 >= F_) c1 -= 32;
        const u16* wk = w + (size_t)((lw & 1) * 32 + l3) * F_;
        wb0 = wk + c0;
        wb1 = wk + 8 * F_ + c1;
        wstep_j = 16 * F_;
        wstep_t = 64 * F_;
      }
      auto stage = [&](int tj, int buf) {
        const int t = (tj + cb) & 7;
        char* sa = smem + buf * K1_STAGE + lw * 8192;
        char* sb = smem + buf * K1_STAGE + K1_A + lw * 4096;
#pragma unroll
        for (int i = 0; i < 4; ++i) {
          GLDS16(xa0 + i * 16 * D_ + t * 64, sa + i * 2048, 0);
          GLDS16(xa1 + i * 16 * D_ + t * 64, sa + i * 2048 + 1024, 0);
        }
#pragma unroll
        for (int i = 0; i < 2; ++i) {
          GLDS16(wb0 + i * wstep_j + t * wstep_t, sb + i * 2048, 0);
          GLDS16(wb1 + i * wstep_j + t * wstep_t, sb + i * 2048 + 1024, 0);
        }
      };
#pragma unroll 1
      for (int t = 0; t < 2; ++t) stage(t, t);
      f32x4 accS[4], accG[4], accC[2];
#pragma unroll
      for (int i = 0; i < 4; ++i) {
        accS[i] = f32x4{0.f, 0.f, 0.f, 0.f};
        accG[i] = f32x4{0.f, 0.f, 0.f, 0.f};
      }
      accC[0] = f32x4{0.f, 0.f, 0.f, 0.f};
      accC[1] = f32x4{0.f, 0.f, 0.f, 0.f};
      const short ONE = 0x3f80;
      const bf16x8 ones = {ONE, ONE, ONE, ONE, ONE, ONE, ONE, ONE};
      const unsigned la0 = sbase + (lw * 64 + fr) * 128 + ch0;
      const unsigned la1 = sbase + (lw * 64 + fr) * 128 + ch1;
      const unsigned lb0 = sbase + K1_A + (lw * 32 + fr) * 128 + ch0;
      const unsigned lb1 = sbase + K1_A + (lw * 32 + fr) * 128 + ch1;
      const unsigned lt0 = sbase + K1_A + (lw >> 1) * 8192 + tr_row + 32 * (((lw & 1) * 2) ^ tswz);
      const unsigned lt1 = sbase + K1_A + (lw >> 1) * 8192 + tr_row + 32 * (((lw & 1) * 2 + 1) ^ tswz);
      int ib = 2, buf = 0;
#pragma unroll 1
      for (int t = 0; t < 8; ++t) {
        if (t < 7) WAIT_V(12); else WAIT_V(0);
        BARRIER();
        const unsigned so = buf * K1_STAGE;
        bf16x8 xf[8], wf[4];
        xf[0] = lds_rd128<0>(la0 + so); xf[1] = lds_rd128<2048>(la0 + so);
        xf[2] = lds_rd128<4096>(la0 + so); xf[3] = lds_rd128<6144>(la0 + so);
        xf[4] = lds_rd128<0>(la1 + so); xf[5] = lds_rd128<2048>(la1 + so);
        xf[6] = lds_rd128<4096>(la1 + so); xf[7] = lds_rd128<6144>(la1 + so);
        if (nk) {
          wf[0] = lds_rd128<0>(lb0 + so); wf[1] = lds_rd128<2048>(lb0 + so);
          wf[2] = lds_rd128<0>(lb1 + so); wf[3] = lds_rd128<2048>(lb1 + so);
        } else {
          wf[0] = tr_frag<0>(lt0 + so); wf[1] = tr_frag<0>(lt1 + so);
          wf[2] = tr_frag<4096>(lt0 + so); wf[3] = tr_frag<4096>(lt1 + so);
        }
        if (t + 2 < 8) stage(t + 2, ib);
        ib = (ib == 2) ? 0 : ib + 1;
        WAIT_L0();
        SCHED0();
#pragma unroll
        for (int i = 0; i < 8; ++i) {
          accS[i & 3] = __builtin_amdgcn_mfma_f32_16x16x32_bf16(ones, xf[i], accS[i & 3], 0, 0, 0);
          accG[i & 3] = __builtin_amdgcn_mfma_f32_16x16x32_bf16(xf[i], xf[i], accG[i & 3], 0, 0, 0);
        }
#pragma unroll
        for (int i = 0; i < 4; ++i)
          accC[i & 1] = __builtin_amdgcn_mfma_f32_16x16x32_bf16(wf[i], ones, accC[i & 1], 0, 0, 0);
        buf = (buf == 2) ? 0 : buf + 1;
      }
      if (fq == (fr >> 2)) {
#pragma unroll
        for (int i = 0; i < 4; ++i) {
          const int r = fr & 3;
          const float qq = r == 0 ? accG[i][0] : r == 1 ? accG[i][1] : r == 2 ? accG[i][2] : accG[i][3];
          const float mean = accS[i][0] * (1.0f / D_);
          const float var = fmaxf(qq * (1.0f / D_) - mean * mean, 0.f);
          const float sd = bf16r(sqrtf(bf16r(bf16r(var) + 1e-5f)));
          f32x2 o = {bf16r(mean), __builtin_amdgcn_rcpf(sd)};
          *(f32x2*)(sst + (lw * 64 + i * 16 + fr) * 2) = o;
        }
      }
      if (fr == 0) {
        *(f32x4*)(scs + lw * 32 + fq * 4) = accC[0];
        *(f32x4*)(scs + lw * 32 + 16 + fq * 4) = accC[1];
      }
      WAIT_L0();
      BARRIER();
      __builtin_amdgcn_s_setprio(0);
    }
    const int sub = lw & 1;
    auto stageB = [&](int j, int slot) {
      const int t = t_begin + j;
      char* dst = smem + slot * K2_STAGE + lw * 8192;
      if (lw < 2) {
        if (t < 64) {
          const u16* A = (t < 32 ? ws_re : ws_im) + (size_t)(rowB + sub * 64 + l3) * S_ + (t & 31) * 64;
#pragma unroll
          for (int i = 0; i < 8; ++i) GLDS16(A + i * 8 * S_ + ((i & 1) ? rt_c1 : rt_c0), dst + i * 1024, 0);
        } else {
          const u16* A = h + (size_t)(rowB + sub * 64 + l3) * FP_ + (t - 64) * 64;
#pragma unroll
          for (int i = 0; i < 8; ++i) GLDS16(A + i * 8 * FP_ + ((i & 1) ? rt_c1 : rt_c0), dst + i * 1024, 16);
        }
      } else {
        if (t < 64) {
          const u16* B = ab + (size_t)(t * 64 + l3) * D_ + n0 + sub * 64;
#pragma unroll
          for (int i = 0; i < 8; ++i) GLDS16(B + i * 8 * D_ + ((i & 1) ? kt_c1 : kt_c0), dst + i * 1024, 16);
        } else {
          const int k0 = (t - 64) * 64;
#pragma unroll
          for (int i = 0; i < 8; ++i) {
            const int krow = min(k0 + i * 8 + l3, F_ - 1);
            GLDS16(w_down + krow * D_ + n0 + sub * 64 + ((i & 1) ? kt_c1 : kt_c0), dst + i * 1024, 0);
          }
        }
      }
    };
    const bool early = (lw < 2) ? (t_begin + K2_NST - 2 < 64) : (t_begin >= 64);
    if (early) {
#pragma unroll 1
      for (int s = 0; s < K2_NST - 1; ++s) stageB(s, s);
    }
    BARRIER();
    BARRIER();
    if (!early) {
#pragma unroll 1
      for (int s = 0; s < K2_NST - 1; ++s) stageB(s, s);
    }
    int islot = K2_NST - 1;
#pragma unroll 1
    for (int j = 0; j < nt; ++j) {
      const int rem = nt - 1 - j;
      if (rem >= 3) WAIT_V(24);
      else if (rem == 2) WAIT_V(16);
      else if (rem == 1) WAIT_V(8);
      else WAIT_V(0);
      BARRIER();
      if (j + K2_NST - 1 < nt) stageB(j + K2_NST - 1, islot);
      islot = (islot == K2_NST - 1) ? 0 : islot + 1;
    }
    return;
  }

  u64 nonce = 0;
  if (wid == 4) {
    nonce = __hip_atomic_load((u64*)(ws + WS_SYNC), RLX_AGENT);
    nonce = ((u64)__builtin_amdgcn_readfirstlane((unsigned)(nonce >> 32)) << 32) |
            (u64)__builtin_amdgcn_readfirstlane((unsigned)nonce);
    if (lane == 0) {
      *snonce = nonce;
      __hip_atomic_store(flag_ptr(ws, FL_ARRIVE + bid), flag_tag(nonce, FL_ARRIVE + bid), RLX_AGENT);
    }
  }
  const int wm = wid >> 1, wn = wid & 1;
  using C0 = std::integral_constant<int, 0>;
  using C1 = std::integral_constant<int, 1>;
  char* dummy = smem + L_DUMMY;
  auto warm = [&](int slot) {
    if (lane >= 16) return;
    const int L = slot * 16 + lane;
    if (q < 3) {
      const int ns = min(nt, 64 - t_begin);
      const int row = L / ns, t = t_begin + (L - row * ns);
      if (row < 32) TOUCH((t < 32 ? ws_re : ws_im) + (size_t)(rowB + cbk * 32 + row) * S_ + (t & 31) * 64, dummy);
    } else {
      const int per = (86 - TB3) * 8;
      const int krow = (TB3 - 64) * 64 + (idx >> 2) * per + (L >> 1);
      if (L < 2 * per && krow < F_) TOUCH(w_down + (size_t)krow * D_ + n0 + (L & 1) * 64, dummy);
    }
  };
  if (!has_a) {
#pragma unroll 1
    for (int i = 0; i < 6; ++i) warm(i * 8 + wid);
  }

  if (has_a) {
    f32x4 acc[4][4];
#pragma unroll
    for (int i = 0; i < 4; ++i)
#pragma unroll
      for (int j = 0; j < 4; ++j) acc[i][j] = f32x4{0.f, 0.f, 0.f, 0.f};
    const unsigned a_b0 = sbase + (wm * 64 + fr) * 128 + ch0, a_b1 = sbase + (wm * 64 + fr) * 128 + ch1;
    const unsigned n_b0 = sbase + K1_A + (wn * 64 + fr) * 128 + ch0;
    const unsigned n_b1 = sbase + K1_A + (wn * 64 + fr) * 128 + ch1;
    const unsigned t_p0 = sbase + K1_A + tr_row + 32 * ((wn * 2) ^ tswz);
    const unsigned t_p1 = sbase + K1_A + tr_row + 32 * ((wn * 2 + 1) ^ tswz);

    auto mainloop = [&](auto NKC) {
      constexpr bool NK = decltype(NKC)::value;
      bf16x8 xf0[4], wf0[4], xf1[4], wf1[4];
      auto rdg = [&](auto KSC, bf16x8(&xf)[4], bf16x8(&wf)[4], unsigned so, int g) {
        constexpr int KS = decltype(KSC)::value;
        const unsigned ab_ = (KS ? a_b1 : a_b0) + so;
        switch (g) {
          case 0: xf[0] = lds_rd128<0>(ab_); break;
          case 2: xf[1] = lds_rd128<2048>(ab_); break;
          case 4: xf[2] = lds_rd128<4096>(ab_); break;
          case 6: xf[3] = lds_rd128<6144>(ab_); break;
          default:
            if constexpr (NK) {
              const unsigned nb_ = (KS ? n_b1 : n_b0) + so;
              if (g == 1) wf[0] = lds_rd128<0>(nb_);
              if (g == 3) wf[1] = lds_rd128<2048>(nb_);
              if (g == 5) wf[2] = lds_rd128<4096>(nb_);
              if (g == 7) wf[3] = lds_rd128<6144>(nb_);
            } else {
              if (g == 1) wf[0] = tr_frag<KS * 4096>(t_p0 + so);
              if (g == 3) wf[1] = tr_frag<KS * 4096>(t_p1 + so);
              if (g == 5) wf[2] = tr_frag<KS * 4096 + 8192>(t_p0 + so);
              if (g == 7) wf[3] = tr_frag<KS * 4096 + 8192>(t_p1 + so);
            }
        }
      };
      BARRIER();
#pragma unroll
      for (int g = 0; g < 8; ++g) rdg(C0{}, xf0, wf0, 0u, g);
      WAIT_L0();
      SCHED0();
      unsigned so = 0;
#pragma unroll 1
      for (int t = 0; t < 7; ++t) {
        if (t > 0) warm((t - 1) * 8 + wid);
        mm16(acc, wf0, xf0, [&](int g) { rdg(C1{}, xf1, wf1, so, g); });
        WAIT_L0();
        SCHED0();
        so = (so == 2 * K1_STAGE) ? 0u : so + K1_STAGE;
        BARRIER();
        mm16(acc, wf1, xf1, [&](int g) { rdg(C0{}, xf0, wf0, so, g); });
        WAIT_L0();
        SCHED0();
      }
      mm16(acc, wf0, xf0, [&](int g) { rdg(C1{}, xf1, wf1, so, g); });
      WAIT_L0();
      SCHED0();
      mm16(acc, wf1, xf1, [&](int) {});
    };
    if (nk) mainloop(std::true_type{});
    else mainloop(std::false_type{});
    BARRIER();
    float mb[4], rinv[4];
#pragma unroll
    for (int mi = 0; mi < 4; ++mi) {
      f32x2 v = *(const f32x2*)(sst + (wm * 64 + mi * 16 + fr) * 2);
      mb[mi] = v[0];
      rinv[mi] = v[1];
    }
    f32x4 csv[4];
#pragma unroll
    for (int ni = 0; ni < 4; ++ni) {
      const int ci = nk ? (wn * 64 + ni * 16) : ((ni >> 1) * 64 + wn * 32 + (ni & 1) * 16);
      csv[ni] = *(const f32x4*)(scs + ci + fq * 4);
    }
    char* scr = smem + L_SCR + wid * 2048;
    const __amdgpu_buffer_rsrc_t rsw = __builtin_amdgcn_make_buffer_rsrc(ws, 0, 16u << 20, 0x00020000);
    if (nk) {
      const float sgn = cb < 4 ? 1.0f : -1.0f;
      const unsigned gbase = (unsigned)WS_AB +
          (unsigned)(((cb >> 2) * S_ + rowA + wm * 64 + (lane >> 3)) * D_ + (cb & 3) * 128 + wn * 64 + (lane & 7) * 8) * 2u;
      u32x2 o[4][4];
#pragma unroll
      for (int mi = 0; mi < 4; ++mi) {
        const float r = rinv[mi] * sgn;
#pragma unroll
        for (int ni = 0; ni < 4; ++ni) {
          f32x4 v = (acc[ni][mi] - mb[mi] * csv[ni]) * r;
          o[mi][ni] = u32x2{pack2(v[0], v[1]), pack2(v[2], v[3])};
        }
      }
#pragma unroll
      for (int mi = 0; mi < 4; ++mi) {
#pragma unroll
        for (int ni = 0; ni < 4; ++ni)
          *(u32x2*)(scr + fr * 128 + (((ni * 2 + (fq >> 1)) ^ (fr & 7)) * 16) + (fq & 1) * 8) = o[mi][ni];
#pragma unroll
        for (int hf = 0; hf < 2; ++hf) {
          const int r2 = hf * 8 + (lane >> 3);
          u32x4 v = *(const u32x4*)(scr + r2 * 128 + (((lane & 7) ^ (r2 & 7)) * 16));
          __builtin_amdgcn_raw_buffer_store_b128(v, rsw, gbase + (unsigned)((mi * 16 + hf * 8) * D_ * 2), 0, 16);
        }
      }
    } else {
      const unsigned vmask = (f0 + wn * 32) < F_ ? 0xffffffffu : 0u;
      const unsigned gbase = (unsigned)WS_H +
          (unsigned)((rowA + wm * 64 + (lane >> 2)) * FP_ + f0 + wn * 32 + (lane & 3) * 8) * 2u;
#pragma unroll
      for (int mp = 0; mp < 2; ++mp) {
#pragma unroll
        for (int mj = 0; mj < 2; ++mj) {
          const int mi = mp * 2 + mj;
#pragma unroll
          for (int ni = 0; ni < 2; ++ni) {
            f32x4 g = (acc[ni][mi] - mb[mi] * csv[ni]) * rinv[mi];
            f32x4 u = (acc[ni + 2][mi] - mb[mi] * csv[ni + 2]) * rinv[mi];
            float hv[4];
#pragma unroll
            for (int e = 0; e < 4; ++e) hv[e] = gelu_tanh(g[e]) * u[e];
            u32x2 o = {pack2(hv[0], hv[1]) & vmask, pack2(hv[2], hv[3]) & vmask};
            *(u32x2*)(scr + mj * 1024 + fr * 64 + (((ni * 2 + (fq >> 1)) ^ ((fr >> 1) & 3)) * 16) + (fq & 1) * 8) = o;
          }
        }
        const int r2 = lane >> 2;
#pragma unroll
        for (int mj = 0; mj < 2; ++mj) {
          u32x4 v = *(const u32x4*)(scr + mj * 1024 + r2 * 64 + (((lane & 3) ^ ((r2 >> 1) & 3)) * 16));
          __builtin_amdgcn_raw_buffer_store_b128(v, rsw, gbase + (unsigned)((mp * 2 + mj) * 16 * FP_ * 2), 0, 16);
        }
      }
    }
  }
  WAIT_V(0);
  WAIT_L0();
  BARRIER();
  if (wid > 4) return;

  if (wid == 4) {
    int last = -1;
    auto wait_dep = [&](int j) {
      const int t = t_begin + j;
      const int fi = FL_READY1 + (t < 64 ? (((t >> 5) * 4 + cbk) * 8 + ((t & 31) >> 2)) : ((8 + t - 64) * 8 + (rbB >> 1)));
      if (fi == last) return;
      last = fi;
      const u64 want = flag_tag(nonce, fi);
      u64* p = flag_ptr(ws, fi);
#pragma unroll 1
      for (unsigned spins = 0; spins < SPIN_CAP; ++spins) {
        const u64 v = __hip_atomic_load(p, RLX_AGENT);
        const bool ok = __builtin_amdgcn_readfirstlane((unsigned)(v == want)) != 0;
        if (ok) break;
        __builtin_amdgcn_s_sleep(1);
      }
    };
#pragma unroll 1
    for (int j = 0; j < K2_NST - 1; ++j) wait_dep(j);
    BARRIER();
#pragma unroll 1
    for (int j = 0; j < nt; ++j) {
      if (j + K2_NST - 1 < nt) wait_dep(j + K2_NST - 1);
      BARRIER();
    }
    return;
  }

  if (wid == 0) {
    nonce = *snonce;
    nonce = ((u64)__builtin_amdgcn_readfirstlane((unsigned)(nonce >> 32)) << 32) |
            (u64)__builtin_amdgcn_readfirstlane((unsigned)nonce);
    if (has_a && lane == 0) __hip_atomic_store(flag_ptr(ws, FL_READY1 + nb), flag_tag(nonce, FL_READY1 + nb), RLX_AGENT);
  }
  BARRIER();
  {
    f32x4 acc[4][4];
#pragma unroll
    for (int i = 0; i < 4; ++i)
#pragma unroll
      for (int j = 0; j < 4; ++j) acc[i][j] = f32x4{0.f, 0.f, 0.f, 0.f};
    const unsigned a_base0 = sbase + (wm * 64 + fr) * 128 + ch0;
    const unsigned a_base1 = sbase + (wm * 64 + fr) * 128 + ch1;
    const unsigned b_base = sbase + 16384 + wn * 8192 + tr_row;
    unsigned b_off[4];
#pragma unroll
    for (int ni = 0; ni < 4; ++ni) b_off[ni] = b_base + 32 * (ni ^ tswz);
    bf16x8 af0[4], bf0[4], af1[4], bf1[4];
    auto rdg = [&](auto KSC, bf16x8(&af)[4], bf16x8(&bfr)[4], unsigned so, int g) {
      constexpr int KS = decltype(KSC)::value;
      const unsigned ab_ = (KS ? a_base1 : a_base0) + so;
      switch (g) {
        case 0: af[0] = lds_rd128<0>(ab_); break;
        case 2: af[1] = lds_rd128<2048>(ab_); break;
        case 4: af[2] = lds_rd128<4096>(ab_); break;
        case 6: af[3] = lds_rd128<6144>(ab_); break;
        case 1: bfr[0] = tr_frag<KS * 4096>(b_off[0] + so); break;
        case 3: bfr[1] = tr_frag<KS * 4096>(b_off[1] + so); break;
        case 5: bfr[2] = tr_frag<KS * 4096>(b_off[2] + so); break;
        default: bfr[3] = tr_frag<KS * 4096>(b_off[3] + so); break;
      }
    };
    BARRIER();
#pragma unroll
    for (int g = 0; g < 8; ++g) rdg(C0{}, af0, bf0, 0u, g);
    WAIT_L0();
    SCHED0();
    unsigned so = 0;
#pragma unroll 1
    for (int j = 0; j < nt - 1; ++j) {
      mm16(acc, bf0, af0, [&](int g) { rdg(C1{}, af1, bf1, so, g); });
      WAIT_L0();
      SCHED0();
      so = (so == (K2_NST - 1) * K2_STAGE) ? 0u : so + K2_STAGE;
      BARRIER();
      mm16(acc, bf1, af1, [&](int g) { rdg(C0{}, af0, bf0, so, g); });
      WAIT_L0();
      SCHED0();
    }
    mm16(acc, bf0, af0, [&](int g) { rdg(C1{}, af1, bf1, so, g); });
    WAIT_L0();
    SCHED0();
    mm16(acc, bf1, af1, [&](int) {});
    const __amdgpu_buffer_rsrc_t rs = __builtin_amdgcn_make_buffer_rsrc(ws + WS_SLAB, 0, 8u << 20, 0x00020000);
    f32x4 own[4];
#pragma unroll
    for (int mi = 0; mi < 4; ++mi) {
      if (mi == q) {
#pragma unroll
        for (int ni = 0; ni < 4; ++ni) own[ni] = acc[ni][mi];
      } else {
        const unsigned base = (unsigned)((((tile * 4 + mi) * 4 + q) * 4 + wid) * 2) * 1024u + lane * 16u;
#pragma unroll
        for (int np = 0; np < 2; ++np) {
          const f32x4 a0 = acc[2 * np][mi], a1 = acc[2 * np + 1][mi];
          u32x4 v = {pack2(a0[0], a0[1]), pack2(a0[2], a0[3]), pack2(a1[0], a1[1]), pack2(a1[2], a1[3])};
          __builtin_amdgcn_raw_buffer_store_b128(v, rs, base + np * 1024u, 0, 16);
        }
      }
    }
    const size_t orow = (size_t)(rowB + wm * 64 + q * 16 + fr) * D_ + n0 + wn * 64 + fq * 4;
    u32x2 xv[4];
#pragma unroll
    for (int ni = 0; ni < 4; ++ni) xv[ni] = *(const u32x2*)(x + orow + ni * 16);
    WAIT_V(0);
    BARRIER();
    if (wid == 0) {
      if (lane == 0)
        __hip_atomic_store(flag_ptr(ws, FL_READY2 + tile * 4 + q), flag_tag(nonce, FL_READY2 + tile * 4 + q), RLX_AGENT);
      const int fi = FL_READY2 + tile * 4 + (lane & 3);
      const u64 want = flag_tag(nonce, fi);
      u64* p = flag_ptr(ws, fi);
#pragma unroll 1
      for (unsigned spins = 0; spins < SPIN_CAP; ++spins) {
        const u64 v = __hip_atomic_load(p, RLX_AGENT);
        if (__all(v == want || (lane & 3) == q)) break;
        __builtin_amdgcn_s_sleep(1);
      }
    }
    BARRIER();
    u32x4 pv[3][2];
#pragma unroll
    for (int sidx = 0; sidx < 3; ++sidx) {
      const int src = sidx + (sidx >= q ? 1 : 0);
      const unsigned base = (unsigned)((((tile * 4 + q) * 4 + src) * 4 + wid) * 2) * 1024u + lane * 16u;
#pragma unroll
      for (int np = 0; np < 2; ++np) pv[sidx][np] = __builtin_amdgcn_raw_buffer_load_b128(rs, base + np * 1024u, 0, 16);
    }
#pragma unroll
    for (int ni = 0; ni < 4; ++ni) {
      f32x4 v = own[ni];
#pragma unroll
      for (int sidx = 0; sidx < 3; ++sidx) {
        const unsigned w0 = pv[sidx][ni >> 1][(ni & 1) * 2], w1 = pv[sidx][ni >> 1][(ni & 1) * 2 + 1];
        v[0] += bflo(w0); v[1] += bfhi(w0); v[2] += bflo(w1); v[3] += bfhi(w1);
      }
      u32x2 o = {pack2(v[0] + bflo(xv[ni][0]), v[1] + bfhi(xv[ni][0])), pack2(v[2] + bflo(xv[ni][1]), v[3] + bfhi(xv[ni][1]))};
      *(u32x2*)(out + orow + ni * 16) = o;
    }
  }
  if (bid == 0 && wid == 0) {
#pragma unroll 1
    for (unsigned spins = 0; spins < SPIN_CAP; ++spins) {
      bool ok = true;
#pragma unroll
      for (int i = 0; i < 4; ++i) {
        const int fi = FL_ARRIVE + i * 64 + lane;
        ok &= __hip_atomic_load(flag_ptr(ws, fi), RLX_AGENT) == flag_tag(nonce, fi);
      }
      if (__all(ok)) break;
      __builtin_amdgcn_s_sleep(1);
    }
    if (lane == 0) __hip_atomic_store((u64*)(ws + WS_SYNC), nonce + 1, RLX_AGENT);
  }
}

extern "C" void kernel_launch(void* const* d_in, const int* in_sizes, int n_in, void* d_out, int out_size,
                              void* d_ws, size_t ws_size, hipStream_t stream) {
  fused_kernel<<<256, 768, 0, stream>>>((const u16*)d_in[0], (const u16*)d_in[1], (const u16*)d_in[2],
                                        (const u16*)d_in[3], (const u16*)d_in[4], (const u16*)d_in[6],
                                        (const u16*)d_in[7], (const u16*)d_in[8], (u16*)d_out, (char*)d_ws);
}
```

```cpp
#include <hip/hip_runtime.h>
#include <hip/hip_bf16.h>
#include <stdint.h>
#include <type_traits>

typedef unsigned short u16;
typedef unsigned long long u64;
typedef short bf16x8 __attribute__((ext_vector_type(8)));
typedef short bf16x4 __attribute__((ext_vector_type(4)));
typedef float f32x4 __attribute__((ext_vector_type(4)));
typedef float f32x2 __attribute__((ext_vector_type(2)));
typedef unsigned u32x4 __attribute__((ext_vector_type(4)));
typedef unsigned u32x2 __attribute__((ext_vector_type(2)));

#define TB1 23
#define TB2 46
#define TB3 69
constexpr int S_ = 2048, D_ = 512, F_ = 1376, FP_ = 1408;

#define LDS_AS __attribute__((address_space(3)))
#define GLDS16(gptr, ldsptr, aux) \
  __builtin_amdgcn_global_load_lds((const unsigned*)(gptr), (unsigned*)(ldsptr), 16, 0, aux)
#define WAIT_V(n) asm volatile("s_waitcnt vmcnt(" #n ")" ::: "memory")
#define BARRIER() asm volatile("s_barrier" ::: "memory")
#define WAIT_L0() asm volatile("s_waitcnt lgkmcnt(0)" ::: "memory")
#define SCHED0() __builtin_amdgcn_sched_barrier(0)
#define RLX_AGENT __ATOMIC_RELAXED, __HIP_MEMORY_SCOPE_AGENT

__device__ __forceinline__ float bf16r(float x) { return __bfloat162float(__float2bfloat16(x)); }
typedef __bf16 bf2_t __attribute__((ext_vector_type(2)));
__device__ __forceinline__ unsigned pack2(float lo, float hi) {
  f32x2 v = {lo, hi};
  return __builtin_bit_cast(unsigned, __builtin_convertvector(v, bf2_t));
}
__device__ __forceinline__ float bflo(unsigned v) { return __builtin_bit_cast(float, v << 16); }
__device__ __forceinline__ float bfhi(unsigned v) { return __builtin_bit_cast(float, v & 0xffff0000u); }

template <int OFF>
__device__ __forceinline__ bf16x8 tr_frag(unsigned addr) {
  bf16x4 a, b;
  asm volatile("ds_read_b64_tr_b16 %0, %2 offset:%3\n\tds_read_b64_tr_b16 %1, %2 offset:%4"
               : "=&v"(a), "=&v"(b)
               : "v"(addr), "i"(OFF), "i"(OFF + 512));
  return __builtin_shufflevector(a, b, 0, 1, 2, 3, 4, 5, 6, 7);
}
template <int OFF>
__device__ __forceinline__ bf16x8 lds_rd128(unsigned addr) {
  bf16x8 a;
  asm volatile("ds_read_b128 %0, %1 offset:%2" : "=&v"(a) : "v"(addr), "i"(OFF));
  return a;
}
__device__ __forceinline__ unsigned lds_addr(const void* p) { return (unsigned)(uintptr_t)(const LDS_AS char*)p; }
__device__ __forceinline__ float gelu_tanh(float g) {
  const float c1 = -2.0f * 0.7978845608028654f * 1.4426950408889634f;
  const float c2 = c1 * 0.044715f;
  float w = g * __builtin_fmaf(g * g, c2, c1);
  float e = __builtin_amdgcn_exp2f(w);
  return g * __builtin_amdgcn_rcpf(1.0f + e);
}
#define TOUCH(gptr, ldsptr) __builtin_amdgcn_global_load_lds((const unsigned*)(gptr), (unsigned*)(ldsptr), 4, 0, 0)
template <class RD>
__device__ __forceinline__ void mm16(f32x4 (&acc)[4][4], const bf16x8 (&wf)[4], const bf16x8 (&xf)[4], RD rd) {
#pragma unroll
  for (int i = 0; i < 16; ++i) {
    const int ni = i >> 2, mi = i & 3;
    acc[ni][mi] = __builtin_amdgcn_mfma_f32_16x16x32_bf16(wf[ni], xf[mi], acc[ni][mi], 0, 0, 0);
    if (i < 8) {
      SCHED0();
      rd(i);
      SCHED0();
    }
  }
  SCHED0();
}

constexpr size_t WS_AB = 0;
constexpr size_t WS_H = 4u << 20;
constexpr size_t WS_SLAB = 16u << 20;
constexpr size_t WS_SYNC = 32u << 20;
constexpr int FL_ARRIVE = 0, FL_READY1 = 256, FL_READY2 = 512;
__device__ __forceinline__ u64* flag_ptr(char* ws, int idx) { return (u64*)(ws + WS_SYNC + 4096 + (size_t)idx * 64); }
__device__ __forceinline__ u64 flag_tag(u64 nonce, int idx) { return nonce + (u64)(idx + 1) * 0x9E3779B97F4A7C15ull; }
constexpr unsigned SPIN_CAP = 1u << 22;

constexpr int K1_A = 32768, K1_B = 16384, K1_STAGE = K1_A + K1_B;
constexpr int K2_STAGE = 32768, K2_NST = 5;
constexpr int L_SCR = 131072;
constexpr int L_SST = 147456, L_SCS = L_SST + 2048, L_NONCE = L_SCS + 512, L_CNT = L_NONCE + 64, L_DUMMY = L_NONCE + 256;

__global__ __launch_bounds__(768, 3) void fused_kernel(const u16* __restrict__ x, const u16* __restrict__ ws_re,
                                                       const u16* __restrict__ ws_im, const u16* __restrict__ wd_re,
                                                       const u16* __restrict__ wd_im, const u16* __restrict__ w_gate,
                                                       const u16* __restrict__ w_up, const u16* __restrict__ w_down,
                                                       u16* __restrict__ out, char* ws) {
  __shared__ __attribute__((aligned(1024))) char smem[K2_NST * K2_STAGE];
  const int tid = threadIdx.x, wid = __builtin_amdgcn_readfirstlane(tid >> 6), lane = tid & 63;
  const int fr = lane & 15, fq = lane >> 4;
  const int bid = blockIdx.x;
  u16* ab = (u16*)(ws + WS_AB);
  u16* h = (u16*)(ws + WS_H);

  const bool has_a = bid < 240;
  const int nb = (bid & 7) * 30 + (bid >> 3);
  const int cb = nb >> 3, rbA = nb & 7;
  const bool nk = cb < 8;
  const int rowA = rbA * 256;
  const int f0 = (cb - 8) * 64;
  const int idx = bid >> 3;
  const int q = (bid & 7) >> 1;
  const int rbB = (bid & 1) * 8 + (idx >> 2), cbk = idx & 3;
  const int rowB = rbB * 128, n0 = cbk * 128, tile = rbB * 4 + cbk;
  const int t_begin = q == 0 ? 0 : q == 1 ? TB1 : q == 2 ? TB2 : TB3;
  const int nt = (q == 0 ? TB1 : q == 1 ? TB2 : q == 2 ? TB3 : 86) - t_begin;

  const int sw = (fr >> 1) & 7;
  const int ch0 = ((fq) ^ sw) * 16, ch1 = ((4 + fq) ^ sw) * 16;
  const int tq = (lane >> 2) & 3, tp = lane & 3;
  const int tswz = ((tq >> 1) & 1) | ((fq & 1) << 1);
  const int tr_row = (8 * fq + tq) * 128 + 8 * tp;
  float* sst = (float*)(smem + L_SST);
  float* scs = (float*)(smem + L_SCS);
  u64* snonce = (u64*)(smem + L_NONCE);
  const unsigned sbase = lds_addr(smem);
  unsigned* scnt = (unsigned*)(smem + L_CNT);
  auto dep_flag = [&](int j) {
    const int t = t_begin + j;
    return FL_READY1 + (t < 64 ? (((t >> 5) * 4 + cbk) * 8 + ((t & 31) >> 2)) : ((8 + t - 64) * 8 + (rbB >> 1)));
  };
  auto wait_flag = [&](u64 nonce_, int fi) {
    const u64 want = flag_tag(nonce_, fi);
    u64* p = flag_ptr(ws, fi);
#pragma unroll 1
    for (unsigned spins = 0; spins < SPIN_CAP; ++spins) {
      const u64 v = __hip_atomic_load(p, RLX_AGENT);
      const bool ok = __builtin_amdgcn_readfirstlane((unsigned)(v == want)) != 0;
      if (ok) break;
      __builtin_amdgcn_s_sleep(1);
    }
  };
  auto uniform64 = [&](u64 v) {
    return ((u64)__builtin_amdgcn_readfirstlane((unsigned)(v >> 32)) << 32) | (u64)__builtin_amdgcn_readfirstlane((unsigned)v);
  };

  if (wid >= 8) {
    const int lw = wid - 8;
    const int l3 = lane >> 3, l7 = lane & 7;
    const int rt_c0 = (l7 ^ (fq & 7)) * 8, rt_c1 = (l7 ^ ((fq + 4) & 7)) * 8;
    const int kt_c0 = (l7 ^ ((fq & 1) << 1)) * 8, kt_c1 = (l7 ^ (((fq & 1) | 2) << 1)) * 8;
    if (has_a) {
      __builtin_amdgcn_s_setprio(2);
      const u16* xa0 = x + (size_t)(rowA + lw * 64 + l3) * D_ + rt_c0;
      const u16* xa1 = x + (size_t)(rowA + lw * 64 + 8 + l3) * D_ + rt_c1;
      const u16 *wb0, *wb1;
      int wstep_j, wstep_t;
      if (nk) {
        const u16* wnk = (cb < 4 ? wd_re : wd_im) + (size_t)((cb & 3) * 128 + lw * 32 + l3) * D_;
        wb0 = wnk + rt_c0;
        wb1 = wnk + 8 * D_ + rt_c1;
        wstep_j = 16 * D_;
        wstep_t = 64;
      } else {
        const u16* w = (lw < 2) ? w_gate : w_up;
        int c0 = f0 + kt_c0, c1 = f0 + kt_c1;
        if (c0 >= F_) c0 -= 32;
        if (c1 >= F_) c1 -= 32;
        const u16* wk = w + (size_t)((lw & 1) * 32 + l3) * F_;
        wb0 = wk + c0;
        wb1 = wk + 8 * F_ + c1;
        wstep_j = 16 * F_;
        wstep_t = 64 * F_;
      }
      auto stage = [&](int tj, int buf) {
        const int t = (tj + cb) & 7;
        char* sa = smem + buf * K1_STAGE + lw * 8192;
        char* sb = smem + buf * K1_STAGE + K1_A + lw * 4096;
#pragma unroll
        for (int i = 0; i < 4; ++i) {
          GLDS16(xa0 + i * 16 * D_ + t * 64, sa + i * 2048, 0);
          GLDS16(xa1 + i * 16 * D_ + t * 64, sa + i * 2048 + 1024, 0);
        }
#pragma unroll
        for (int i = 0; i < 2; ++i) {
          GLDS16(wb0 + i * wstep_j + t * wstep_t, sb + i * 2048, 0);
          GLDS16(wb1 + i * wstep_j + t * wstep_t, sb + i * 2048 + 1024, 0);
        }
      };
#pragma unroll 1
      for (int t = 0; t < 2; ++t) stage(t, t);
      f32x4 accS[4], accG[4], accC[2];
#pragma unroll
      for (int i = 0; i < 4; ++i) {
        accS[i] = f32x4{0.f, 0.f, 0.f, 0.f};
        accG[i] = f32x4{0.f, 0.f, 0.f, 0.f};
      }
      accC[0] = f32x4{0.f, 0.f, 0.f, 0.f};
      accC[1] = f32x4{0.f, 0.f, 0.f, 0.f};
      const short ONE = 0x3f80;
      const bf16x8 ones = {ONE, ONE, ONE, ONE, ONE, ONE, ONE, ONE};
      const unsigned la0 = sbase + (lw * 64 + fr) * 128 + ch0;
      const unsigned la1 = sbase + (lw * 64 + fr) * 128 + ch1;
      const unsigned lb0 = sbase + K1_A + (lw * 32 + fr) * 128 + ch0;
      const unsigned lb1 = sbase + K1_A + (lw * 32 + fr) * 128 + ch1;
      const unsigned lt0 = sbase + K1_A + (lw >> 1) * 8192 + tr_row + 32 * (((lw & 1) * 2) ^ tswz);
      const unsigned lt1 = sbase + K1_A + (lw >> 1) * 8192 + tr_row + 32 * (((lw & 1) * 2 + 1) ^ tswz);
      int ib = 2, buf = 0;
#pragma unroll 1
      for (int t = 0; t < 8; ++t) {
        if (t < 7) WAIT_V(12); else WAIT_V(0);
        BARRIER();
        const unsigned so = buf * K1_STAGE;
        bf16x8 xf[8], wf[4];
        xf[0] = lds_rd128<0>(la0 + so); xf[1] = lds_rd128<2048>(la0 + so);
        xf[2] = lds_rd128<4096>(la0 + so); xf[3] = lds_rd128<6144>(la0 + so);
        xf[4] = lds_rd128<0>(la1 + so); xf[5] = lds_rd128<2048>(la1 + so);
        xf[6] = lds_rd128<4096>(la1 + so); xf[7] = lds_rd128<6144>(la1 + so);
        if (nk) {
          wf[0] = lds_rd128<0>(lb0 + so); wf[1] = lds_rd128<2048>(lb0 + so);
          wf[2] = lds_rd128<0>(lb1 + so); wf[3] = lds_rd128<2048>(lb1 + so);
        } else {
          wf[0] = tr_frag<0>(lt0 + so); wf[1] = tr_frag<0>(lt1 + so);
          wf[2] = tr_frag<4096>(lt0 + so); wf[3] = tr_frag<4096>(lt1 + so);
        }
        if (t + 2 < 8) stage(t + 2, ib);
        ib = (ib == 2) ? 0 : ib + 1;
        WAIT_L0();
        SCHED0();
#pragma unroll
        for (int i = 0; i < 8; ++i) {
          accS[i & 3] = __builtin_amdgcn_mfma_f32_16x16x32_bf16(ones, xf[i], accS[i & 3], 0, 0, 0);
          accG[i & 3] = __builtin_amdgcn_mfma_f32_16x16x32_bf16(xf[i], xf[i], accG[i & 3], 0, 0, 0);
        }
#pragma unroll
        for (int i = 0; i < 4; ++i)
          accC[i & 1] = __builtin_amdgcn_mfma_f32_16x16x32_bf16(wf[i], ones, accC[i & 1], 0, 0, 0);
        buf = (buf == 2) ? 0 : buf + 1;
      }
      if (fq == (fr >> 2)) {
#pragma unroll
        for (int i = 0; i < 4; ++i) {
          const int r = fr & 3;
          const float qq = r == 0 ? accG[i][0] : r == 1 ? accG[i][1] : r == 2 ? accG[i][2] : accG[i][3];
          const float mean = accS[i][0] * (1.0f / D_);
          const float var = fmaxf(qq * (1.0f / D_) - mean * mean, 0.f);
          const float sd = bf16r(sqrtf(bf16r(bf16r(var) + 1e-5f)));
          f32x2 o = {bf16r(mean), __builtin_amdgcn_rcpf(sd)};
          *(f32x2*)(sst + (lw * 64 + i * 16 + fr) * 2) = o;
        }
      }
      if (fr == 0) {
        *(f32x4*)(scs + lw * 32 + fq * 4) = accC[0];
        *(f32x4*)(scs + lw * 32 + 16 + fq * 4) = accC[1];
      }
      WAIT_L0();
      BARRIER();
      __builtin_amdgcn_s_setprio(0);
    }
    const int sub = lw & 1;
    auto stageB = [&](int j, int slot) {
      const int t = t_begin + j;
      char* dst = smem + slot * K2_STAGE + lw * 8192;
      if (lw < 2) {
        if (t < 64) {
          const u16* A = (t < 32 ? ws_re : ws_im) + (size_t)(rowB + sub * 64 + l3) * S_ + (t & 31) * 64;
#pragma unroll
          for (int i = 0; i < 8; ++i) GLDS16(A + i * 8 * S_ + ((i & 1) ? rt_c1 : rt_c0), dst + i * 1024, 0);
        } else {
          const u16* A = h + (size_t)(rowB + sub * 64 + l3) * FP_ + (t - 64) * 64;
#pragma unroll
          for (int i = 0; i < 8; ++i) GLDS16(A + i * 8 * FP_ + ((i & 1) ? rt_c1 : rt_c0), dst + i * 1024, 16);
        }
      } else {
        if (t < 64) {
          const u16* B = ab + (size_t)(t * 64 + l3) * D_ + n0 + sub * 64;
#pragma unroll
          for (int i = 0; i < 8; ++i) GLDS16(B + i * 8 * D_ + ((i & 1) ? kt_c1 : kt_c0), dst + i * 1024, 16);
        } else {
          const int k0 = (t - 64) * 64;
#pragma unroll
          for (int i = 0; i < 8; ++i) {
            const int krow = min(k0 + i * 8 + l3, F_ - 1);
            GLDS16(w_down + krow * D_ + n0 + sub * 64 + ((i & 1) ? kt_c1 : kt_c0), dst + i * 1024, 0);
          }
        }
      }
    };
    const bool early = (lw < 2) ? (t_begin + K2_NST - 2 < 64) : (t_begin >= 64);
    if (!has_a) BARRIER();
    if (!early) {
      const u64 nonce_ = uniform64(*snonce);
      int last = -1;
#pragma unroll 1
      for (int j = 0; j < K2_NST - 1; ++j) {
        const int fi = dep_flag(j);
        if (fi != last) wait_flag(nonce_, fi);
        last = fi;
      }
    }
#pragma unroll 1
    for (int s = 0; s < K2_NST - 1; ++s) stageB(s, s);
    int islot = K2_NST - 1;
#pragma unroll 1
    for (int j = 0; j < nt; ++j) {
      const int rem = nt - 1 - j;
      if (rem >= 3) WAIT_V(24);
      else if (rem == 2) WAIT_V(16);
      else if (rem == 1) WAIT_V(8);
      else WAIT_V(0);
      BARRIER();
      if (j + K2_NST - 1 < nt) stageB(j + K2_NST - 1, islot);
      islot = (islot == K2_NST - 1) ? 0 : islot + 1;
    }
    return;
  }

  u64 nonce = 0;
  if (wid == 4) nonce = __hip_atomic_load((u64*)(ws + WS_SYNC), RLX_AGENT);
  auto share_nonce = [&]() {
    if (wid == 4) {
      nonce = uniform64(nonce);
      if (lane == 0) {
        *snonce = nonce;
        *scnt = 0u;
        __hip_atomic_store(flag_ptr(ws, FL_ARRIVE + bid), flag_tag(nonce, FL_ARRIVE + bid), RLX_AGENT);
      }
      WAIT_L0();
    }
  };
  const int wm = wid >> 1, wn = wid & 1;
  using C0 = std::integral_constant<int, 0>;
  using C1 = std::integral_constant<int, 1>;
  char* dummy = smem + L_DUMMY;
  auto warm = [&](int slot) {
    if (lane >= 16) return;
    const int L = slot * 16 + lane;
    if (q < 3) {
      const int ns = min(nt, 64 - t_begin);
      const int row = L / ns, t = t_begin + (L - row * ns);
      if (row < 32) TOUCH((t < 32 ? ws_re : ws_im) + (size_t)(rowB + cbk * 32 + row) * S_ + (t & 31) * 64, dummy);
    } else {
      const int per = (86 - TB3) * 8;
      const int krow = (TB3 - 64) * 64 + (idx >> 2) * per + (L >> 1);
      if (L < 2 * per && krow < F_) TOUCH(w_down + (size_t)krow * D_ + n0 + (L & 1) * 64, dummy);
    }
  };
  if (!has_a) {
    share_nonce();
    BARRIER();
#pragma unroll 1
    for (int i = 0; i < 6; ++i) warm(i * 8 + wid);
  }

  if (has_a) {
    f32x4 acc[4][4];
#pragma unroll
    for (int i = 0; i < 4; ++i)
#pragma unroll
      for (int j = 0; j < 4; ++j) acc[i][j] = f32x4{0.f, 0.f, 0.f, 0.f};
    const unsigned a_b0 = sbase + (wm * 64 + fr) * 128 + ch0, a_b1 = sbase + (wm * 64 + fr) * 128 + ch1;
    const unsigned n_b0 = sbase + K1_A + (wn * 64 + fr) * 128 + ch0;
    const unsigned n_b1 = sbase + K1_A + (wn * 64 + fr) * 128 + ch1;
    const unsigned t_p0 = sbase + K1_A + tr_row + 32 * ((wn * 2) ^ tswz);
    const unsigned t_p1 = sbase + K1_A + tr_row + 32 * ((wn * 2 + 1) ^ tswz);

    auto mainloop = [&](auto NKC) {
      constexpr bool NK = decltype(NKC)::value;
      bf16x8 xf0[4], wf0[4], xf1[4], wf1[4];
      auto rdg = [&](auto KSC, bf16x8(&xf)[4], bf16x8(&wf)[4], unsigned so, int g) {
        constexpr int KS = decltype(KSC)::value;
        const unsigned ab_ = (KS ? a_b1 : a_b0) + so;
        switch (g) {
          case 0: xf[0] = lds_rd128<0>(ab_); break;
          case 2: xf[1] = lds_rd128<2048>(ab_); break;
          case 4: xf[2] = lds_rd128<4096>(ab_); break;
          case 6: xf[3] = lds_rd128<6144>(ab_); break;
          default:
            if constexpr (NK) {
              const unsigned nb_ = (KS ? n_b1 : n_b0) + so;
              if (g == 1) wf[0] = lds_rd128<0>(nb_);
              if (g == 3) wf[1] = lds_rd128<2048>(nb_);
              if (g == 5) wf[2] = lds_rd128<4096>(nb_);
              if (g == 7) wf[3] = lds_rd128<6144>(nb_);
            } else {
              if (g == 1) wf[0] = tr_frag<KS * 4096>(t_p0 + so);
              if (g == 3) wf[1] = tr_frag<KS * 4096>(t_p1 + so);
              if (g == 5) wf[2] = tr_frag<KS * 4096 + 8192>(t_p0 + so);
              if (g == 7) wf[3] = tr_frag<KS * 4096 + 8192>(t_p1 + so);
            }
        }
      };
      BARRIER();
#pragma unroll
      for (int g = 0; g < 8; ++g) rdg(C0{}, xf0, wf0, 0u, g);
      WAIT_L0();
      SCHED0();
      unsigned so = 0;
#pragma unroll 1
      for (int t = 0; t < 7; ++t) {
        if (t > 0) warm((t - 1) * 8 + wid);
        mm16(acc, wf0, xf0, [&](int g) { rdg(C1{}, xf1, wf1, so, g); });
        WAIT_L0();
        SCHED0();
        so = (so == 2 * K1_STAGE) ? 0u : so + K1_STAGE;
        BARRIER();
        mm16(acc, wf1, xf1, [&](int g) { rdg(C0{}, xf0, wf0, so, g); });
        WAIT_L0();
        SCHED0();
      }
      mm16(acc, wf0, xf0, [&](int g) { rdg(C1{}, xf1, wf1, so, g); });
      WAIT_L0();
      SCHED0();
      mm16(acc, wf1, xf1, [&](int) {});
    };
    if (nk) mainloop(std::true_type{});
    else mainloop(std::false_type{});
    share_nonce();
    BARRIER();
    float mb[4], rinv[4];
#pragma unroll
    for (int mi = 0; mi < 4; ++mi) {
      f32x2 v = *(const f32x2*)(sst + (wm * 64 + mi * 16 + fr) * 2);
      mb[mi] = v[0];
      rinv[mi] = v[1];
    }
    f32x4 csv[4];
#pragma unroll
    for (int ni = 0; ni < 4; ++ni) {
      const int ci = nk ? (wn * 64 + ni * 16) : ((ni >> 1) * 64 + wn * 32 + (ni & 1) * 16);
      csv[ni] = *(const f32x4*)(scs + ci + fq * 4);
    }
    char* scr = smem + L_SCR + wid * 2048;
    const __amdgpu_buffer_rsrc_t rsw = __builtin_amdgcn_make_buffer_rsrc(ws, 0, 16u << 20, 0x00020000);
    if (nk) {
      const float sgn = cb < 4 ? 1.0f : -1.0f;
      const unsigned gbase = (unsigned)WS_AB +
          (unsigned)(((cb >> 2) * S_ + rowA + wm * 64 + (lane >> 3)) * D_ + (cb & 3) * 128 + wn * 64 + (lane & 7) * 8) * 2u;
      u32x2 o[4][4];
#pragma unroll
      for (int mi = 0; mi < 4; ++mi) {
        const float r = rinv[mi] * sgn;
#pragma unroll
        for (int ni = 0; ni < 4; ++ni) {
          f32x4 v = (acc[ni][mi] - mb[mi] * csv[ni]) * r;
          o[mi][ni] = u32x2{pack2(v[0], v[1]), pack2(v[2], v[3])};
        }
      }
#pragma unroll
      for (int mi = 0; mi < 4; ++mi) {
#pragma unroll
        for (int ni = 0; ni < 4; ++ni)
          *(u32x2*)(scr + fr * 128 + (((ni * 2 + (fq >> 1)) ^ (fr & 7)) * 16) + (fq & 1) * 8) = o[mi][ni];
#pragma unroll
        for (int hf = 0; hf < 2; ++hf) {
          const int r2 = hf * 8 + (lane >> 3);
          u32x4 v = *(const u32x4*)(scr + r2 * 128 + (((lane & 7) ^ (r2 & 7)) * 16));
          __builtin_amdgcn_raw_buffer_store_b128(v, rsw, gbase + (unsigned)((mi * 16 + hf * 8) * D_ * 2), 0, 16);
        }
      }
    } else {
      const unsigned vmask = (f0 + wn * 32) < F_ ? 0xffffffffu : 0u;
      const unsigned gbase = (unsigned)WS_H +
          (unsigned)((rowA + wm * 64 + (lane >> 2)) * FP_ + f0 + wn * 32 + (lane & 3) * 8) * 2u;
#pragma unroll
      for (int mp = 0; mp < 2; ++mp) {
#pragma unroll
        for (int mj = 0; mj < 2; ++mj) {
          const int mi = mp * 2 + mj;
#pragma unroll
          for (int ni = 0; ni < 2; ++ni) {
            f32x4 g = (acc[ni][mi] - mb[mi] * csv[ni]) * rinv[mi];
            f32x4 u = (acc[ni + 2][mi] - mb[mi] * csv[ni + 2]) * rinv[mi];
            float hv[4];
#pragma unroll
            for (int e = 0; e < 4; ++e) hv[e] = gelu_tanh(g[e]) * u[e];
            u32x2 o = {pack2(hv[0], hv[1]) & vmask, pack2(hv[2], hv[3]) & vmask};
            *(u32x2*)(scr + mj * 1024 + fr * 64 + (((ni * 2 + (fq >> 1)) ^ ((fr >> 1) & 3)) * 16) + (fq & 1) * 8) = o;
          }
        }
        const int r2 = lane >> 2;
#pragma unroll
        for (int mj = 0; mj < 2; ++mj) {
          u32x4 v = *(const u32x4*)(scr + mj * 1024 + r2 * 64 + (((lane & 3) ^ ((r2 >> 1) & 3)) * 16));
          __builtin_amdgcn_raw_buffer_store_b128(v, rsw, gbase + (unsigned)((mp * 2 + mj) * 16 * FP_ * 2), 0, 16);
        }
      }
    }
  }
  WAIT_V(0);
  if (has_a && lane == 0) __hip_atomic_fetch_add(scnt, 1u, __ATOMIC_RELAXED, __HIP_MEMORY_SCOPE_WORKGROUP);
  if (wid > 4) return;

  if (wid == 4) {
    int last = dep_flag(K2_NST - 2);
#pragma unroll 1
    for (int j = 0; j < nt; ++j) {
      if (j + K2_NST - 1 < nt) {
        const int fi = dep_flag(j + K2_NST - 1);
        if (fi != last) wait_flag(nonce, fi);
        last = fi;
      }
      BARRIER();
    }
    return;
  }

  if (wid == 0) {
    nonce = uniform64(*snonce);
    if (has_a) {
#pragma unroll 1
      for (unsigned spins = 0; spins < SPIN_CAP; ++spins) {
        const unsigned c = __builtin_amdgcn_readfirstlane(__hip_atomic_load(scnt, __ATOMIC_RELAXED, __HIP_MEMORY_SCOPE_WORKGROUP));
        if (c >= 8u) break;
        __builtin_amdgcn_s_sleep(1);
      }
      if (lane == 0) __hip_atomic_store(flag_ptr(ws, FL_READY1 + nb), flag_tag(nonce, FL_READY1 + nb), RLX_AGENT);
    }
  }
  {
    f32x4 acc[4][4];
#pragma unroll
    for (int i = 0; i < 4; ++i)
#pragma unroll
      for (int j = 0; j < 4; ++j) acc[i][j] = f32x4{0.f, 0.f, 0.f, 0.f};
    const unsigned a_base0 = sbase + (wm * 64 + fr) * 128 + ch0;
    const unsigned a_base1 = sbase + (wm * 64 + fr) * 128 + ch1;
    const unsigned b_base = sbase + 16384 + wn * 8192 + tr_row;
    unsigned b_off[4];
#pragma unroll
    for (int ni = 0; ni < 4; ++ni) b_off[ni] = b_base + 32 * (ni ^ tswz);
    bf16x8 af0[4], bf0[4], af1[4], bf1[4];
    auto rdg = [&](auto KSC, bf16x8(&af)[4], bf16x8(&bfr)[4], unsigned so, int g) {
      constexpr int KS = decltype(KSC)::value;
      const unsigned ab_ = (KS ? a_base1 : a_base0) + so;
      switch (g) {
        case 0: af[0] = lds_rd128<0>(ab_); break;
        case 2: af[1] = lds_rd128<2048>(ab_); break;
        case 4: af[2] = lds_rd128<4096>(ab_); break;
        case 6: af[3] = lds_rd128<6144>(ab_); break;
        case 1: bfr[0] = tr_frag<KS * 4096>(b_off[0] + so); break;
        case 3: bfr[1] = tr_frag<KS * 4096>(b_off[1] + so); break;
        case 5: bfr[2] = tr_frag<KS * 4096>(b_off[2] + so); break;
        default: bfr[3] = tr_frag<KS * 4096>(b_off[3] + so); break;
      }
    };
    BARRIER();
#pragma unroll
    for (int g = 0; g < 8; ++g) rdg(C0{}, af0, bf0, 0u, g);
    WAIT_L0();
    SCHED0();
    unsigned so = 0;
#pragma unroll 1
    for (int j = 0; j < nt - 1; ++j) {
      mm16(acc, bf0, af0, [&](int g) { rdg(C1{}, af1, bf1, so, g); });
      WAIT_L0();
      SCHED0();
      so = (so == (K2_NST - 1) * K2_STAGE) ? 0u : so + K2_STAGE;
      BARRIER();
      mm16(acc, bf1, af1, [&](int g) { rdg(C0{}, af0, bf0, so, g); });
      WAIT_L0();
      SCHED0();
    }
    mm16(acc, bf0, af0, [&](int g) { rdg(C1{}, af1, bf1, so, g); });
    WAIT_L0();
    SCHED0();
    mm16(acc, bf1, af1, [&](int) {});
    const __amdgpu_buffer_rsrc_t rs = __builtin_amdgcn_make_buffer_rsrc(ws + WS_SLAB, 0, 8u << 20, 0x00020000);
    f32x4 own[4];
#pragma unroll
    for (int mi = 0; mi < 4; ++mi) {
      if (mi == q) {
#pragma unroll
        for (int ni = 0; ni < 4; ++ni) own[ni] = acc[ni][mi];
      } else {
        const unsigned base = (unsigned)((((tile * 4 + mi) * 4 + q) * 4 + wid) * 2) * 1024u + lane * 16u;
#pragma unroll
        for (int np = 0; np < 2; ++np) {
          const f32x4 a0 = acc[2 * np][mi], a1 = acc[2 * np + 1][mi];
          u32x4 v = {pack2(a0[0], a0[1]), pack2(a0[2], a0[3]), pack2(a1[0], a1[1]), pack2(a1[2], a1[3])};
          __builtin_amdgcn_raw_buffer_store_b128(v, rs, base + np * 1024u, 0, 16);
        }
      }
    }
    const size_t orow = (size_t)(rowB + wm * 64 + q * 16 + fr) * D_ + n0 + wn * 64 + fq * 4;
    u32x2 xv[4];
#pragma unroll
    for (int ni = 0; ni < 4; ++ni) xv[ni] = *(const u32x2*)(x + orow + ni * 16);
    WAIT_V(0);
    BARRIER();
    if (wid == 0) {
      if (lane == 0)
        __hip_atomic_store(flag_ptr(ws, FL_READY2 + tile * 4 + q), flag_tag(nonce, FL_READY2 + tile * 4 + q), RLX_AGENT);
      const int fi = FL_READY2 + tile * 4 + (lane & 3);
      const u64 want = flag_tag(nonce, fi);
      u64* p = flag_ptr(ws, fi);
#pragma unroll 1
      for (unsigned spins = 0; spins < SPIN_CAP; ++spins) {
        const u64 v = __hip_atomic_load(p, RLX_AGENT);
        if (__all(v == want || (lane & 3) == q)) break;
        __builtin_amdgcn_s_sleep(1);
      }
    }
    BARRIER();
    u32x4 pv[3][2];
#pragma unroll
    for (int sidx = 0; sidx < 3; ++sidx) {
      const int src = sidx + (sidx >= q ? 1 : 0);
      const unsigned base = (unsigned)((((tile * 4 + q) * 4 + src) * 4 + wid) * 2) * 1024u + lane * 16u;
#pragma unroll
      for (int np = 0; np < 2; ++np) pv[sidx][np] = __builtin_amdgcn_raw_buffer_load_b128(rs, base + np * 1024u, 0, 16);
    }
#pragma unroll
    for (int ni = 0; ni < 4; ++ni) {
      f32x4 v = own[ni];
#pragma unroll
      for (int sidx = 0; sidx < 3; ++sidx) {
        const unsigned w0 = pv[sidx][ni >> 1][(ni & 1) * 2], w1 = pv[sidx][ni >> 1][(ni & 1) * 2 + 1];
        v[0] += bflo(w0); v[1] += bfhi(w0); v[2] += bflo(w1); v[3] += bfhi(w1);
      }
      u32x2 o = {pack2(v[0] + bflo(xv[ni][0]), v[1] + bfhi(xv[ni][0])), pack2(v[2] + bflo(xv[ni][1]), v[3] + bfhi(xv[ni][1]))};
      *(u32x2*)(out + orow + ni * 16) = o;
    }
  }
  if (bid == 0 && wid == 0) {
#pragma unroll 1
    for (unsigned spins = 0; spins < SPIN_CAP; ++spins) {
      bool ok = true;
#pragma unroll
      for (int i = 0; i < 4; ++i) {
        const int fi = FL_ARRIVE + i * 64 + lane;
        ok &= __hip_atomic_load(flag_ptr(ws, fi), RLX_AGENT) == flag_tag(nonce, fi);
      }
      if (__all(ok)) break;
      __builtin_amdgcn_s_sleep(1);
    }
    if (lane == 0) __hip_atomic_store((u64*)(ws + WS_SYNC), nonce + 1, RLX_AGENT);
  }
}

extern "C" void kernel_launch(void* const* d_in, const int* in_sizes, int n_in, void* d_out, int out_size,
                              void* d_ws, size_t ws_size, hipStream_t stream) {
  fused_kernel<<<256, 768, 0, stream>>>((const u16*)d_in[0], (const u16*)d_in[1], (const u16*)d_in[2],
                                        (const u16*)d_in[3], (const u16*)d_in[4], (const u16*)d_in[6],
                                        (const u16*)d_in[7], (const u16*)d_in[8], (u16*)d_out, (char*)d_ws);
}
```

```cpp
#include <hip/hip_runtime.h>
#include <hip/hip_bf16.h>
#include <stdint.h>
#include <type_traits>

typedef unsigned short u16;
typedef unsigned long long u64;
typedef short bf16x8 __attribute__((ext_vector_type(8)));
typedef short bf16x4 __attribute__((ext_vector_type(4)));
typedef float f32x4 __attribute__((ext_vector_type(4)));
typedef float f32x2 __attribute__((ext_vector_type(2)));
typedef unsigned u32x4 __attribute__((ext_vector_type(4)));
typedef unsigned u32x2 __attribute__((ext_vector_type(2)));

constexpr int S_ = 2048, D_ = 512, F_ = 1376, FP_ = 1408;

#define LDS_AS __attribute__((address_space(3)))
#define GLDS16(gptr, ldsptr, aux) \
  __builtin_amdgcn_global_load_lds((const unsigned*)(gptr), (unsigned*)(ldsptr), 16, 0, aux)
#define WAIT_V(n) asm volatile("s_waitcnt vmcnt(" #n ")" ::: "memory")
#define BARRIER() asm volatile("s_barrier" ::: "memory")
#define WAIT_L0() asm volatile("s_waitcnt lgkmcnt(0)" ::: "memory")
#define SCHED0() __builtin_amdgcn_sched_barrier(0)
#define RLX_AGENT __ATOMIC_RELAXED, __HIP_MEMORY_SCOPE_AGENT

__device__ __forceinline__ float bf16r(float x) { return __bfloat162float(__float2bfloat16(x)); }
typedef __bf16 bf2_t __attribute__((ext_vector_type(2)));
__device__ __forceinline__ unsigned pack2(float lo, float hi) {
  f32x2 v = {lo, hi};
  return __builtin_bit_cast(unsigned, __builtin_convertvector(v, bf2_t));
}
__device__ __forceinline__ float bflo(unsigned v) { return __builtin_bit_cast(float, v << 16); }
__device__ __forceinline__ float bfhi(unsigned v) { return __builtin_bit_cast(float, v & 0xffff0000u); }

template <int OFF>
__device__ __forceinline__ bf16x8 tr_frag(unsigned addr) {
  bf16x4 a, b;
  asm volatile("ds_read_b64_tr_b16 %0, %2 offset:%3\n\tds_read_b64_tr_b16 %1, %2 offset:%4"
               : "=&v"(a), "=&v"(b)
               : "v"(addr), "i"(OFF), "i"(OFF + 512));
  return __builtin_shufflevector(a, b, 0, 1, 2, 3, 4, 5, 6, 7);
}
template <int OFF>
__device__ __forceinline__ bf16x8 lds_rd128(unsigned addr) {
  bf16x8 a;
  asm volatile("ds_read_b128 %0, %1 offset:%2" : "=&v"(a) : "v"(addr), "i"(OFF));
  return a;
}
__device__ __forceinline__ unsigned lds_addr(const void* p) { return (unsigned)(uintptr_t)(const LDS_AS char*)p; }
__device__ __forceinline__ float gelu_tanh(float g) {
  const float c1 = -2.0f * 0.7978845608028654f * 1.4426950408889634f;
  const float c2 = c1 * 0.044715f;
  float w = g * __builtin_fmaf(g * g, c2, c1);
  float e = __builtin_amdgcn_exp2f(w);
  return g * __builtin_amdgcn_rcpf(1.0f + e);
}
#define TOUCH(gptr, ldsptr) __builtin_amdgcn_global_load_lds((const unsigned*)(gptr), (unsigned*)(ldsptr), 4, 0, 0)
template <class RD>
__device__ __forceinline__ void mm16(f32x4 (&acc)[4][4], const bf16x8 (&wf)[4], const bf16x8 (&xf)[4], RD rd) {
#pragma unroll
  for (int i = 0; i < 16; ++i) {
    const int ni = i >> 2, mi = i & 3;
    acc[ni][mi] = __builtin_amdgcn_mfma_f32_16x16x32_bf16(wf[ni], xf[mi], acc[ni][mi], 0, 0, 0);
    if (i < 8) {
      SCHED0();
      rd(i);
      SCHED0();
    }
  }
  SCHED0();
}

constexpr size_t WS_AB = 0;
constexpr size_t WS_H = 4u << 20;
constexpr size_t WS_SLAB = 16u << 20;
constexpr size_t WS_SYNC = 32u << 20;
constexpr int FL_ARRIVE = 0, FL_READY1 = 256, FL_READY2 = 512;
__device__ __forceinline__ u64* flag_ptr(char* ws, int idx) { return (u64*)(ws + WS_SYNC + 4096 + (size_t)idx * 64); }
__device__ __forceinline__ u64 flag_tag(u64 nonce, int idx) { return nonce + (u64)(idx + 1) * 0x9E3779B97F4A7C15ull; }
constexpr unsigned SPIN_CAP = 1u << 17;

constexpr int K1_A = 32768, K1_B = 16384, K1_STAGE = K1_A + K1_B;
constexpr int K2_STAGE = 32768, K2_NST = 5;
constexpr int L_SCR = 131072;
constexpr int L_SST = 147456, L_SCS = L_SST + 2048, L_NONCE = L_SCS + 512, L_CNT = L_NONCE + 64, L_DUMMY = L_NONCE + 256;

__global__ __launch_bounds__(768, 3) void fused_kernel(const u16* __restrict__ x, const u16* __restrict__ ws_re,
                                                       const u16* __restrict__ ws_im, const u16* __restrict__ wd_re,
                                                       const u16* __restrict__ wd_im, const u16* __restrict__ w_gate,
                                                       const u16* __restrict__ w_up, const u16* __restrict__ w_down,
                                                       u16* __restrict__ out, char* ws) {
  __shared__ __attribute__((aligned(1024))) char smem[K2_NST * K2_STAGE];
  const int tid = threadIdx.x, wid = __builtin_amdgcn_readfirstlane(tid >> 6), lane = tid & 63;
  const int fr = lane & 15, fq = lane >> 4;
  const int bid = blockIdx.x;
  u16* ab = (u16*)(ws + WS_AB);
  u16* h = (u16*)(ws + WS_H);

  const bool has_a = bid < 240;
  const int nb = (bid & 7) * 30 + (bid >> 3);
  const int cb = nb >> 3, rbA = nb & 7;
  const bool nk = cb < 8;
  const int rowA = rbA * 256;
  const int f0 = (cb - 8) * 64;
  const int idx = bid >> 3;
  const int q = (bid & 7) >> 1;
  const int rbB = (bid & 1) * 8 + (idx >> 2), cbk = idx & 3;
  const int rowB = rbB * 128, n0 = cbk * 128, tile = rbB * 4 + cbk;
  const int hq0 = q < 2 ? 6 * q : 12 + 5 * (q - 2);
  const int nt = q < 2 ? 22 : 21;
  auto t_of = [&](int j) { return j < 16 ? 16 * q + j : 64 + hq0 + (j - 16); };

  const int sw = (fr >> 1) & 7;
  const int ch0 = ((fq) ^ sw) * 16, ch1 = ((4 + fq) ^ sw) * 16;
  const int tq = (lane >> 2) & 3, tp = lane & 3;
  const int tswz = ((tq >> 1) & 1) | ((fq & 1) << 1);
  const int tr_row = (8 * fq + tq) * 128 + 8 * tp;
  float* sst = (float*)(smem + L_SST);
  float* scs = (float*)(smem + L_SCS);
  u64* snonce = (u64*)(smem + L_NONCE);
  const unsigned sbase = lds_addr(smem);
  unsigned* scnt = (unsigned*)(smem + L_CNT);
  auto dep_flag = [&](int j) {
    const int t = t_of(j);
    return FL_READY1 + (t < 64 ? (((t >> 5) * 4 + cbk) * 8 + ((t & 31) >> 2)) : ((8 + t - 64) * 8 + (rbB >> 1)));
  };
  auto wait_flag = [&](u64 nonce_, int fi) {
    const u64 want = flag_tag(nonce_, fi);
    u64* p = flag_ptr(ws, fi);
#pragma unroll 1
    for (unsigned spins = 0; spins < SPIN_CAP; ++spins) {
      const u64 v = __hip_atomic_load(p, RLX_AGENT);
      const bool ok = __builtin_amdgcn_readfirstlane((unsigned)(v == want)) != 0;
      if (ok) break;
      __builtin_amdgcn_s_sleep(1);
    }
  };
  auto uniform64 = [&](u64 v) {
    return ((u64)__builtin_amdgcn_readfirstlane((unsigned)(v >> 32)) << 32) | (u64)__builtin_amdgcn_readfirstlane((unsigned)v);
  };

  if (wid >= 8) {
    const int lw = wid - 8;
    const int l3 = lane >> 3, l7 = lane & 7;
    const int rt_c0 = (l7 ^ (fq & 7)) * 8, rt_c1 = (l7 ^ ((fq + 4) & 7)) * 8;
    const int kt_c0 = (l7 ^ ((fq & 1) << 1)) * 8, kt_c1 = (l7 ^ (((fq & 1) | 2) << 1)) * 8;
    if (has_a) {
      __builtin_amdgcn_s_setprio(2);
      const u16* xa0 = x + (size_t)(rowA + lw * 64 + l3) * D_ + rt_c0;
      const u16* xa1 = x + (size_t)(rowA + lw * 64 + 8 + l3) * D_ + rt_c1;
      const u16 *wb0, *wb1;
      int wstep_j, wstep_t;
      if (nk) {
        const u16* wnk = (cb < 4 ? wd_re : wd_im) + (size_t)((cb & 3) * 128 + lw * 32 + l3) * D_;
        wb0 = wnk + rt_c0;
        wb1 = wnk + 8 * D_ + rt_c1;
        wstep_j = 16 * D_;
        wstep_t = 64;
      } else {
        const u16* w = (lw < 2) ? w_gate : w_up;
        int c0 = f0 + kt_c0, c1 = f0 + kt_c1;
        if (c0 >= F_) c0 -= 32;
        if (c1 >= F_) c1 -= 32;
        const u16* wk = w + (size_t)((lw & 1) * 32 + l3) * F_;
        wb0 = wk + c0;
        wb1 = wk + 8 * F_ + c1;
        wstep_j = 16 * F_;
        wstep_t = 64 * F_;
      }
      auto stage = [&](int tj, int buf) {
        const int t = (tj + cb) & 7;
        char* sa = smem + buf * K1_STAGE + lw * 8192;
        char* sb = smem + buf * K1_STAGE + K1_A + lw * 4096;
#pragma unroll
        for (int i = 0; i < 4; ++i) {
          GLDS16(xa0 + i * 16 * D_ + t * 64, sa + i * 2048, 0);
          GLDS16(xa1 + i * 16 * D_ + t * 64, sa + i * 2048 + 1024, 0);
        }
#pragma unroll
        for (int i = 0; i < 2; ++i) {
          GLDS16(wb0 + i * wstep_j + t * wstep_t, sb + i * 2048, 0);
          GLDS16(wb1 + i * wstep_j + t * wstep_t, sb + i * 2048 + 1024, 0);
        }
      };
#pragma unroll 1
      for (int t = 0; t < 2; ++t) stage(t, t);
      f32x4 accS[4], accG[4], accC[2];
#pragma unroll
      for (int i = 0; i < 4; ++i) {
        accS[i] = f32x4{0.f, 0.f, 0.f, 0.f};
        accG[i] = f32x4{0.f, 0.f, 0.f, 0.f};
      }
      accC[0] = f32x4{0.f, 0.f, 0.f, 0.f};
      accC[1] = f32x4{0.f, 0.f, 0.f, 0.f};
      const short ONE = 0x3f80;
      const bf16x8 ones = {ONE, ONE, ONE, ONE, ONE, ONE, ONE, ONE};
      const unsigned la0 = sbase + (lw * 64 + fr) * 128 + ch0;
      const unsigned la1 = sbase + (lw * 64 + fr) * 128 + ch1;
      const unsigned lb0 = sbase + K1_A + (lw * 32 + fr) * 128 + ch0;
      const unsigned lb1 = sbase + K1_A + (lw * 32 + fr) * 128 + ch1;
      const unsigned lt0 = sbase + K1_A + (lw >> 1) * 8192 + tr_row + 32 * (((lw & 1) * 2) ^ tswz);
      const unsigned lt1 = sbase + K1_A + (lw >> 1) * 8192 + tr_row + 32 * (((lw & 1) * 2 + 1) ^ tswz);
      int ib = 2, buf = 0;
#pragma unroll 1
      for (int t = 0; t < 8; ++t) {
        if (t < 7) WAIT_V(12); else WAIT_V(0);
        BARRIER();
        const unsigned so = buf * K1_STAGE;
        bf16x8 xf[8], wf[4];
        xf[0] = lds_rd128<0>(la0 + so); xf[1] = lds_rd128<2048>(la0 + so);
        xf[2] = lds_rd128<4096>(la0 + so); xf[3] = lds_rd128<6144>(la0 + so);
        xf[4] = lds_rd128<0>(la1 + so); xf[5] = lds_rd128<2048>(la1 + so);
        xf[6] = lds_rd128<4096>(la1 + so); xf[7] = lds_rd128<6144>(la1 + so);
        if (nk) {
          wf[0] = lds_rd128<0>(lb0 + so); wf[1] = lds_rd128<2048>(lb0 + so);
          wf[2] = lds_rd128<0>(lb1 + so); wf[3] = lds_rd128<2048>(lb1 + so);
        } else {
          wf[0] = tr_frag<0>(lt0 + so); wf[1] = tr_frag<0>(lt1 + so);
          wf[2] = tr_frag<4096>(lt0 + so); wf[3] = tr_frag<4096>(lt1 + so);
        }
        if (t + 2 < 8) stage(t + 2, ib);
        ib = (ib == 2) ? 0 : ib + 1;
        WAIT_L0();
        SCHED0();
#pragma unroll
        for (int i = 0; i < 8; ++i) {
          accS[i & 3] = __builtin_amdgcn_mfma_f32_16x16x32_bf16(ones, xf[i], accS[i & 3], 0, 0, 0);
          accG[i & 3] = __builtin_amdgcn_mfma_f32_16x16x32_bf16(xf[i], xf[i], accG[i & 3], 0, 0, 0);
        }
#pragma unroll
        for (int i = 0; i < 4; ++i)
          accC[i & 1] = __builtin_amdgcn_mfma_f32_16x16x32_bf16(wf[i], ones, accC[i & 1], 0, 0, 0);
        buf = (buf == 2) ? 0 : buf + 1;
      }
      if (fq == (fr >> 2)) {
#pragma unroll
        for (int i = 0; i < 4; ++i) {
          const int r = fr & 3;
          const float qq = r == 0 ? accG[i][0] : r == 1 ? accG[i][1] : r == 2 ? accG[i][2] : accG[i][3];
          const float mean = accS[i][0] * (1.0f / D_);
          const float var = fmaxf(qq * (1.0f / D_) - mean * mean, 0.f);
          const float sd = bf16r(sqrtf(bf16r(bf16r(var) + 1e-5f)));
          f32x2 o = {bf16r(mean), __builtin_amdgcn_rcpf(sd)};
          *(f32x2*)(sst + (lw * 64 + i * 16 + fr) * 2) = o;
        }
      }
      if (fr == 0) {
        *(f32x4*)(scs + lw * 32 + fq * 4) = accC[0];
        *(f32x4*)(scs + lw * 32 + 16 + fq * 4) = accC[1];
      }
      WAIT_L0();
      BARRIER();
      __builtin_amdgcn_s_setprio(0);
    }
    const int sub = lw & 1;
    auto stageB = [&](int j, int slot) {
      const int t = t_of(j);
      char* dst = smem + slot * K2_STAGE + lw * 8192;
      if (lw < 2) {
        if (t < 64) {
          const u16* A = (t < 32 ? ws_re : ws_im) + (size_t)(rowB + sub * 64 + l3) * S_ + (t & 31) * 64;
#pragma unroll
          for (int i = 0; i < 8; ++i) GLDS16(A + i * 8 * S_ + ((i & 1) ? rt_c1 : rt_c0), dst + i * 1024, 0);
        } else {
          const u16* A = h + (size_t)(rowB + sub * 64 + l3) * FP_ + (t - 64) * 64;
#pragma unroll
          for (int i = 0; i < 8; ++i) GLDS16(A + i * 8 * FP_ + ((i & 1) ? rt_c1 : rt_c0), dst + i * 1024, 16);
        }
      } else {
        if (t < 64) {
          const u16* B = ab + (size_t)(t * 64 + l3) * D_ + n0 + sub * 64;
#pragma unroll
          for (int i = 0; i < 8; ++i) GLDS16(B + i * 8 * D_ + ((i & 1) ? kt_c1 : kt_c0), dst + i * 1024, 16);
        } else {
          const int k0 = (t - 64) * 64;
#pragma unroll
          for (int i = 0; i < 8; ++i) {
            const int krow = min(k0 + i * 8 + l3, F_ - 1);
            GLDS16(w_down + krow * D_ + n0 + sub * 64 + ((i & 1) ? kt_c1 : kt_c0), dst + i * 1024, 0);
          }
        }
      }
    };
    const bool early = lw < 2;
    if (!has_a) BARRIER();
    if (!early) {
      const u64 nonce_ = uniform64(*snonce);
      int last = -1;
#pragma unroll 1
      for (int j = 0; j < K2_NST - 1; ++j) {
        const int fi = dep_flag(j);
        if (fi != last) wait_flag(nonce_, fi);
        last = fi;
      }
    }
#pragma unroll 1
    for (int s = 0; s < K2_NST - 1; ++s) stageB(s, s);
    int islot = K2_NST - 1;
#pragma unroll 1
    for (int j = 0; j < nt; ++j) {
      const int rem = nt - 1 - j;
      if (rem >= 3) WAIT_V(24);
      else if (rem == 2) WAIT_V(16);
      else if (rem == 1) WAIT_V(8);
      else WAIT_V(0);
      BARRIER();
      if (j + K2_NST - 1 < nt) stageB(j + K2_NST - 1, islot);
      islot = (islot == K2_NST - 1) ? 0 : islot + 1;
    }
    return;
  }

  u64 nonce = 0;
  if (wid == 4) nonce = __hip_atomic_load((u64*)(ws + WS_SYNC), RLX_AGENT);
  auto share_nonce = [&]() {
    if (wid == 4) {
      nonce = uniform64(nonce);
      if (lane == 0) {
        *snonce = nonce;
        *scnt = 0u;
        __hip_atomic_store(flag_ptr(ws, FL_ARRIVE + bid), flag_tag(nonce, FL_ARRIVE + bid), RLX_AGENT);
      }
      WAIT_L0();
    }
  };
  const int wm = wid >> 1, wn = wid & 1;
  using C0 = std::integral_constant<int, 0>;
  using C1 = std::integral_constant<int, 1>;
  char* dummy = smem + L_DUMMY;
  auto warm = [&](int slot) {
    if (lane >= 16) return;
    if (slot < 32) {
      const int t = 16 * q + lane;
      TOUCH((t < 32 ? ws_re : ws_im) + (size_t)(rowB + cbk * 32 + slot) * S_ + (t & 31) * 64, dummy);
    } else {
      const int L = (slot - 32) * 16 + lane, per = (nt - 16) * 8;
      const int krow = hq0 * 64 + (idx >> 2) * per + (L >> 1);
      if (L < 2 * per && krow < F_) TOUCH(w_down + (size_t)krow * D_ + n0 + (L & 1) * 64, dummy);
    }
  };
  if (!has_a) {
    share_nonce();
    BARRIER();
#pragma unroll 1
    for (int i = 0; i < 6; ++i) warm(i * 8 + wid);
  }

  if (has_a) {
    f32x4 acc[4][4];
#pragma unroll
    for (int i = 0; i < 4; ++i)
#pragma unroll
      for (int j = 0; j < 4; ++j) acc[i][j] = f32x4{0.f, 0.f, 0.f, 0.f};
    const unsigned a_b0 = sbase + (wm * 64 + fr) * 128 + ch0, a_b1 = sbase + (wm * 64 + fr) * 128 + ch1;
    const unsigned n_b0 = sbase + K1_A + (wn * 64 + fr) * 128 + ch0;
    const unsigned n_b1 = sbase + K1_A + (wn * 64 + fr) * 128 + ch1;
    const unsigned t_p0 = sbase + K1_A + tr_row + 32 * ((wn * 2) ^ tswz);
    const unsigned t_p1 = sbase + K1_A + tr_row + 32 * ((wn * 2 + 1) ^ tswz);

    auto mainloop = [&](auto NKC) {
      constexpr bool NK = decltype(NKC)::value;
      bf16x8 xf0[4], wf0[4], xf1[4], wf1[4];
      auto rdg = [&](auto KSC, bf16x8(&xf)[4], bf16x8(&wf)[4], unsigned so, int g) {
        constexpr int KS = decltype(KSC)::value;
        const unsigned ab_ = (KS ? a_b1 : a_b0) + so;
        switch (g) {
          case 0: xf[0] = lds_rd128<0>(ab_); break;
          case 2: xf[1] = lds_rd128<2048>(ab_); break;
          case 4: xf[2] = lds_rd128<4096>(ab_); break;
          case 6: xf[3] = lds_rd128<6144>(ab_); break;
          default:
            if constexpr (NK) {
              const unsigned nb_ = (KS ? n_b1 : n_b0) + so;
              if (g == 1) wf[0] = lds_rd128<0>(nb_);
              if (g == 3) wf[1] = lds_rd128<2048>(nb_);
              if (g == 5) wf[2] = lds_rd128<4096>(nb_);
              if (g == 7) wf[3] = lds_rd128<6144>(nb_);
            } else {
              if (g == 1) wf[0] = tr_frag<KS * 4096>(t_p0 + so);
              if (g == 3) wf[1] = tr_frag<KS * 4096>(t_p1 + so);
              if (g == 5) wf[2] = tr_frag<KS * 4096 + 8192>(t_p0 + so);
              if (g == 7) wf[3] = tr_frag<KS * 4096 + 8192>(t_p1 + so);
            }
        }
      };
      BARRIER();
#pragma unroll
      for (int g = 0; g < 8; ++g) rdg(C0{}, xf0, wf0, 0u, g);
      WAIT_L0();
      SCHED0();
      unsigned so = 0;
#pragma unroll 1
      for (int t = 0; t < 7; ++t) {
        if (t > 0) warm((t - 1) * 8 + wid);
        mm16(acc, wf0, xf0, [&](int g) { rdg(C1{}, xf1, wf1, so, g); });
        WAIT_L0();
        SCHED0();
        so = (so == 2 * K1_STAGE) ? 0u : so + K1_STAGE;
        BARRIER();
        mm16(acc, wf1, xf1, [&](int g) { rdg(C0{}, xf0, wf0, so, g); });
        WAIT_L0();
        SCHED0();
      }
      mm16(acc, wf0, xf0, [&](int g) { rdg(C1{}, xf1, wf1, so, g); });
      WAIT_L0();
      SCHED0();
      mm16(acc, wf1, xf1, [&](int) {});
    };
    if (nk) mainloop(std::true_type{});
    else mainloop(std::false_type{});
    share_nonce();
    BARRIER();
    float mb[4], rinv[4];
#pragma unroll
    for (int mi = 0; mi < 4; ++mi) {
      f32x2 v = *(const f32x2*)(sst + (wm * 64 + mi * 16 + fr) * 2);
      mb[mi] = v[0];
      rinv[mi] = v[1];
    }
    f32x4 csv[4];
#pragma unroll
    for (int ni = 0; ni < 4; ++ni) {
      const int ci = nk ? (wn * 64 + ni * 16) : ((ni >> 1) * 64 + wn * 32 + (ni & 1) * 16);
      csv[ni] = *(const f32x4*)(scs + ci + fq * 4);
    }
    char* scr = smem + L_SCR + wid * 2048;
    const __amdgpu_buffer_rsrc_t rsw = __builtin_amdgcn_make_buffer_rsrc(ws, 0, 16u << 20, 0x00020000);
    if (nk) {
      const float sgn = cb < 4 ? 1.0f : -1.0f;
      const unsigned gbase = (unsigned)WS_AB +
          (unsigned)(((cb >> 2) * S_ + rowA + wm * 64 + (lane >> 3)) * D_ + (cb & 3) * 128 + wn * 64 + (lane & 7) * 8) * 2u;
      u32x2 o[4][4];
#pragma unroll
      for (int mi = 0; mi < 4; ++mi) {
        const float r = rinv[mi] * sgn;
#pragma unroll
        for (int ni = 0; ni < 4; ++ni) {
          f32x4 v = (acc[ni][mi] - mb[mi] * csv[ni]) * r;
          o[mi][ni] = u32x2{pack2(v[0], v[1]), pack2(v[2], v[3])};
        }
      }
#pragma unroll
      for (int mi = 0; mi < 4; ++mi) {
#pragma unroll
        for (int ni = 0; ni < 4; ++ni)
          *(u32x2*)(scr + fr * 128 + (((ni * 2 + (fq >> 1)) ^ (fr & 7)) * 16) + (fq & 1) * 8) = o[mi][ni];
#pragma unroll
        for (int hf = 0; hf < 2; ++hf) {
          const int r2 = hf * 8 + (lane >> 3);
          u32x4 v = *(const u32x4*)(scr + r2 * 128 + (((lane & 7) ^ (r2 & 7)) * 16));
          __builtin_amdgcn_raw_buffer_store_b128(v, rsw, gbase + (unsigned)((mi * 16 + hf * 8) * D_ * 2), 0, 16);
        }
      }
    } else {
      const unsigned vmask = (f0 + wn * 32) < F_ ? 0xffffffffu : 0u;
      const unsigned gbase = (unsigned)WS_H +
          (unsigned)((rowA + wm * 64 + (lane >> 2)) * FP_ + f0 + wn * 32 + (lane & 3) * 8) * 2u;
#pragma unroll
      for (int mp = 0; mp < 2; ++mp) {
#pragma unroll
        for (int mj = 0; mj < 2; ++mj) {
          const int mi = mp * 2 + mj;
#pragma unroll
          for (int ni = 0; ni < 2; ++ni) {
            f32x4 g = (acc[ni][mi] - mb[mi] * csv[ni]) * rinv[mi];
            f32x4 u = (acc[ni + 2][mi] - mb[mi] * csv[ni + 2]) * rinv[mi];
            float hv[4];
#pragma unroll
            for (int e = 0; e < 4; ++e) hv[e] = gelu_tanh(g[e]) * u[e];
            u32x2 o = {pack2(hv[0], hv[1]) & vmask, pack2(hv[2], hv[3]) & vmask};
            *(u32x2*)(scr + mj * 1024 + fr * 64 + (((ni * 2 + (fq >> 1)) ^ ((fr >> 1) & 3)) * 16) + (fq & 1) * 8) = o;
          }
        }
        const int r2 = lane >> 2;
#pragma unroll
        for (int mj = 0; mj < 2; ++mj) {
          u32x4 v = *(const u32x4*)(scr + mj * 1024 + r2 * 64 + (((lane & 3) ^ ((r2 >> 1) & 3)) * 16));
          __builtin_amdgcn_raw_buffer_store_b128(v, rsw, gbase + (unsigned)((mp * 2 + mj) * 16 * FP_ * 2), 0, 16);
        }
      }
    }
  }
  WAIT_V(0);
  if (has_a && lane == 0) __hip_atomic_fetch_add(scnt, 1u, __ATOMIC_RELAXED, __HIP_MEMORY_SCOPE_WORKGROUP);
  if (wid > 4) return;

  if (wid == 4) {
    int last = dep_flag(K2_NST - 2);
#pragma unroll 1
    for (int j = 0; j < nt; ++j) {
      if (j + K2_NST - 1 < nt) {
        const int fi = dep_flag(j + K2_NST - 1);
        if (fi != last) wait_flag(nonce, fi);
        last = fi;
      }
      BARRIER();
    }
    return;
  }

  if (wid == 0) {
    nonce = uniform64(*snonce);
    if (has_a) {
#pragma unroll 1
      for (unsigned spins = 0; spins < SPIN_CAP; ++spins) {
        const unsigned c = __builtin_amdgcn_readfirstlane(__hip_atomic_load(scnt, __ATOMIC_RELAXED, __HIP_MEMORY_SCOPE_WORKGROUP));
        if (c >= 8u) break;
        __builtin_amdgcn_s_sleep(1);
      }
      if (lane == 0) __hip_atomic_store(flag_ptr(ws, FL_READY1 + nb), flag_tag(nonce, FL_READY1 + nb), RLX_AGENT);
    }
  }
  {
    f32x4 acc[4][4];
#pragma unroll
    for (int i = 0; i < 4; ++i)
#pragma unroll
      for (int j = 0; j < 4; ++j) acc[i][j] = f32x4{0.f, 0.f, 0.f, 0.f};
    const unsigned a_base0 = sbase + (wm * 64 + fr) * 128 + ch0;
    const unsigned a_base1 = sbase + (wm * 64 + fr) * 128 + ch1;
    const unsigned b_base = sbase + 16384 + wn * 8192 + tr_row;
    unsigned b_off[4];
#pragma unroll
    for (int ni = 0; ni < 4; ++ni) b_off[ni] = b_base + 32 * (ni ^ tswz);
    bf16x8 af0[4], bf0[4], af1[4], bf1[4];
    auto rdg = [&](auto KSC, bf16x8(&af)[4], bf16x8(&bfr)[4], unsigned so, int g) {
      constexpr int KS = decltype(KSC)::value;
      const unsigned ab_ = (KS ? a_base1 : a_base0) + so;
      switch (g) {
        case 0: af[0] = lds_rd128<0>(ab_); break;
        case 2: af[1] = lds_rd128<2048>(ab_); break;
        case 4: af[2] = lds_rd128<4096>(ab_); break;
        case 6: af[3] = lds_rd128<6144>(ab_); break;
        case 1: bfr[0] = tr_frag<KS * 4096>(b_off[0] + so); break;
        case 3: bfr[1] = tr_frag<KS * 4096>(b_off[1] + so); break;
        case 5: bfr[2] = tr_frag<KS * 4096>(b_off[2] + so); break;
        default: bfr[3] = tr_frag<KS * 4096>(b_off[3] + so); break;
      }
    };
    BARRIER();
#pragma unroll
    for (int g = 0; g < 8; ++g) rdg(C0{}, af0, bf0, 0u, g);
    WAIT_L0();
    SCHED0();
    unsigned so = 0;
#pragma unroll 1
    for (int j = 0; j < nt - 1; ++j) {
      mm16(acc, bf0, af0, [&](int g) { rdg(C1{}, af1, bf1, so, g); });
      WAIT_L0();
      SCHED0();
      so = (so == (K2_NST - 1) * K2_STAGE) ? 0u : so + K2_STAGE;
      BARRIER();
      mm16(acc, bf1, af1, [&](int g) { rdg(C0{}, af0, bf0, so, g); });
      WAIT_L0();
      SCHED0();
    }
    mm16(acc, bf0, af0, [&](int g) { rdg(C1{}, af1, bf1, so, g); });
    WAIT_L0();
    SCHED0();
    mm16(acc, bf1, af1, [&](int) {});
    const __amdgpu_buffer_rsrc_t rs = __builtin_amdgcn_make_buffer_rsrc(ws + WS_SLAB, 0, 8u << 20, 0x00020000);
    f32x4 own[4];
#pragma unroll
    for (int mi = 0; mi < 4; ++mi) {
      if (mi == q) {
#pragma unroll
        for (int ni = 0; ni < 4; ++ni) own[ni] = acc[ni][mi];
      } else {
        const unsigned base = (unsigned)((((tile * 4 + mi) * 4 + q) * 4 + wid) * 2) * 1024u + lane * 16u;
#pragma unroll
        for (int np = 0; np < 2; ++np) {
          const f32x4 a0 = acc[2 * np][mi], a1 = acc[2 * np + 1][mi];
          u32x4 v = {pack2(a0[0], a0[1]), pack2(a0[2], a0[3]), pack2(a1[0], a1[1]), pack2(a1[2], a1[3])};
          __builtin_amdgcn_raw_buffer_store_b128(v, rs, base + np * 1024u, 0, 16);
        }
      }
    }
    const size_t orow = (size_t)(rowB + wm * 64 + q * 16 + fr) * D_ + n0 + wn * 64 + fq * 4;
    u32x2 xv[4];
#pragma unroll
    for (int ni = 0; ni < 4; ++ni) xv[ni] = *(const u32x2*)(x + orow + ni * 16);
    WAIT_V(0);
    BARRIER();
    if (wid == 0) {
      if (lane == 0)
        __hip_atomic_store(flag_ptr(ws, FL_READY2 + tile * 4 + q), flag_tag(nonce, FL_READY2 + tile * 4 + q), RLX_AGENT);
      const int fi = FL_READY2 + tile * 4 + (lane & 3);
      const u64 want = flag_tag(nonce, fi);
      u64* p = flag_ptr(ws, fi);
#pragma unroll 1
      for (unsigned spins = 0; spins < SPIN_CAP; ++spins) {
        const u64 v = __hip_atomic_load(p, RLX_AGENT);
        if (__all(v == want || (lane & 3) == q)) break;
        __builtin_amdgcn_s_sleep(1);
      }
    }
    BARRIER();
    u32x4 pv[3][2];
#pragma unroll
    for (int sidx = 0; sidx < 3; ++sidx) {
      const int src = sidx + (sidx >= q ? 1 : 0);
      const unsigned base = (unsigned)((((tile * 4 + q) * 4 + src) * 4 + wid) * 2) * 1024u + lane * 16u;
#pragma unroll
      for (int np = 0; np < 2; ++np) pv[sidx][np] = __builtin_amdgcn_raw_buffer_load_b128(rs, base + np * 1024u, 0, 16);
    }
#pragma unroll
    for (int ni = 0; ni < 4; ++ni) {
      f32x4 v = own[ni];
#pragma unroll
      for (int sidx = 0; sidx < 3; ++sidx) {
        const unsigned w0 = pv[sidx][ni >> 1][(ni & 1) * 2], w1 = pv[sidx][ni >> 1][(ni & 1) * 2 + 1];
        v[0] += bflo(w0); v[1] += bfhi(w0); v[2] += bflo(w1); v[3] += bfhi(w1);
      }
      u32x2 o = {pack2(v[0] + bflo(xv[ni][0]), v[1] + bfhi(xv[ni][0])), pack2(v[2] + bflo(xv[ni][1]), v[3] + bfhi(xv[ni][1]))};
      *(u32x2*)(out + orow + ni * 16) = o;
    }
  }
  if (bid == 0 && wid == 0) {
#pragma unroll 1
    for (unsigned spins = 0; spins < SPIN_CAP; ++spins) {
      bool ok = true;
#pragma unroll
      for (int i = 0; i < 4; ++i) {
        const int fi = FL_ARRIVE + i * 64 + lane;
        ok &= __hip_atomic_load(flag_ptr(ws, fi), RLX_AGENT) == flag_tag(nonce, fi);
      }
      if (__all(ok)) break;
      __builtin_amdgcn_s_sleep(1);
    }
    if (lane == 0) __hip_atomic_store((u64*)(ws + WS_SYNC), nonce + 1, RLX_AGENT);
  }
}

extern "C" void kernel_launch(void* const* d_in, const int* in_sizes, int n_in, void* d_out, int out_size,
                              void* d_ws, size_t ws_size, hipStream_t stream) {
  fused_kernel<<<256, 768, 0, stream>>>((const u16*)d_in[0], (const u16*)d_in[1], (const u16*)d_in[2],
                                        (const u16*)d_in[3], (const u16*)d_in[4], (const u16*)d_in[6],
                                        (const u16*)d_in[7], (const u16*)d_in[8], (u16*)d_out, (char*)d_ws);
}
```

```cpp
#include <hip/hip_runtime.h>
#include <hip/hip_bf16.h>
#include <stdint.h>
#include <type_traits>

typedef unsigned short u16;
typedef unsigned long long u64;
typedef short bf16x8 __attribute__((ext_vector_type(8)));
typedef short bf16x4 __attribute__((ext_vector_type(4)));
typedef float f32x4 __attribute__((ext_vector_type(4)));
typedef float f32x2 __attribute__((ext_vector_type(2)));
typedef unsigned u32x4 __attribute__((ext_vector_type(4)));
typedef unsigned u32x2 __attribute__((ext_vector_type(2)));

constexpr int S_ = 2048, D_ = 512, F_ = 1376, FP_ = 1408;

#define LDS_AS __attribute__((address_space(3)))
#define GLDS16(gptr, ldsptr, aux) \
  __builtin_amdgcn_global_load_lds((const unsigned*)(gptr), (unsigned*)(ldsptr), 16, 0, aux)
#define WAIT_V(n) asm volatile("s_waitcnt vmcnt(" #n ")" ::: "memory")
#define BARRIER() asm volatile("s_barrier" ::: "memory")
#define WAIT_L0() asm volatile("s_waitcnt lgkmcnt(0)" ::: "memory")
#define SCHED0() __builtin_amdgcn_sched_barrier(0)
#define RLX_AGENT __ATOMIC_RELAXED, __HIP_MEMORY_SCOPE_AGENT

__device__ __forceinline__ float bf16r(float x) { return __bfloat162float(__float2bfloat16(x)); }
typedef __bf16 bf2_t __attribute__((ext_vector_type(2)));
__device__ __forceinline__ unsigned pack2(float lo, float hi) {
  f32x2 v = {lo, hi};
  return __builtin_bit_cast(unsigned, __builtin_convertvector(v, bf2_t));
}
__device__ __forceinline__ float bflo(unsigned v) { return __builtin_bit_cast(float, v << 16); }
__device__ __forceinline__ float bfhi(unsigned v) { return __builtin_bit_cast(float, v & 0xffff0000u); }

template <int OFF>
__device__ __forceinline__ bf16x8 tr_frag(unsigned addr) {
  bf16x4 a, b;
  asm volatile("ds_read_b64_tr_b16 %0, %2 offset:%3\n\tds_read_b64_tr_b16 %1, %2 offset:%4"
               : "=&v"(a), "=&v"(b)
               : "v"(addr), "i"(OFF), "i"(OFF + 512));
  return __builtin_shufflevector(a, b, 0, 1, 2, 3, 4, 5, 6, 7);
}
template <int OFF>
__device__ __forceinline__ bf16x8 lds_rd128(unsigned addr) {
  bf16x8 a;
  asm volatile("ds_read_b128 %0, %1 offset:%2" : "=&v"(a) : "v"(addr), "i"(OFF));
  return a;
}
__device__ __forceinline__ unsigned lds_addr(const void* p) { return (unsigned)(uintptr_t)(const LDS_AS char*)p; }
__device__ __forceinline__ float gelu_tanh(float g) {
  const float c1 = -2.0f * 0.7978845608028654f * 1.4426950408889634f;
  const float c2 = c1 * 0.044715f;
  float w = g * __builtin_fmaf(g * g, c2, c1);
  float e = __builtin_amdgcn_exp2f(w);
  return g * __builtin_amdgcn_rcpf(1.0f + e);
}
#define TOUCH(gptr, ldsptr) __builtin_amdgcn_global_load_lds((const unsigned*)(gptr), (unsigned*)(ldsptr), 4, 0, 0)
template <class RD>
__device__ __forceinline__ void mm16(f32x4 (&acc)[4][4], const bf16x8 (&wf)[4], const bf16x8 (&xf)[4], RD rd) {
#pragma unroll
  for (int i = 0; i < 16; ++i) {
    const int ni = i >> 2, mi = i & 3;
    acc[ni][mi] = __builtin_amdgcn_mfma_f32_16x16x32_bf16(wf[ni], xf[mi], acc[ni][mi], 0, 0, 0);
    if (i < 8) {
      SCHED0();
      rd(i);
      SCHED0();
    }
  }
  SCHED0();
}

constexpr size_t WS_AB = 0;
constexpr size_t WS_H = 4u << 20;
constexpr size_t WS_SLAB = 16u << 20;
constexpr size_t WS_SYNC = 32u << 20;
constexpr int FL_ARRIVE = 0, FL_READY1 = 256, FL_READY2 = 512;
__device__ __forceinline__ u64* flag_ptr(char* ws, int idx) { return (u64*)(ws + WS_SYNC + 4096 + (size_t)idx * 64); }
__device__ __forceinline__ u64 flag_tag(u64 nonce, int idx) { return nonce + (u64)(idx + 1) * 0x9E3779B97F4A7C15ull; }
constexpr unsigned SPIN_CAP = 1u << 17;

constexpr int K1_A = 32768, K1_B = 16384, K1_STAGE = K1_A + K1_B;
constexpr int K2_STAGE = 32768, K2_NST = 5;
constexpr int L_SCR = 131072;
constexpr int L_SST = 147456, L_SCS = L_SST + 2048, L_NONCE = L_SCS + 512, L_CNT = L_NONCE + 64, L_DUMMY = L_NONCE + 256;

__global__ __launch_bounds__(768, 3) void fused_kernel(const u16* __restrict__ x, const u16* __restrict__ ws_re,
                                                       const u16* __restrict__ ws_im, const u16* __restrict__ wd_re,
                                                       const u16* __restrict__ wd_im, const u16* __restrict__ w_gate,
                                                       const u16* __restrict__ w_up, const u16* __restrict__ w_down,
                                                       u16* __restrict__ out, char* ws) {
  __shared__ __attribute__((aligned(1024))) char smem[K2_NST * K2_STAGE];
  const int tid = threadIdx.x, wid = __builtin_amdgcn_readfirstlane(tid >> 6), lane = tid & 63;
  const int fr = lane & 15, fq = lane >> 4;
  const int bid = blockIdx.x;
  u16* ab = (u16*)(ws + WS_AB);
  u16* h = (u16*)(ws + WS_H);

  const bool has_a = bid < 240;
  const int nb = (bid & 7) * 30 + (bid >> 3);
  const int cb = nb >> 3, rbA = nb & 7;
  const bool nk = cb < 8;
  const int rowA = rbA * 256;
  const int f0 = (cb - 8) * 64;
  const int idx = bid >> 3;
  const int q = (bid & 7) >> 1;
  const int rbB = (bid & 1) * 8 + (idx >> 2), cbk = idx & 3;
  const int rowB = rbB * 128, n0 = cbk * 128, tile = rbB * 4 + cbk;
  const int hq0 = q < 2 ? 6 * q : 12 + 5 * (q - 2);
  const int nt = q < 2 ? 22 : 21;
  auto t_of = [&](int j) { return j < 16 ? 16 * q + j : 64 + hq0 + (j - 16); };

  const int sw = (fr >> 1) & 7;
  const int ch0 = ((fq) ^ sw) * 16, ch1 = ((4 + fq) ^ sw) * 16;
  const int tq = (lane >> 2) & 3, tp = lane & 3;
  const int tswz = ((tq >> 1) & 1) | ((fq & 1) << 1);
  const int tr_row = (8 * fq + tq) * 128 + 8 * tp;
  float* sst = (float*)(smem + L_SST);
  float* scs = (float*)(smem + L_SCS);
  u64* snonce = (u64*)(smem + L_NONCE);
  const unsigned sbase = lds_addr(smem);
  unsigned* scnt = (unsigned*)(smem + L_CNT);
  auto dep_flag = [&](int j) {
    const int t = t_of(j);
    return FL_READY1 + (t < 64 ? (((t >> 5) * 4 + cbk) * 8 + ((t & 31) >> 2)) : ((8 + t - 64) * 8 + (rbB >> 1)));
  };
  auto wait_flag = [&](u64 nonce_, int fi) {
    const u64 want = flag_tag(nonce_, fi);
    u64* p = flag_ptr(ws, fi);
#pragma unroll 1
    for (unsigned spins = 0; spins < SPIN_CAP; ++spins) {
      const u64 v = __hip_atomic_load(p, RLX_AGENT);
      const bool ok = __builtin_amdgcn_readfirstlane((unsigned)(v == want)) != 0;
      if (ok) break;
      __builtin_amdgcn_s_sleep(1);
    }
  };
  auto uniform64 = [&](u64 v) {
    return ((u64)__builtin_amdgcn_readfirstlane((unsigned)(v >> 32)) << 32) | (u64)__builtin_amdgcn_readfirstlane((unsigned)v);
  };

  if (wid >= 8) {
    const int lw = wid - 8;
    const int l3 = lane >> 3, l7 = lane & 7;
    const int rt_c0 = (l7 ^ (fq & 7)) * 8, rt_c1 = (l7 ^ ((fq + 4) & 7)) * 8;
    const int kt_c0 = (l7 ^ ((fq & 1) << 1)) * 8, kt_c1 = (l7 ^ (((fq & 1) | 2) << 1)) * 8;
    if (has_a) {
      __builtin_amdgcn_s_setprio(2);
      const u16* xa0 = x + (size_t)(rowA + lw * 64 + l3) * D_ + rt_c0;
      const u16* xa1 = x + (size_t)(rowA + lw * 64 + 8 + l3) * D_ + rt_c1;
      const u16 *wb0, *wb1;
      int wstep_j, wstep_t;
      if (nk) {
        const u16* wnk = (cb < 4 ? wd_re : wd_im) + (size_t)((cb & 3) * 128 + lw * 32 + l3) * D_;
        wb0 = wnk + rt_c0;
        wb1 = wnk + 8 * D_ + rt_c1;
        wstep_j = 16 * D_;
        wstep_t = 64;
      } else {
        const u16* w = (lw < 2) ? w_gate : w_up;
        int c0 = f0 + kt_c0, c1 = f0 + kt_c1;
        if (c0 >= F_) c0 -= 32;
        if (c1 >= F_) c1 -= 32;
        const u16* wk = w + (size_t)((lw & 1) * 32 + l3) * F_;
        wb0 = wk + c0;
        wb1 = wk + 8 * F_ + c1;
        wstep_j = 16 * F_;
        wstep_t = 64 * F_;
      }
      auto stage = [&](int tj, int buf) {
        const int t = (tj + cb) & 7;
        char* sa = smem + buf * K1_STAGE + lw * 8192;
        char* sb = smem + buf * K1_STAGE + K1_A + lw * 4096;
#pragma unroll
        for (int i = 0; i < 4; ++i) {
          GLDS16(xa0 + i * 16 * D_ + t * 64, sa + i * 2048, 0);
          GLDS16(xa1 + i * 16 * D_ + t * 64, sa + i * 2048 + 1024, 0);
        }
#pragma unroll
        for (int i = 0; i < 2; ++i) {
          GLDS16(wb0 + i * wstep_j + t * wstep_t, sb + i * 2048, 0);
          GLDS16(wb1 + i * wstep_j + t * wstep_t, sb + i * 2048 + 1024, 0);
        }
      };
#pragma unroll 1
      for (int t = 0; t < 2; ++t) stage(t, t);
      f32x4 accS[4], accG[4], accC[2];
#pragma unroll
      for (int i = 0; i < 4; ++i) {
        accS[i] = f32x4{0.f, 0.f, 0.f, 0.f};
        accG[i] = f32x4{0.f, 0.f, 0.f, 0.f};
      }
      accC[0] = f32x4{0.f, 0.f, 0.f, 0.f};
      accC[1] = f32x4{0.f, 0.f, 0.f, 0.f};
      const short ONE = 0x3f80;
      const bf16x8 ones = {ONE, ONE, ONE, ONE, ONE, ONE, ONE, ONE};
      const unsigned la0 = sbase + (lw * 64 + fr) * 128 + ch0;
      const unsigned la1 = sbase + (lw * 64 + fr) * 128 + ch1;
      const unsigned lb0 = sbase + K1_A + (lw * 32 + fr) * 128 + ch0;
      const unsigned lb1 = sbase + K1_A + (lw * 32 + fr) * 128 + ch1;
      const unsigned lt0 = sbase + K1_A + (lw >> 1) * 8192 + tr_row + 32 * (((lw & 1) * 2) ^ tswz);
      const unsigned lt1 = sbase + K1_A + (lw >> 1) * 8192 + tr_row + 32 * (((lw & 1) * 2 + 1) ^ tswz);
      int ib = 2, buf = 0;
#pragma unroll 1
      for (int t = 0; t < 8; ++t) {
        if (t < 7) WAIT_V(12); else WAIT_V(0);
        BARRIER();
        const unsigned so = buf * K1_STAGE;
        bf16x8 xf[8], wf[4];
        xf[0] = lds_rd128<0>(la0 + so); xf[1] = lds_rd128<2048>(la0 + so);
        xf[2] = lds_rd128<4096>(la0 + so); xf[3] = lds_rd128<6144>(la0 + so);
        xf[4] = lds_rd128<0>(la1 + so); xf[5] = lds_rd128<2048>(la1 + so);
        xf[6] = lds_rd128<4096>(la1 + so); xf[7] = lds_rd128<6144>(la1 + so);
        if (nk) {
          wf[0] = lds_rd128<0>(lb0 + so); wf[1] = lds_rd128<2048>(lb0 + so);
          wf[2] = lds_rd128<0>(lb1 + so); wf[3] = lds_rd128<2048>(lb1 + so);
        } else {
          wf[0] = tr_frag<0>(lt0 + so); wf[1] = tr_frag<0>(lt1 + so);
          wf[2] = tr_frag<4096>(lt0 + so); wf[3] = tr_frag<4096>(lt1 + so);
        }
        if (t + 2 < 8) stage(t + 2, ib);
        ib = (ib == 2) ? 0 : ib + 1;
        WAIT_L0();
        SCHED0();
#pragma unroll
        for (int i = 0; i < 8; ++i) {
          accS[i & 3] = __builtin_amdgcn_mfma_f32_16x16x32_bf16(ones, xf[i], accS[i & 3], 0, 0, 0);
          accG[i & 3] = __builtin_amdgcn_mfma_f32_16x16x32_bf16(xf[i], xf[i], accG[i & 3], 0, 0, 0);
        }
#pragma unroll
        for (int i = 0; i < 4; ++i)
          accC[i & 1] = __builtin_amdgcn_mfma_f32_16x16x32_bf16(wf[i], ones, accC[i & 1], 0, 0, 0);
        buf = (buf == 2) ? 0 : buf + 1;
      }
      if (fq == (fr >> 2)) {
#pragma unroll
        for (int i = 0; i < 4; ++i) {
          const int r = fr & 3;
          const float qq = r == 0 ? accG[i][0] : r == 1 ? accG[i][1] : r == 2 ? accG[i][2] : accG[i][3];
          const float mean = accS[i][0] * (1.0f / D_);
          const float var = fmaxf(qq * (1.0f / D_) - mean * mean, 0.f);
          const float sd = bf16r(sqrtf(bf16r(bf16r(var) + 1e-5f)));
          f32x2 o = {bf16r(mean), __builtin_amdgcn_rcpf(sd)};
          *(f32x2*)(sst + (lw * 64 + i * 16 + fr) * 2) = o;
        }
      }
      if (fr == 0) {
        *(f32x4*)(scs + lw * 32 + fq * 4) = accC[0];
        *(f32x4*)(scs + lw * 32 + 16 + fq * 4) = accC[1];
      }
      WAIT_L0();
      BARRIER();
      __builtin_amdgcn_s_setprio(0);
    }
    const int sub = lw & 1;
    auto stageB = [&](int j, int slot) {
      const int t = t_of(j);
      char* dst = smem + slot * K2_STAGE + lw * 8192;
      if (lw < 2) {
        if (t < 64) {
          const u16* A = (t < 32 ? ws_re : ws_im) + (size_t)(rowB + sub * 64 + l3) * S_ + (t & 31) * 64;
#pragma unroll
          for (int i = 0; i < 8; ++i) GLDS16(A + i * 8 * S_ + ((i & 1) ? rt_c1 : rt_c0), dst + i * 1024, 0);
        } else {
          const u16* A = h + (size_t)(rowB + sub * 64 + l3) * FP_ + (t - 64) * 64;
#pragma unroll
          for (int i = 0; i < 8; ++i) GLDS16(A + i * 8 * FP_ + ((i & 1) ? rt_c1 : rt_c0), dst + i * 1024, 16);
        }
      } else {
        if (t < 64) {
          const u16* B = ab + (size_t)(t * 64 + l3) * D_ + n0 + sub * 64;
#pragma unroll
          for (int i = 0; i < 8; ++i) GLDS16(B + i * 8 * D_ + ((i & 1) ? kt_c1 : kt_c0), dst + i * 1024, 16);
        } else {
          const int k0 = (t - 64) * 64;
#pragma unroll
          for (int i = 0; i < 8; ++i) {
            const int krow = min(k0 + i * 8 + l3, F_ - 1);
            GLDS16(w_down + krow * D_ + n0 + sub * 64 + ((i & 1) ? kt_c1 : kt_c0), dst + i * 1024, 0);
          }
        }
      }
    };
    const bool early = lw < 2;
    if (!has_a) BARRIER();
    if (!early) {
      const u64 nonce_ = uniform64(*snonce);
      int last = -1;
#pragma unroll 1
      for (int j = 0; j < K2_NST - 1; ++j) {
        const int fi = dep_flag(j);
        if (fi != last) wait_flag(nonce_, fi);
        last = fi;
      }
    }
#pragma unroll 1
    for (int s = 0; s < K2_NST - 1; ++s) stageB(s, s);
    int islot = K2_NST - 1;
#pragma unroll 1
    for (int j = 0; j < nt; ++j) {
      const int rem = nt - 1 - j;
      if (rem >= 3) WAIT_V(24);
      else if (rem == 2) WAIT_V(16);
      else if (rem == 1) WAIT_V(8);
      else WAIT_V(0);
      BARRIER();
      if (j + K2_NST - 1 < nt) stageB(j + K2_NST - 1, islot);
      islot = (islot == K2_NST - 1) ? 0 : islot + 1;
    }
    return;
  }

  u64 nonce = 0;
  if (wid == 4) nonce = __hip_atomic_load((u64*)(ws + WS_SYNC), RLX_AGENT);
  auto share_nonce = [&]() {
    if (wid == 4) {
      nonce = uniform64(nonce);
      if (lane == 0) {
        *snonce = nonce;
        *scnt = 0u;
        __hip_atomic_store(flag_ptr(ws, FL_ARRIVE + bid), flag_tag(nonce, FL_ARRIVE + bid), RLX_AGENT);
      }
      WAIT_L0();
    }
  };
  const int wm = wid >> 1, wn = wid & 1;
  using C0 = std::integral_constant<int, 0>;
  using C1 = std::integral_constant<int, 1>;
  char* dummy = smem + L_DUMMY;
  auto warm = [&](int slot) {
    if (lane >= 16) return;
    if (slot < 32) {
      const int t = 16 * q + lane;
      TOUCH((t < 32 ? ws_re : ws_im) + (size_t)(rowB + cbk * 32 + slot) * S_ + (t & 31) * 64, dummy);
    } else {
      const int L = (slot - 32) * 16 + lane, per = (nt - 16) * 8;
      const int krow = hq0 * 64 + (idx >> 2) * per + (L >> 1);
      if (L < 2 * per && krow < F_) TOUCH(w_down + (size_t)krow * D_ + n0 + (L & 1) * 64, dummy);
    }
  };
  if (!has_a) {
    share_nonce();
    BARRIER();
#pragma unroll 1
    for (int i = 0; i < 6; ++i) warm(i * 8 + wid);
  }

  if (has_a) {
    f32x4 acc[4][4];
#pragma unroll
    for (int i = 0; i < 4; ++i)
#pragma unroll
      for (int j = 0; j < 4; ++j) acc[i][j] = f32x4{0.f, 0.f, 0.f, 0.f};
    const unsigned a_b0 = sbase + (wm * 64 + fr) * 128 + ch0, a_b1 = sbase + (wm * 64 + fr) * 128 + ch1;
    const unsigned n_b0 = sbase + K1_A + (wn * 64 + fr) * 128 + ch0;
    const unsigned n_b1 = sbase + K1_A + (wn * 64 + fr) * 128 + ch1;
    const unsigned t_p0 = sbase + K1_A + tr_row + 32 * ((wn * 2) ^ tswz);
    const unsigned t_p1 = sbase + K1_A + tr_row + 32 * ((wn * 2 + 1) ^ tswz);

    auto mainloop = [&](auto NKC) {
      constexpr bool NK = decltype(NKC)::value;
      bf16x8 xf0[4], wf0[4], xf1[4], wf1[4];
      auto rdg = [&](auto KSC, bf16x8(&xf)[4], bf16x8(&wf)[4], unsigned so, int g) {
        constexpr int KS = decltype(KSC)::value;
        const unsigned ab_ = (KS ? a_b1 : a_b0) + so;
        switch (g) {
          case 0: xf[0] = lds_rd128<0>(ab_); break;
          case 2: xf[1] = lds_rd128<2048>(ab_); break;
          case 4: xf[2] = lds_rd128<4096>(ab_); break;
          case 6: xf[3] = lds_rd128<6144>(ab_); break;
          default:
            if constexpr (NK) {
              const unsigned nb_ = (KS ? n_b1 : n_b0) + so;
              if (g == 1) wf[0] = lds_rd128<0>(nb_);
              if (g == 3) wf[1] = lds_rd128<2048>(nb_);
              if (g == 5) wf[2] = lds_rd128<4096>(nb_);
              if (g == 7) wf[3] = lds_rd128<6144>(nb_);
            } else {
              if (g == 1) wf[0] = tr_frag<KS * 4096>(t_p0 + so);
              if (g == 3) wf[1] = tr_frag<KS * 4096>(t_p1 + so);
              if (g == 5) wf[2] = tr_frag<KS * 4096 + 8192>(t_p0 + so);
              if (g == 7) wf[3] = tr_frag<KS * 4096 + 8192>(t_p1 + so);
            }
        }
      };
      BARRIER();
#pragma unroll
      for (int g = 0; g < 8; ++g) rdg(C0{}, xf0, wf0, 0u, g);
      WAIT_L0();
      SCHED0();
      unsigned so = 0;
#pragma unroll 1
      for (int t = 0; t < 7; ++t) {
        if (t > 0) warm((t - 1) * 8 + wid);
        mm16(acc, wf0, xf0, [&](int g) { rdg(C1{}, xf1, wf1, so, g); });
        WAIT_L0();
        SCHED0();
        so = (so == 2 * K1_STAGE) ? 0u : so + K1_STAGE;
        BARRIER();
        mm16(acc, wf1, xf1, [&](int g) { rdg(C0{}, xf0, wf0, so, g); });
        WAIT_L0();
        SCHED0();
      }
      mm16(acc, wf0, xf0, [&](int g) { rdg(C1{}, xf1, wf1, so, g); });
      WAIT_L0();
      SCHED0();
      mm16(acc, wf1, xf1, [&](int) {});
    };
    if (nk) mainloop(std::true_type{});
    else mainloop(std::false_type{});
    share_nonce();
    BARRIER();
    float mb[4], rinv[4];
#pragma unroll
    for (int mi = 0; mi < 4; ++mi) {
      f32x2 v = *(const f32x2*)(sst + (wm * 64 + mi * 16 + fr) * 2);
      mb[mi] = v[0];
      rinv[mi] = v[1];
    }
    f32x4 csv[4];
#pragma unroll
    for (int ni = 0; ni < 4; ++ni) {
      const int ci = nk ? (wn * 64 + ni * 16) : ((ni >> 1) * 64 + wn * 32 + (ni & 1) * 16);
      csv[ni] = *(const f32x4*)(scs + ci + fq * 4);
    }
    char* scr = smem + L_SCR + wid * 2048;
    const __amdgpu_buffer_rsrc_t rsw = __builtin_amdgcn_make_buffer_rsrc(ws, 0, 16u << 20, 0x00020000);
    if (nk) {
      const float sgn = cb < 4 ? 1.0f : -1.0f;
      const unsigned gbase = (unsigned)WS_AB +
          (unsigned)(((cb >> 2) * S_ + rowA + wm * 64 + (lane >> 3)) * D_ + (cb & 3) * 128 + wn * 64 + (lane & 7) * 8) * 2u;
      u32x2 o[4][4];
#pragma unroll
      for (int mi = 0; mi < 4; ++mi) {
        const float r = rinv[mi] * sgn;
#pragma unroll
        for (int ni = 0; ni < 4; ++ni) {
          f32x4 v = (acc[ni][mi] - mb[mi] * csv[ni]) * r;
          o[mi][ni] = u32x2{pack2(v[0], v[1]), pack2(v[2], v[3])};
        }
      }
#pragma unroll
      for (int mi = 0; mi < 4; ++mi) {
#pragma unroll
        for (int ni = 0; ni < 4; ++ni)
          *(u32x2*)(scr + fr * 128 + (((ni * 2 + (fq >> 1)) ^ (fr & 7)) * 16) + (fq & 1) * 8) = o[mi][ni];
#pragma unroll
        for (int hf = 0; hf < 2; ++hf) {
          const int r2 = hf * 8 + (lane >> 3);
          u32x4 v = *(const u32x4*)(scr + r2 * 128 + (((lane & 7) ^ (r2 & 7)) * 16));
          __builtin_amdgcn_raw_buffer_store_b128(v, rsw, gbase + (unsigned)((mi * 16 + hf * 8) * D_ * 2), 0, 16);
        }
      }
    } else {
      const unsigned vmask = (f0 + wn * 32) < F_ ? 0xffffffffu : 0u;
      const unsigned gbase = (unsigned)WS_H +
          (unsigned)((rowA + wm * 64 + (lane >> 2)) * FP_ + f0 + wn * 32 + (lane & 3) * 8) * 2u;
#pragma unroll
      for (int mp = 0; mp < 2; ++mp) {
#pragma unroll
        for (int mj = 0; mj < 2; ++mj) {
          const int mi = mp * 2 + mj;
#pragma unroll
          for (int ni = 0; ni < 2; ++ni) {
            f32x4 g = (acc[ni][mi] - mb[mi] * csv[ni]) * rinv[mi];
            f32x4 u = (acc[ni + 2][mi] - mb[mi] * csv[ni + 2]) * rinv[mi];
            float hv[4];
#pragma unroll
            for (int e = 0; e < 4; ++e) hv[e] = gelu_tanh(g[e]) * u[e];
            u32x2 o = {pack2(hv[0], hv[1]) & vmask, pack2(hv[2], hv[3]) & vmask};
            *(u32x2*)(scr + mj * 1024 + fr * 64 + (((ni * 2 + (fq >> 1)) ^ ((fr >> 1) & 3)) * 16) + (fq & 1) * 8) = o;
          }
        }
        const int r2 = lane >> 2;
#pragma unroll
        for (int mj = 0; mj < 2; ++mj) {
          u32x4 v = *(const u32x4*)(scr + mj * 1024 + r2 * 64 + (((lane & 3) ^ ((r2 >> 1) & 3)) * 16));
          __builtin_amdgcn_raw_buffer_store_b128(v, rsw, gbase + (unsigned)((mp * 2 + mj) * 16 * FP_ * 2), 0, 16);
        }
      }
    }
  }
  WAIT_V(0);
  if (wid != 4) nonce = uniform64(*snonce);
  if (has_a && lane == 0) {
    const unsigned old = __hip_atomic_fetch_add(scnt, 1u, __ATOMIC_RELAXED, __HIP_MEMORY_SCOPE_WORKGROUP);
    if (old == 7u) __hip_atomic_store(flag_ptr(ws, FL_READY1 + nb), flag_tag(nonce, FL_READY1 + nb), RLX_AGENT);
  }
  if (wid > 4) return;

  if (wid == 4) {
    int last = dep_flag(K2_NST - 2);
#pragma unroll 1
    for (int j = 0; j < nt; ++j) {
      if (j + K2_NST - 1 < nt) {
        const int fi = dep_flag(j + K2_NST - 1);
        if (fi != last) wait_flag(nonce, fi);
        last = fi;
      }
      BARRIER();
    }
    return;
  }

  {
    f32x4 acc[4][4];
#pragma unroll
    for (int i = 0; i < 4; ++i)
#pragma unroll
      for (int j = 0; j < 4; ++j) acc[i][j] = f32x4{0.f, 0.f, 0.f, 0.f};
    const unsigned a_base0 = sbase + (wm * 64 + fr) * 128 + ch0;
    const unsigned a_base1 = sbase + (wm * 64 + fr) * 128 + ch1;
    const unsigned b_base = sbase + 16384 + wn * 8192 + tr_row;
    unsigned b_off[4];
#pragma unroll
    for (int ni = 0; ni < 4; ++ni) b_off[ni] = b_base + 32 * (ni ^ tswz);
    bf16x8 af0[4], bf0[4], af1[4], bf1[4];
    auto rdg = [&](auto KSC, bf16x8(&af)[4], bf16x8(&bfr)[4], unsigned so, int g) {
      constexpr int KS = decltype(KSC)::value;
      const unsigned ab_ = (KS ? a_base1 : a_base0) + so;
      switch (g) {
        case 0: af[0] = lds_rd128<0>(ab_); break;
        case 2: af[1] = lds_rd128<2048>(ab_); break;
        case 4: af[2] = lds_rd128<4096>(ab_); break;
        case 6: af[3] = lds_rd128<6144>(ab_); break;
        case 1: bfr[0] = tr_frag<KS * 4096>(b_off[0] + so); break;
        case 3: bfr[1] = tr_frag<KS * 4096>(b_off[1] + so); break;
        case 5: bfr[2] = tr_frag<KS * 4096>(b_off[2] + so); break;
        default: bfr[3] = tr_frag<KS * 4096>(b_off[3] + so); break;
      }
    };
    BARRIER();
#pragma unroll
    for (int g = 0; g < 8; ++g) rdg(C0{}, af0, bf0, 0u, g);
    WAIT_L0();
    SCHED0();
    unsigned so = 0;
#pragma unroll 1
    for (int j = 0; j < nt - 1; ++j) {
      mm16(acc, bf0, af0, [&](int g) { rdg(C1{}, af1, bf1, so, g); });
      WAIT_L0();
      SCHED0();
      so = (so == (K2_NST - 1) * K2_STAGE) ? 0u : so + K2_STAGE;
      BARRIER();
      mm16(acc, bf1, af1, [&](int g) { rdg(C0{}, af0, bf0, so, g); });
      WAIT_L0();
      SCHED0();
    }
    mm16(acc, bf0, af0, [&](int g) { rdg(C1{}, af1, bf1, so, g); });
    WAIT_L0();
    SCHED0();
    mm16(acc, bf1, af1, [&](int) {});
    const __amdgpu_buffer_rsrc_t rs = __builtin_amdgcn_make_buffer_rsrc(ws + WS_SLAB, 0, 8u << 20, 0x00020000);
    f32x4 own[4];
#pragma unroll
    for (int mi = 0; mi < 4; ++mi) {
      if (mi == q) {
#pragma unroll
        for (int ni = 0; ni < 4; ++ni) own[ni] = acc[ni][mi];
      } else {
        const unsigned base = (unsigned)((((tile * 4 + mi) * 4 + q) * 4 + wid) * 2) * 1024u + lane * 16u;
#pragma unroll
        for (int np = 0; np < 2; ++np) {
          const f32x4 a0 = acc[2 * np][mi], a1 = acc[2 * np + 1][mi];
          u32x4 v = {pack2(a0[0], a0[1]), pack2(a0[2], a0[3]), pack2(a1[0], a1[1]), pack2(a1[2], a1[3])};
          __builtin_amdgcn_raw_buffer_store_b128(v, rs, base + np * 1024u, 0, 16);
        }
      }
    }
    const size_t orow = (size_t)(rowB + wm * 64 + q * 16 + fr) * D_ + n0 + wn * 64 + fq * 4;
    u32x2 xv[4];
#pragma unroll
    for (int ni = 0; ni < 4; ++ni) xv[ni] = *(const u32x2*)(x + orow + ni * 16);
    WAIT_V(0);
    {
      const int fbase = FL_READY2 + (tile * 4 + wid) * 4;
      if (lane == 0) __hip_atomic_store(flag_ptr(ws, fbase + q), flag_tag(nonce, fbase + q), RLX_AGENT);
      const int fi = fbase + (lane & 3);
      const u64 want = flag_tag(nonce, fi);
      u64* p = flag_ptr(ws, fi);
#pragma unroll 1
      for (unsigned spins = 0; spins < SPIN_CAP; ++spins) {
        const u64 v = __hip_atomic_load(p, RLX_AGENT);
        if (__all(v == want || (lane & 3) == q)) break;
        __builtin_amdgcn_s_sleep(1);
      }
      __builtin_amdgcn_fence(__ATOMIC_ACQUIRE, "wavefront");
    }
    u32x4 pv[3][2];
#pragma unroll
    for (int sidx = 0; sidx < 3; ++sidx) {
      const int src = sidx + (sidx >= q ? 1 : 0);
      const unsigned base = (unsigned)((((tile * 4 + q) * 4 + src) * 4 + wid) * 2) * 1024u + lane * 16u;
#pragma unroll
      for (int np = 0; np < 2; ++np) pv[sidx][np] = __builtin_amdgcn_raw_buffer_load_b128(rs, base + np * 1024u, 0, 16);
    }
#pragma unroll
    for (int ni = 0; ni < 4; ++ni) {
      f32x4 v = own[ni];
#pragma unroll
      for (int sidx = 0; sidx < 3; ++sidx) {
        const unsigned w0 = pv[sidx][ni >> 1][(ni & 1) * 2], w1 = pv[sidx][ni >> 1][(ni & 1) * 2 + 1];
        v[0] += bflo(w0); v[1] += bfhi(w0); v[2] += bflo(w1); v[3] += bfhi(w1);
      }
      u32x2 o = {pack2(v[0] + bflo(xv[ni][0]), v[1] + bfhi(xv[ni][0])), pack2(v[2] + bflo(xv[ni][1]), v[3] + bfhi(xv[ni][1]))};
      *(u32x2*)(out + orow + ni * 16) = o;
    }
  }
  if (bid == 0 && wid == 0) {
#pragma unroll 1
    for (unsigned spins = 0; spins < SPIN_CAP; ++spins) {
      bool ok = true;
#pragma unroll
      for (int i = 0; i < 4; ++i) {
        const int fi = FL_ARRIVE + i * 64 + lane;
        ok &= __hip_atomic_load(flag_ptr(ws, fi), RLX_AGENT) == flag_tag(nonce, fi);
      }
      if (__all(ok)) break;
      __builtin_amdgcn_s_sleep(1);
    }
    if (lane == 0) __hip_atomic_store((u64*)(ws + WS_SYNC), nonce + 1, RLX_AGENT);
  }
}

extern "C" void kernel_launch(void* const* d_in, const int* in_sizes, int n_in, void* d_out, int out_size,
                              void* d_ws, size_t ws_size, hipStream_t stream) {
  fused_kernel<<<256, 768, 0, stream>>>((const u16*)d_in[0], (const u16*)d_in[1], (const u16*)d_in[2],
                                        (const u16*)d_in[3], (const u16*)d_in[4], (const u16*)d_in[6],
                                        (const u16*)d_in[7], (const u16*)d_in[8], (u16*)d_out, (char*)d_ws);
}
```

```cpp
#include <hip/hip_runtime.h>
#include <hip/hip_bf16.h>
#include <stdint.h>
#include <type_traits>

typedef unsigned short u16;
typedef unsigned long long u64;
typedef short bf16x8 __attribute__((ext_vector_type(8)));
typedef short bf16x4 __attribute__((ext_vector_type(4)));
typedef float f32x4 __attribute__((ext_vector_type(4)));
typedef float f32x2 __attribute__((ext_vector_type(2)));
typedef unsigned u32x4 __attribute__((ext_vector_type(4)));
typedef unsigned u32x2 __attribute__((ext_vector_type(2)));

constexpr int S_ = 2048, D_ = 512, F_ = 1376, FP_ = 1408;

#define LDS_AS __attribute__((address_space(3)))
#define GLDS16(gptr, ldsptr, aux) \
  __builtin_amdgcn_global_load_lds((const unsigned*)(gptr), (unsigned*)(ldsptr), 16, 0, aux)
#define WAIT_V(n) asm volatile("s_waitcnt vmcnt(" #n ")" ::: "memory")
#define BARRIER() asm volatile("s_barrier" ::: "memory")
#define WAIT_L0() asm volatile("s_waitcnt lgkmcnt(0)" ::: "memory")
#define SCHED0() __builtin_amdgcn_sched_barrier(0)
#define RLX_AGENT __ATOMIC_RELAXED, __HIP_MEMORY_SCOPE_AGENT

__device__ __forceinline__ float bf16r(float x) { return __bfloat162float(__float2bfloat16(x)); }
typedef __bf16 bf2_t __attribute__((ext_vector_type(2)));
__device__ __forceinline__ unsigned pack2(float lo, float hi) {
  f32x2 v = {lo, hi};
  return __builtin_bit_cast(unsigned, __builtin_convertvector(v, bf2_t));
}
__device__ __forceinline__ float bflo(unsigned v) { return __builtin_bit_cast(float, v << 16); }
__device__ __forceinline__ float bfhi(unsigned v) { return __builtin_bit_cast(float, v & 0xffff0000u); }

template <int OFF>
__device__ __forceinline__ bf16x8 tr_frag(unsigned addr) {
  bf16x4 a, b;
  asm volatile("ds_read_b64_tr_b16 %0, %2 offset:%3\n\tds_read_b64_tr_b16 %1, %2 offset:%4"
               : "=&v"(a), "=&v"(b)
               : "v"(addr), "i"(OFF), "i"(OFF + 512));
  return __builtin_shufflevector(a, b, 0, 1, 2, 3, 4, 5, 6, 7);
}
template <int OFF>
__device__ __forceinline__ bf16x8 lds_rd128(unsigned addr) {
  bf16x8 a;
  asm volatile("ds_read_b128 %0, %1 offset:%2" : "=&v"(a) : "v"(addr), "i"(OFF));
  return a;
}
__device__ __forceinline__ unsigned lds_addr(const void* p) { return (unsigned)(uintptr_t)(const LDS_AS char*)p; }
__device__ __forceinline__ float gelu_tanh(float g) {
  const float c1 = -2.0f * 0.7978845608028654f * 1.4426950408889634f;
  const float c2 = c1 * 0.044715f;
  float w = g * __builtin_fmaf(g * g, c2, c1);
  float e = __builtin_amdgcn_exp2f(w);
  return g * __builtin_amdgcn_rcpf(1.0f + e);
}
#define TOUCH(gptr, ldsptr) __builtin_amdgcn_global_load_lds((const unsigned*)(gptr), (unsigned*)(ldsptr), 4, 0, 0)
template <class RD>
__device__ __forceinline__ void mm16(f32x4 (&acc)[4][4], const bf16x8 (&wf)[4], const bf16x8 (&xf)[4], RD rd) {
#pragma unroll
  for (int i = 0; i < 16; ++i) {
    const int ni = i >> 2, mi = i & 3;
    acc[ni][mi] = __builtin_amdgcn_mfma_f32_16x16x32_bf16(wf[ni], xf[mi], acc[ni][mi], 0, 0, 0);
    if (i < 8) {
      SCHED0();
      rd(i);
      SCHED0();
    }
  }
  SCHED0();
}

constexpr size_t WS_AB = 0;
constexpr size_t WS_H = 4u << 20;
constexpr size_t WS_SLAB = 16u << 20;
constexpr size_t WS_SYNC = 32u << 20;
constexpr int FL_ARRIVE = 0, FL_READY1 = 256, FL_READY2 = 512;
__device__ __forceinline__ u64* flag_ptr(char* ws, int idx) { return (u64*)(ws + WS_SYNC + 4096 + (size_t)idx * 64); }
__device__ __forceinline__ u64 flag_tag(u64 nonce, int idx) { return nonce + (u64)(idx + 1) * 0x9E3779B97F4A7C15ull; }
constexpr unsigned SPIN_CAP = 1u << 17;

constexpr int K1_A = 32768, K1_B = 16384, K1_STAGE = K1_A + K1_B;
constexpr int K2_STAGE = 32768, K2_NST = 5;
constexpr int L_SCR = 131072;
constexpr int L_SST = 147456, L_SCS = L_SST + 2048, L_NONCE = L_SCS + 512, L_CNT = L_NONCE + 64, L_DUMMY = L_NONCE + 256;

__global__ __launch_bounds__(768, 3) void fused_kernel(const u16* __restrict__ x, const u16* __restrict__ ws_re,
                                                       const u16* __restrict__ ws_im, const u16* __restrict__ wd_re,
                                                       const u16* __restrict__ wd_im, const u16* __restrict__ w_gate,
                                                       const u16* __restrict__ w_up, const u16* __restrict__ w_down,
                                                       u16* __restrict__ out, char* ws) {
  __shared__ __attribute__((aligned(1024))) char smem[K2_NST * K2_STAGE];
  const int tid = threadIdx.x, wid = __builtin_amdgcn_readfirstlane(tid >> 6), lane = tid & 63;
  const int fr = lane & 15, fq = lane >> 4;
  const int bid = blockIdx.x;
  u16* ab = (u16*)(ws + WS_AB);
  u16* h = (u16*)(ws + WS_H);

  const bool has_a = bid < 240;
  const int nb = (bid & 7) * 30 + (bid >> 3);
  const int cb = nb >> 3, rbA = nb & 7;
  const bool nk = cb < 8;
  const int rowA = rbA * 256;
  const int f0 = (cb - 8) * 64;
  const int idx = bid >> 3;
  const int q = (bid & 7) >> 1;
  const int rbB = (bid & 1) * 8 + (idx >> 2), cbk = idx & 3;
  const int rowB = rbB * 128, n0 = cbk * 128, tile = rbB * 4 + cbk;
  const int hq0 = q < 2 ? 6 * q : 12 + 5 * (q - 2);
  const int nt = q < 2 ? 22 : 21;
  auto t_of = [&](int j) { return j < 16 ? 16 * q + j : 64 + hq0 + (j - 16); };

  const int sw = (fr >> 1) & 7;
  const int ch0 = ((fq) ^ sw) * 16, ch1 = ((4 + fq) ^ sw) * 16;
  const int tq = (lane >> 2) & 3, tp = lane & 3;
  const int tswz = ((tq >> 1) & 1) | ((fq & 1) << 1);
  const int tr_row = (8 * fq + tq) * 128 + 8 * tp;
  float* sst = (float*)(smem + L_SST);
  float* scs = (float*)(smem + L_SCS);
  u64* snonce = (u64*)(smem + L_NONCE);
  const unsigned sbase = lds_addr(smem);
  unsigned* scnt = (unsigned*)(smem + L_CNT);
  auto dep_flag = [&](int j) {
    const int t = t_of(j);
    return FL_READY1 + (t < 64 ? (((t >> 5) * 4 + cbk) * 8 + ((t & 31) >> 2)) : ((8 + t - 64) * 8 + (rbB >> 1)));
  };
  auto wait_flag = [&](u64 nonce_, int fi) {
    const u64 want = flag_tag(nonce_, fi);
    u64* p = flag_ptr(ws, fi);
#pragma unroll 1
    for (unsigned spins = 0; spins < SPIN_CAP; ++spins) {
      const u64 v = __hip_atomic_load(p, RLX_AGENT);
      const bool ok = __builtin_amdgcn_readfirstlane((unsigned)(v == want)) != 0;
      if (ok) break;
      __builtin_amdgcn_s_sleep(1);
    }
  };
  auto uniform64 = [&](u64 v) {
    const unsigned hi = (unsigned)__builtin_amdgcn_readfirstlane((int)(unsigned)(v >> 32));
    const unsigned lo = (unsigned)__builtin_amdgcn_readfirstlane((int)(unsigned)v);
    return ((u64)hi << 32) | (u64)lo;
  };

  if (wid >= 8) {
    const int lw = wid - 8;
    const int l3 = lane >> 3, l7 = lane & 7;
    const int rt_c0 = (l7 ^ (fq & 7)) * 8, rt_c1 = (l7 ^ ((fq + 4) & 7)) * 8;
    const int kt_c0 = (l7 ^ ((fq & 1) << 1)) * 8, kt_c1 = (l7 ^ (((fq & 1) | 2) << 1)) * 8;
    if (has_a) {
      __builtin_amdgcn_s_setprio(2);
      const u16* xa0 = x + (size_t)(rowA + lw * 64 + l3) * D_ + rt_c0;
      const u16* xa1 = x + (size_t)(rowA + lw * 64 + 8 + l3) * D_ + rt_c1;
      const u16 *wb0, *wb1;
      int wstep_j, wstep_t;
      if (nk) {
        const u16* wnk = (cb < 4 ? wd_re : wd_im) + (size_t)((cb & 3) * 128 + lw * 32 + l3) * D_;
        wb0 = wnk + rt_c0;
        wb1 = wnk + 8 * D_ + rt_c1;
        wstep_j = 16 * D_;
        wstep_t = 64;
      } else {
        const u16* w = (lw < 2) ? w_gate : w_up;
        int c0 = f0 + kt_c0, c1 = f0 + kt_c1;
        if (c0 >= F_) c0 -= 32;
        if (c1 >= F_) c1 -= 32;
        const u16* wk = w + (size_t)((lw & 1) * 32 + l3) * F_;
        wb0 = wk + c0;
        wb1 = wk + 8 * F_ + c1;
        wstep_j = 16 * F_;
        wstep_t = 64 * F_;
      }
      auto stage = [&](int tj, int buf) {
        const int t = (tj + cb) & 7;
        char* sa = smem + buf * K1_STAGE + lw * 8192;
        char* sb = smem + buf * K1_STAGE + K1_A + lw * 4096;
#pragma unroll
        for (int i = 0; i < 4; ++i) {
          GLDS16(xa0 + i * 16 * D_ + t * 64, sa + i * 2048, 0);
          GLDS16(xa1 + i * 16 * D_ + t * 64, sa + i * 2048 + 1024, 0);
        }
#pragma unroll
        for (int i = 0; i < 2; ++i) {
          GLDS16(wb0 + i * wstep_j + t * wstep_t, sb + i * 2048, 0);
          GLDS16(wb1 + i * wstep_j + t * wstep_t, sb + i * 2048 + 1024, 0);
        }
      };
#pragma unroll 1
      for (int t = 0; t < 2; ++t) stage(t, t);
      f32x4 accS[4], accG[4], accC[2];
#pragma unroll
      for (int i = 0; i < 4; ++i) {
        accS[i] = f32x4{0.f, 0.f, 0.f, 0.f};
        accG[i] = f32x4{0.f, 0.f, 0.f, 0.f};
      }
      accC[0] = f32x4{0.f, 0.f, 0.f, 0.f};
      accC[1] = f32x4{0.f, 0.f, 0.f, 0.f};
      const short ONE = 0x3f80;
      const bf16x8 ones = {ONE, ONE, ONE, ONE, ONE, ONE, ONE, ONE};
      const unsigned la0 = sbase + (lw * 64 + fr) * 128 + ch0;
      const unsigned la1 = sbase + (lw * 64 + fr) * 128 + ch1;
      const unsigned lb0 = sbase + K1_A + (lw * 32 + fr) * 128 + ch0;
      const unsigned lb1 = sbase + K1_A + (lw * 32 + fr) * 128 + ch1;
      const unsigned lt0 = sbase + K1_A + (lw >> 1) * 8192 + tr_row + 32 * (((lw & 1) * 2) ^ tswz);
      const unsigned lt1 = sbase + K1_A + (lw >> 1) * 8192 + tr_row + 32 * (((lw & 1) * 2 + 1) ^ tswz);
      int ib = 2, buf = 0;
#pragma unroll 1
      for (int t = 0; t < 8; ++t) {
        if (t < 7) WAIT_V(12); else WAIT_V(0);
        BARRIER();
        const unsigned so = buf * K1_STAGE;
        bf16x8 xf[8], wf[4];
        xf[0] = lds_rd128<0>(la0 + so); xf[1] = lds_rd128<2048>(la0 + so);
        xf[2] = lds_rd128<4096>(la0 + so); xf[3] = lds_rd128<6144>(la0 + so);
        xf[4] = lds_rd128<0>(la1 + so); xf[5] = lds_rd128<2048>(la1 + so);
        xf[6] = lds_rd128<4096>(la1 + so); xf[7] = lds_rd128<6144>(la1 + so);
        if (nk) {
          wf[0] = lds_rd128<0>(lb0 + so); wf[1] = lds_rd128<2048>(lb0 + so);
          wf[2] = lds_rd128<0>(lb1 + so); wf[3] = lds_rd128<2048>(lb1 + so);
        } else {
          wf[0] = tr_frag<0>(lt0 + so); wf[1] = tr_frag<0>(lt1 + so);
          wf[2] = tr_frag<4096>(lt0 + so); wf[3] = tr_frag<4096>(lt1 + so);
        }
        if (t + 2 < 8) stage(t + 2, ib);
        ib = (ib == 2) ? 0 : ib + 1;
        WAIT_L0();
        SCHED0();
#pragma unroll
        for (int i = 0; i < 8; ++i) {
          accS[i & 3] = __builtin_amdgcn_mfma_f32_16x16x32_bf16(ones, xf[i], accS[i & 3], 0, 0, 0);
          accG[i & 3] = __builtin_amdgcn_mfma_f32_16x16x32_bf16(xf[i], xf[i], accG[i & 3], 0, 0, 0);
        }
#pragma unroll
        for (int i = 0; i < 4; ++i)
          accC[i & 1] = __builtin_amdgcn_mfma_f32_16x16x32_bf16(wf[i], ones, accC[i & 1], 0, 0, 0);
        buf = (buf == 2) ? 0 : buf + 1;
      }
      if (fq == (fr >> 2)) {
#pragma unroll
        for (int i = 0; i < 4; ++i) {
          const int r = fr & 3;
          const float qq = r == 0 ? accG[i][0] : r == 1 ? accG[i][1] : r == 2 ? accG[i][2] : accG[i][3];
          const float mean = accS[i][0] * (1.0f / D_);
          const float var = fmaxf(qq * (1.0f / D_) - mean * mean, 0.f);
          const float sd = bf16r(sqrtf(bf16r(bf16r(var) + 1e-5f)));
          f32x2 o = {bf16r(mean), __builtin_amdgcn_rcpf(sd)};
          *(f32x2*)(sst + (lw * 64 + i * 16 + fr) * 2) = o;
        }
      }
      if (fr == 0) {
        *(f32x4*)(scs + lw * 32 + fq * 4) = accC[0];
        *(f32x4*)(scs + lw * 32 + 16 + fq * 4) = accC[1];
      }
      WAIT_L0();
      BARRIER();
      __builtin_amdgcn_s_setprio(0);
    }
    const int sub = lw & 1;
    auto stageB = [&](int j, int slot) {
      const int t = t_of(j);
      char* dst = smem + slot * K2_STAGE + lw * 8192;
      if (lw < 2) {
        if (t < 64) {
          const u16* A = (t < 32 ? ws_re : ws_im) + (size_t)(rowB + sub * 64 + l3) * S_ + (t & 31) * 64;
#pragma unroll
          for (int i = 0; i < 8; ++i) GLDS16(A + i * 8 * S_ + ((i & 1) ? rt_c1 : rt_c0), dst + i * 1024, 0);
        } else {
          const u16* A = h + (size_t)(rowB + sub * 64 + l3) * FP_ + (t - 64) * 64;
#pragma unroll
          for (int i = 0; i < 8; ++i) GLDS16(A + i * 8 * FP_ + ((i & 1) ? rt_c1 : rt_c0), dst + i * 1024, 16);
        }
      } else {
        if (t < 64) {
          const u16* B = ab + (size_t)(t * 64 + l3) * D_ + n0 + sub * 64;
#pragma unroll
          for (int i = 0; i < 8; ++i) GLDS16(B + i * 8 * D_ + ((i & 1) ? kt_c1 : kt_c0), dst + i * 1024, 16);
        } else {
          const int k0 = (t - 64) * 64;
#pragma unroll
          for (int i = 0; i < 8; ++i) {
            const int krow = min(k0 + i * 8 + l3, F_ - 1);
            GLDS16(w_down + krow * D_ + n0 + sub * 64 + ((i & 1) ? kt_c1 : kt_c0), dst + i * 1024, 0);
          }
        }
      }
    };
    const bool early = lw < 2;
    if (!has_a) BARRIER();
    if (!early) {
      const u64 nonce_ = uniform64(*snonce);
      int last = -1;
#pragma unroll 1
      for (int j = 0; j < K2_NST - 1; ++j) {
        const int fi = dep_flag(j);
        if (fi != last) wait_flag(nonce_, fi);
        last = fi;
      }
    }
#pragma unroll 1
    for (int s = 0; s < K2_NST - 1; ++s) stageB(s, s);
    int islot = K2_NST - 1;
#pragma unroll 1
    for (int j = 0; j < nt; ++j) {
      const int rem = nt - 1 - j;
      if (rem >= 3) WAIT_V(24);
      else if (rem == 2) WAIT_V(16);
      else if (rem == 1) WAIT_V(8);
      else WAIT_V(0);
      BARRIER();
      if (j + K2_NST - 1 < nt) stageB(j + K2_NST - 1, islot);
      islot = (islot == K2_NST - 1) ? 0 : islot + 1;
    }
    return;
  }

  u64 nonce = 0;
  if (wid == 4) nonce = __hip_atomic_load((u64*)(ws + WS_SYNC), RLX_AGENT);
  auto share_nonce = [&]() {
    if (wid == 4) {
      nonce = uniform64(nonce);
      if (lane == 0) {
        *snonce = nonce;
        *scnt = 0u;
        __hip_atomic_store(flag_ptr(ws, FL_ARRIVE + bid), flag_tag(nonce, FL_ARRIVE + bid), RLX_AGENT);
      }
      WAIT_L0();
    }
  };
  const int wm = wid >> 1, wn = wid & 1;
  using C0 = std::integral_constant<int, 0>;
  using C1 = std::integral_constant<int, 1>;
  char* dummy = smem + L_DUMMY;
  auto warm = [&](int slot) {
    if (lane >= 16) return;
    if (slot < 32) {
      const int t = 16 * q + lane;
      TOUCH((t < 32 ? ws_re : ws_im) + (size_t)(rowB + cbk * 32 + slot) * S_ + (t & 31) * 64, dummy);
    } else {
      const int L = (slot - 32) * 16 + lane, per = (nt - 16) * 8;
      const int krow = hq0 * 64 + (idx >> 2) * per + (L >> 1);
      if (L < 2 * per && krow < F_) TOUCH(w_down + (size_t)krow * D_ + n0 + (L & 1) * 64, dummy);
    }
  };
  if (!has_a) {
    share_nonce();
    BARRIER();
#pragma unroll 1
    for (int i = 0; i < 6; ++i) warm(i * 8 + wid);
  }

  if (has_a) {
    f32x4 acc[4][4];
#pragma unroll
    for (int i = 0; i < 4; ++i)
#pragma unroll
      for (int j = 0; j < 4; ++j) acc[i][j] = f32x4{0.f, 0.f, 0.f, 0.f};
    const unsigned a_b0 = sbase + (wm * 64 + fr) * 128 + ch0, a_b1 = sbase + (wm * 64 + fr) * 128 + ch1;
    const unsigned n_b0 = sbase + K1_A + (wn * 64 + fr) * 128 + ch0;
    const unsigned n_b1 = sbase + K1_A + (wn * 64 + fr) * 128 + ch1;
    const unsigned t_p0 = sbase + K1_A + tr_row + 32 * ((wn * 2) ^ tswz);
    const unsigned t_p1 = sbase + K1_A + tr_row + 32 * ((wn * 2 + 1) ^ tswz);

    auto mainloop = [&](auto NKC) {
      constexpr bool NK = decltype(NKC)::value;
      bf16x8 xf0[4], wf0[4], xf1[4], wf1[4];
      auto rdg = [&](auto KSC, bf16x8(&xf)[4], bf16x8(&wf)[4], unsigned so, int g) {
        constexpr int KS = decltype(KSC)::value;
        const unsigned ab_ = (KS ? a_b1 : a_b0) + so;
        switch (g) {
          case 0: xf[0] = lds_rd128<0>(ab_); break;
          case 2: xf[1] = lds_rd128<2048>(ab_); break;
          case 4: xf[2] = lds_rd128<4096>(ab_); break;
          case 6: xf[3] = lds_rd128<6144>(ab_); break;
          default:
            if constexpr (NK) {
              const unsigned nb_ = (KS ? n_b1 : n_b0) + so;
              if (g == 1) wf[0] = lds_rd128<0>(nb_);
              if (g == 3) wf[1] = lds_rd128<2048>(nb_);
              if (g == 5) wf[2] = lds_rd128<4096>(nb_);
              if (g == 7) wf[3] = lds_rd128<6144>(nb_);
            } else {
              if (g == 1) wf[0] = tr_frag<KS * 4096>(t_p0 + so);
              if (g == 3) wf[1] = tr_frag<KS * 4096>(t_p1 + so);
              if (g == 5) wf[2] = tr_frag<KS * 4096 + 8192>(t_p0 + so);
              if (g == 7) wf[3] = tr_frag<KS * 4096 + 8192>(t_p1 + so);
            }
        }
      };
      BARRIER();
#pragma unroll
      for (int g = 0; g < 8; ++g) rdg(C0{}, xf0, wf0, 0u, g);
      WAIT_L0();
      SCHED0();
      unsigned so = 0;
#pragma unroll 1
      for (int t = 0; t < 7; ++t) {
        if (t > 0) warm((t - 1) * 8 + wid);
        mm16(acc, wf0, xf0, [&](int g) { rdg(C1{}, xf1, wf1, so, g); });
        WAIT_L0();
        SCHED0();
        so = (so == 2 * K1_STAGE) ? 0u : so + K1_STAGE;
        BARRIER();
        mm16(acc, wf1, xf1, [&](int g) { rdg(C0{}, xf0, wf0, so, g); });
        WAIT_L0();
        SCHED0();
      }
      mm16(acc, wf0, xf0, [&](int g) { rdg(C1{}, xf1, wf1, so, g); });
      WAIT_L0();
      SCHED0();
      mm16(acc, wf1, xf1, [&](int) {});
    };
    if (nk) mainloop(std::true_type{});
    else mainloop(std::false_type{});
    share_nonce();
    BARRIER();
    float mb[4], rinv[4];
#pragma unroll
    for (int mi = 0; mi < 4; ++mi) {
      f32x2 v = *(const f32x2*)(sst + (wm * 64 + mi * 16 + fr) * 2);
      mb[mi] = v[0];
      rinv[mi] = v[1];
    }
    f32x4 csv[4];
#pragma unroll
    for (int ni = 0; ni < 4; ++ni) {
      const int ci = nk ? (wn * 64 + ni * 16) : ((ni >> 1) * 64 + wn * 32 + (ni & 1) * 16);
      csv[ni] = *(const f32x4*)(scs + ci + fq * 4);
    }
    char* scr = smem + L_SCR + wid * 2048;
    const __amdgpu_buffer_rsrc_t rsw = __builtin_amdgcn_make_buffer_rsrc(ws, 0, 16u << 20, 0x00020000);
    if (nk) {
      const float sgn = cb < 4 ? 1.0f : -1.0f;
      const unsigned gbase = (unsigned)WS_AB +
          (unsigned)(((cb >> 2) * S_ + rowA + wm * 64 + (lane >> 3)) * D_ + (cb & 3) * 128 + wn * 64 + (lane & 7) * 8) * 2u;
      u32x2 o[4][4];
#pragma unroll
      for (int mi = 0; mi < 4; ++mi) {
        const float r = rinv[mi] * sgn;
#pragma unroll
        for (int ni = 0; ni < 4; ++ni) {
          f32x4 v = (acc[ni][mi] - mb[mi] * csv[ni]) * r;
          o[mi][ni] = u32x2{pack2(v[0], v[1]), pack2(v[2], v[3])};
        }
      }
#pragma unroll
      for (int mi = 0; mi < 4; ++mi) {
#pragma unroll
        for (int ni = 0; ni < 4; ++ni)
          *(u32x2*)(scr + fr * 128 + (((ni * 2 + (fq >> 1)) ^ (fr & 7)) * 16) + (fq & 1) * 8) = o[mi][ni];
#pragma unroll
        for (int hf = 0; hf < 2; ++hf) {
          const int r2 = hf * 8 + (lane >> 3);
          u32x4 v = *(const u32x4*)(scr + r2 * 128 + (((lane & 7) ^ (r2 & 7)) * 16));
          __builtin_amdgcn_raw_buffer_store_b128(v, rsw, gbase + (unsigned)((mi * 16 + hf * 8) * D_ * 2), 0, 16);
        }
      }
    } else {
      const unsigned vmask = (f0 + wn * 32) < F_ ? 0xffffffffu : 0u;
      const unsigned gbase = (unsigned)WS_H +
          (unsigned)((rowA + wm * 64 + (lane >> 2)) * FP_ + f0 + wn * 32 + (lane & 3) * 8) * 2u;
#pragma unroll
      for (int mp = 0; mp < 2; ++mp) {
#pragma unroll
        for (int mj = 0; mj < 2; ++mj) {
          const int mi = mp * 2 + mj;
#pragma unroll
          for (int ni = 0; ni < 2; ++ni) {
            f32x4 g = (acc[ni][mi] - mb[mi] * csv[ni]) * rinv[mi];
            f32x4 u = (acc[ni + 2][mi] - mb[mi] * csv[ni + 2]) * rinv[mi];
            float hv[4];
#pragma unroll
            for (int e = 0; e < 4; ++e) hv[e] = gelu_tanh(g[e]) * u[e];
            u32x2 o = {pack2(hv[0], hv[1]) & vmask, pack2(hv[2], hv[3]) & vmask};
            *(u32x2*)(scr + mj * 1024 + fr * 64 + (((ni * 2 + (fq >> 1)) ^ ((fr >> 1) & 3)) * 16) + (fq & 1) * 8) = o;
          }
        }
        const int r2 = lane >> 2;
#pragma unroll
        for (int mj = 0; mj < 2; ++mj) {
          u32x4 v = *(const u32x4*)(scr + mj * 1024 + r2 * 64 + (((lane & 3) ^ ((r2 >> 1) & 3)) * 16));
          __builtin_amdgcn_raw_buffer_store_b128(v, rsw, gbase + (unsigned)((mp * 2 + mj) * 16 * FP_ * 2), 0, 16);
        }
      }
    }
  }
  WAIT_V(0);
  if (wid != 4) nonce = uniform64(*snonce);
  if (has_a && lane == 0) {
    const unsigned old = __hip_atomic_fetch_add(scnt, 1u, __ATOMIC_RELAXED, __HIP_MEMORY_SCOPE_WORKGROUP);
    if (old == 7u) __hip_atomic_store(flag_ptr(ws, FL_READY1 + nb), flag_tag(nonce, FL_READY1 + nb), RLX_AGENT);
  }
  if (wid > 4) return;

  if (wid == 4) {
    int last = dep_flag(K2_NST - 2);
#pragma unroll 1
    for (int j = 0; j < nt; ++j) {
      if (j + K2_NST - 1 < nt) {
        const int fi = dep_flag(j + K2_NST - 1);
        if (fi != last) wait_flag(nonce, fi);
        last = fi;
      }
      BARRIER();
    }
    return;
  }

  {
    f32x4 acc[4][4];
#pragma unroll
    for (int i = 0; i < 4; ++i)
#pragma unroll
      for (int j = 0; j < 4; ++j) acc[i][j] = f32x4{0.f, 0.f, 0.f, 0.f};
    const unsigned a_base0 = sbase + (wm * 64 + fr) * 128 + ch0;
    const unsigned a_base1 = sbase + (wm * 64 + fr) * 128 + ch1;
    const unsigned b_base = sbase + 16384 + wn * 8192 + tr_row;
    unsigned b_off[4];
#pragma unroll
    for (int ni = 0; ni < 4; ++ni) b_off[ni] = b_base + 32 * (ni ^ tswz);
    bf16x8 af0[4], bf0[4], af1[4], bf1[4];
    auto rdg = [&](auto KSC, bf16x8(&af)[4], bf16x8(&bfr)[4], unsigned so, int g) {
      constexpr int KS = decltype(KSC)::value;
      const unsigned ab_ = (KS ? a_base1 : a_base0) + so;
      switch (g) {
        case 0: af[0] = lds_rd128<0>(ab_); break;
        case 2: af[1] = lds_rd128<2048>(ab_); break;
        case 4: af[2] = lds_rd128<4096>(ab_); break;
        case 6: af[3] = lds_rd128<6144>(ab_); break;
        case 1: bfr[0] = tr_frag<KS * 4096>(b_off[0] + so); break;
        case 3: bfr[1] = tr_frag<KS * 4096>(b_off[1] + so); break;
        case 5: bfr[2] = tr_frag<KS * 4096>(b_off[2] + so); break;
        default: bfr[3] = tr_frag<KS * 4096>(b_off[3] + so); break;
      }
    };
    BARRIER();
#pragma unroll
    for (int g = 0; g < 8; ++g) rdg(C0{}, af0, bf0, 0u, g);
    WAIT_L0();
    SCHED0();
    unsigned so = 0;
#pragma unroll 1
    for (int j = 0; j < nt - 1; ++j) {
      mm16(acc, bf0, af0, [&](int g) { rdg(C1{}, af1, bf1, so, g); });
      WAIT_L0();
      SCHED0();
      so = (so == (K2_NST - 1) * K2_STAGE) ? 0u : so + K2_STAGE;
      BARRIER();
      mm16(acc, bf1, af1, [&](int g) { rdg(C0{}, af0, bf0, so, g); });
      WAIT_L0();
      SCHED0();
    }
    mm16(acc, bf0, af0, [&](int g) { rdg(C1{}, af1, bf1, so, g); });
    WAIT_L0();
    SCHED0();
    mm16(acc, bf1, af1, [&](int) {});
    const __amdgpu_buffer_rsrc_t rs = __builtin_amdgcn_make_buffer_rsrc(ws + WS_SLAB, 0, 8u << 20, 0x00020000);
    f32x4 own[4];
#pragma unroll
    for (int mi = 0; mi < 4; ++mi) {
      if (mi == q) {
#pragma unroll
        for (int ni = 0; ni < 4; ++ni) own[ni] = acc[ni][mi];
      } else {
        const unsigned base = (unsigned)((((tile * 4 + mi) * 4 + q) * 4 + wid) * 2) * 1024u + lane * 16u;
#pragma unroll
        for (int np = 0; np < 2; ++np) {
          const f32x4 a0 = acc[2 * np][mi], a1 = acc[2 * np + 1][mi];
          u32x4 v = {pack2(a0[0], a0[1]), pack2(a0[2], a0[3]), pack2(a1[0], a1[1]), pack2(a1[2], a1[3])};
          __builtin_amdgcn_raw_buffer_store_b128(v, rs, base + np * 1024u, 0, 16);
        }
      }
    }
    const size_t orow = (size_t)(rowB + wm * 64 + q * 16 + fr) * D_ + n0 + wn * 64 + fq * 4;
    u32x2 xv[4];
#pragma unroll
    for (int ni = 0; ni < 4; ++ni) xv[ni] = *(const u32x2*)(x + orow + ni * 16);
    WAIT_V(0);
    BARRIER();
    if (wid == 0) {
      if (lane == 0)
        __hip_atomic_store(flag_ptr(ws, FL_READY2 + tile * 4 + q), flag_tag(nonce, FL_READY2 + tile * 4 + q), RLX_AGENT);
      const int fi = FL_READY2 + tile * 4 + (lane & 3);
      const u64 want = flag_tag(nonce, fi);
      u64* p = flag_ptr(ws, fi);
#pragma unroll 1
      for (unsigned spins = 0; spins < SPIN_CAP; ++spins) {
        const u64 v = __hip_atomic_load(p, RLX_AGENT);
        if (__all(v == want || (lane & 3) == q)) break;
        __builtin_amdgcn_s_sleep(1);
      }
    }
    BARRIER();
    u32x4 pv[3][2];
#pragma unroll
    for (int sidx = 0; sidx < 3; ++sidx) {
      const int src = sidx + (sidx >= q ? 1 : 0);
      const unsigned base = (unsigned)((((tile * 4 + q) * 4 + src) * 4 + wid) * 2) * 1024u + lane * 16u;
#pragma unroll
      for (int np = 0; np < 2; ++np) pv[sidx][np] = __builtin_amdgcn_raw_buffer_load_b128(rs, base + np * 1024u, 0, 16);
    }
#pragma unroll
    for (int ni = 0; ni < 4; ++ni) {
      f32x4 v = own[ni];
#pragma unroll
      for (int sidx = 0; sidx < 3; ++sidx) {
        const unsigned w0 = pv[sidx][ni >> 1][(ni & 1) * 2], w1 = pv[sidx][ni >> 1][(ni & 1) * 2 + 1];
        v[0] += bflo(w0); v[1] += bfhi(w0); v[2] += bflo(w1); v[3] += bfhi(w1);
      }
      u32x2 o = {pack2(v[0] + bflo(xv[ni][0]), v[1] + bfhi(xv[ni][0])), pack2(v[2] + bflo(xv[ni][1]), v[3] + bfhi(xv[ni][1]))};
      *(u32x2*)(out + orow + ni * 16) = o;
    }
  }
  if (bid == 0 && wid == 0) {
#pragma unroll 1
    for (unsigned spins = 0; spins < SPIN_CAP; ++spins) {
      bool ok = true;
#pragma unroll
      for (int i = 0; i < 4; ++i) {
        const int fi = FL_ARRIVE + i * 64 + lane;
        ok &= __hip_atomic_load(flag_ptr(ws, fi), RLX_AGENT) == flag_tag(nonce, fi);
      }
      if (__all(ok)) break;
      __builtin_amdgcn_s_sleep(1);
    }
    if (lane == 0) __hip_atomic_store((u64*)(ws + WS_SYNC), nonce + 1, RLX_AGENT);
  }
}

extern "C" void kernel_launch(void* const* d_in, const int* in_sizes, int n_in, void* d_out, int out_size,
                              void* d_ws, size_t ws_size, hipStream_t stream) {
  fused_kernel<<<256, 768, 0, stream>>>((const u16*)d_in[0], (const u16*)d_in[1], (const u16*)d_in[2],
                                        (const u16*)d_in[3], (const u16*)d_in[4], (const u16*)d_in[6],
                                        (const u16*)d_in[7], (const u16*)d_in[8], (u16*)d_out, (char*)d_ws);
}
```

```cpp
#include <hip/hip_runtime.h>
#include <hip/hip_bf16.h>
#include <stdint.h>
#include <type_traits>

typedef unsigned short u16;
typedef unsigned long long u64;
typedef short bf16x8 __attribute__((ext_vector_type(8)));
typedef short bf16x4 __attribute__((ext_vector_type(4)));
typedef float f32x4 __attribute__((ext_vector_type(4)));
typedef float f32x2 __attribute__((ext_vector_type(2)));
typedef unsigned u32x4 __attribute__((ext_vector_type(4)));
typedef unsigned u32x2 __attribute__((ext_vector_type(2)));

constexpr int S_ = 2048, D_ = 512, F_ = 1376, FP_ = 1408;

#define LDS_AS __attribute__((address_space(3)))
#define GLDS16(gptr, ldsptr, aux) \
  __builtin_amdgcn_global_load_lds((const unsigned*)(gptr), (unsigned*)(ldsptr), 16, 0, aux)
#define WAIT_V(n) asm volatile("s_waitcnt vmcnt(" #n ")" ::: "memory")
#define BARRIER() asm volatile("s_barrier" ::: "memory")
#define WAIT_L0() asm volatile("s_waitcnt lgkmcnt(0)" ::: "memory")
#define SCHED0() __builtin_amdgcn_sched_barrier(0)
#define RLX_AGENT __ATOMIC_RELAXED, __HIP_MEMORY_SCOPE_AGENT

__device__ __forceinline__ float bf16r(float x) { return __bfloat162float(__float2bfloat16(x)); }
typedef __bf16 bf2_t __attribute__((ext_vector_type(2)));
__device__ __forceinline__ unsigned pack2(float lo, float hi) {
  f32x2 v = {lo, hi};
  return __builtin_bit_cast(unsigned, __builtin_convertvector(v, bf2_t));
}
__device__ __forceinline__ float bflo(unsigned v) { return __builtin_bit_cast(float, v << 16); }
__device__ __forceinline__ float bfhi(unsigned v) { return __builtin_bit_cast(float, v & 0xffff0000u); }

template <int OFF>
__device__ __forceinline__ bf16x8 tr_frag(unsigned addr) {
  bf16x4 a, b;
  asm volatile("ds_read_b64_tr_b16 %0, %2 offset:%3\n\tds_read_b64_tr_b16 %1, %2 offset:%4"
               : "=&v"(a), "=&v"(b)
               : "v"(addr), "i"(OFF), "i"(OFF + 512));
  return __builtin_shufflevector(a, b, 0, 1, 2, 3, 4, 5, 6, 7);
}
template <int OFF>
__device__ __forceinline__ bf16x8 lds_rd128(unsigned addr) {
  bf16x8 a;
  asm volatile("ds_read_b128 %0, %1 offset:%2" : "=&v"(a) : "v"(addr), "i"(OFF));
  return a;
}
__device__ __forceinline__ unsigned lds_addr(const void* p) { return (unsigned)(uintptr_t)(const LDS_AS char*)p; }
__device__ __forceinline__ float gelu_tanh(float g) {
  const float c1 = -2.0f * 0.7978845608028654f * 1.4426950408889634f;
  const float c2 = c1 * 0.044715f;
  float w = g * __builtin_fmaf(g * g, c2, c1);
  float e = __builtin_amdgcn_exp2f(w);
  return g * __builtin_amdgcn_rcpf(1.0f + e);
}
#define TOUCH(gptr, ldsptr) __builtin_amdgcn_global_load_lds((const unsigned*)(gptr), (unsigned*)(ldsptr), 4, 0, 0)
template <class RD>
__device__ __forceinline__ void mm16(f32x4 (&acc)[4][4], const bf16x8 (&wf)[4], const bf16x8 (&xf)[4], RD rd) {
#pragma unroll
  for (int i = 0; i < 16; ++i) {
    const int ni = i >> 2, mi = i & 3;
    acc[ni][mi] = __builtin_amdgcn_mfma_f32_16x16x32_bf16(wf[ni], xf[mi], acc[ni][mi], 0, 0, 0);
    if (i < 8) {
      SCHED0();
      rd(i);
      SCHED0();
    }
  }
  SCHED0();
}

constexpr size_t WS_AB = 0;
constexpr size_t WS_H = 4u << 20;
constexpr size_t WS_SLAB = 16u << 20;
constexpr size_t WS_SYNC = 32u << 20;
constexpr int FL_ARRIVE = 0, FL_READY1 = 256, FL_READY2 = 512;
__device__ __forceinline__ u64* flag_ptr(char* ws, int idx) { return (u64*)(ws + WS_SYNC + 4096 + (size_t)idx * 64); }
__device__ __forceinline__ u64 flag_tag(u64 nonce, int idx) { return nonce + (u64)(idx + 1) * 0x9E3779B97F4A7C15ull; }
constexpr unsigned SPIN_CAP = 1u << 17;

constexpr int K1_A = 32768, K1_B = 16384, K1_STAGE = K1_A + K1_B;
constexpr int K2_STAGE = 32768, K2_NST = 5;
constexpr int L_SCR = 131072;
constexpr int L_SST = 147456, L_SCS = L_SST + 2048, L_NONCE = L_SCS + 512, L_CNT = L_NONCE + 64, L_DUMMY = L_NONCE + 256;

__global__ __launch_bounds__(768, 3) void fused_kernel(const u16* __restrict__ x, const u16* __restrict__ ws_re,
                                                       const u16* __restrict__ ws_im, const u16* __restrict__ wd_re,
                                                       const u16* __restrict__ wd_im, const u16* __restrict__ w_gate,
                                                       const u16* __restrict__ w_up, const u16* __restrict__ w_down,
                                                       u16* __restrict__ out, char* ws) {
  __shared__ __attribute__((aligned(1024))) char smem[K2_NST * K2_STAGE];
  const int tid = threadIdx.x, wid = __builtin_amdgcn_readfirstlane(tid >> 6), lane = tid & 63;
  const int fr = lane & 15, fq = lane >> 4;
  const int bid = blockIdx.x;
  u16* ab = (u16*)(ws + WS_AB);
  u16* h = (u16*)(ws + WS_H);

  const bool has_a = bid < 240;
  const int nb = (bid & 7) * 30 + (bid >> 3);
  const int cb = nb >> 3, rbA = nb & 7;
  const bool nk = cb < 8;
  const int rowA = rbA * 256;
  const int f0 = (cb - 8) * 64;
  const int idx = bid >> 3;
  const int q = (bid & 7) >> 1;
  const int rbB = (bid & 1) * 8 + (idx >> 2), cbk = idx & 3;
  const int rowB = rbB * 128, n0 = cbk * 128, tile = rbB * 4 + cbk;
  const int hq0 = q < 2 ? 6 * q : 12 + 5 * (q - 2);
  const int nt = q < 2 ? 22 : 21;
  auto t_of = [&](int j) { return j < 16 ? 16 * q + j : 64 + hq0 + (j - 16); };

  const int sw = (fr >> 1) & 7;
  const int ch0 = ((fq) ^ sw) * 16, ch1 = ((4 + fq) ^ sw) * 16;
  const int tq = (lane >> 2) & 3, tp = lane & 3;
  const int tswz = ((tq >> 1) & 1) | ((fq & 1) << 1);
  const int tr_row = (8 * fq + tq) * 128 + 8 * tp;
  float* sst = (float*)(smem + L_SST);
  float* scs = (float*)(smem + L_SCS);
  u64* snonce = (u64*)(smem + L_NONCE);
  const unsigned sbase = lds_addr(smem);
  unsigned* scnt = (unsigned*)(smem + L_CNT);
  auto dep_flag = [&](int j) {
    const int t = t_of(j);
    return FL_READY1 + (t < 64 ? (((t >> 5) * 4 + cbk) * 8 + ((t & 31) >> 2)) : ((8 + t - 64) * 8 + (rbB >> 1)));
  };
  auto wait_flag = [&](u64 nonce_, int fi) {
    const u64 want = flag_tag(nonce_, fi);
    u64* p = flag_ptr(ws, fi);
#pragma unroll 1
    for (unsigned spins = 0; spins < SPIN_CAP; ++spins) {
      const u64 v = __hip_atomic_load(p, RLX_AGENT);
      const bool ok = __builtin_amdgcn_readfirstlane((unsigned)(v == want)) != 0;
      if (ok) break;
      __builtin_amdgcn_s_sleep(1);
    }
  };
  auto uniform64 = [&](u64 v) {
    const unsigned hi = (unsigned)__builtin_amdgcn_readfirstlane((int)(unsigned)(v >> 32));
    const unsigned lo = (unsigned)__builtin_amdgcn_readfirstlane((int)(unsigned)v);
    return ((u64)hi << 32) | (u64)lo;
  };

  if (wid >= 8) {
    const int lw = wid - 8;
    const int l3 = lane >> 3, l7 = lane & 7;
    const int rt_c0 = (l7 ^ (fq & 7)) * 8, rt_c1 = (l7 ^ ((fq + 4) & 7)) * 8;
    const int kt_c0 = (l7 ^ ((fq & 1) << 1)) * 8, kt_c1 = (l7 ^ (((fq & 1) | 2) << 1)) * 8;
    if (has_a) {
      __builtin_amdgcn_s_setprio(2);
      const u16* xa0 = x + (size_t)(rowA + lw * 64 + l3) * D_ + rt_c0;
      const u16* xa1 = x + (size_t)(rowA + lw * 64 + 8 + l3) * D_ + rt_c1;
      const u16 *wb0, *wb1;
      int wstep_j, wstep_t;
      if (nk) {
        const u16* wnk = (cb < 4 ? wd_re : wd_im) + (size_t)((cb & 3) * 128 + lw * 32 + l3) * D_;
        wb0 = wnk + rt_c0;
        wb1 = wnk + 8 * D_ + rt_c1;
        wstep_j = 16 * D_;
        wstep_t = 64;
      } else {
        const u16* w = (lw < 2) ? w_gate : w_up;
        int c0 = f0 + kt_c0, c1 = f0 + kt_c1;
        if (c0 >= F_) c0 -= 32;
        if (c1 >= F_) c1 -= 32;
        const u16* wk = w + (size_t)((lw & 1) * 32 + l3) * F_;
        wb0 = wk + c0;
        wb1 = wk + 8 * F_ + c1;
        wstep_j = 16 * F_;
        wstep_t = 64 * F_;
      }
      auto stage = [&](int tj, int buf) {
        const int t = (tj + cb) & 7;
        char* sa = smem + buf * K1_STAGE + lw * 8192;
        char* sb = smem + buf * K1_STAGE + K1_A + lw * 4096;
#pragma unroll
        for (int i = 0; i < 4; ++i) {
          GLDS16(xa0 + i * 16 * D_ + t * 64, sa + i * 2048, 0);
          GLDS16(xa1 + i * 16 * D_ + t * 64, sa + i * 2048 + 1024, 0);
        }
#pragma unroll
        for (int i = 0; i < 2; ++i) {
          GLDS16(wb0 + i * wstep_j + t * wstep_t, sb + i * 2048, 0);
          GLDS16(wb1 + i * wstep_j + t * wstep_t, sb + i * 2048 + 1024, 0);
        }
      };
#pragma unroll 1
      for (int t = 0; t < 2; ++t) stage(t, t);
      f32x4 accS[4], accG[4], accC[2];
#pragma unroll
      for (int i = 0; i < 4; ++i) {
        accS[i] = f32x4{0.f, 0.f, 0.f, 0.f};
        accG[i] = f32x4{0.f, 0.f, 0.f, 0.f};
      }
      accC[0] = f32x4{0.f, 0.f, 0.f, 0.f};
      accC[1] = f32x4{0.f, 0.f, 0.f, 0.f};
      const short ONE = 0x3f80;
      const bf16x8 ones = {ONE, ONE, ONE, ONE, ONE, ONE, ONE, ONE};
      const unsigned la0 = sbase + (lw * 64 + fr) * 128 + ch0;
      const unsigned la1 = sbase + (lw * 64 + fr) * 128 + ch1;
      const unsigned lb0 = sbase + K1_A + (lw * 32 + fr) * 128 + ch0;
      const unsigned lb1 = sbase + K1_A + (lw * 32 + fr) * 128 + ch1;
      const unsigned lt0 = sbase + K1_A + (lw >> 1) * 8192 + tr_row + 32 * (((lw & 1) * 2) ^ tswz);
      const unsigned lt1 = sbase + K1_A + (lw >> 1) * 8192 + tr_row + 32 * (((lw & 1) * 2 + 1) ^ tswz);
      int ib = 2, buf = 0;
#pragma unroll 1
      for (int t = 0; t < 8; ++t) {
        if (t < 7) WAIT_V(12); else WAIT_V(0);
        BARRIER();
        const unsigned so = buf * K1_STAGE;
        bf16x8 xf[8], wf[4];
        xf[0] = lds_rd128<0>(la0 + so); xf[1] = lds_rd128<2048>(la0 + so);
        xf[2] = lds_rd128<4096>(la0 + so); xf[3] = lds_rd128<6144>(la0 + so);
        xf[4] = lds_rd128<0>(la1 + so); xf[5] = lds_rd128<2048>(la1 + so);
        xf[6] = lds_rd128<4096>(la1 + so); xf[7] = lds_rd128<6144>(la1 + so);
        if (nk) {
          wf[0] = lds_rd128<0>(lb0 + so); wf[1] = lds_rd128<2048>(lb0 + so);
          wf[2] = lds_rd128<0>(lb1 + so); wf[3] = lds_rd128<2048>(lb1 + so);
        } else {
          wf[0] = tr_frag<0>(lt0 + so); wf[1] = tr_frag<0>(lt1 + so);
          wf[2] = tr_frag<4096>(lt0 + so); wf[3] = tr_frag<4096>(lt1 + so);
        }
        if (t + 2 < 8) stage(t + 2, ib);
        ib = (ib == 2) ? 0 : ib + 1;
        WAIT_L0();
        SCHED0();
#pragma unroll
        for (int i = 0; i < 8; ++i) {
          accS[i & 3] = __builtin_amdgcn_mfma_f32_16x16x32_bf16(ones, xf[i], accS[i & 3], 0, 0, 0);
          accG[i & 3] = __builtin_amdgcn_mfma_f32_16x16x32_bf16(xf[i], xf[i], accG[i & 3], 0, 0, 0);
        }
#pragma unroll
        for (int i = 0; i < 4; ++i)
          accC[i & 1] = __builtin_amdgcn_mfma_f32_16x16x32_bf16(wf[i], ones, accC[i & 1], 0, 0, 0);
        buf = (buf == 2) ? 0 : buf + 1;
      }
      if (fq == (fr >> 2)) {
#pragma unroll
        for (int i = 0; i < 4; ++i) {
          const int r = fr & 3;
          const float qq = r == 0 ? accG[i][0] : r == 1 ? accG[i][1] : r == 2 ? accG[i][2] : accG[i][3];
          const float mean = accS[i][0] * (1.0f / D_);
          const float var = fmaxf(qq * (1.0f / D_) - mean * mean, 0.f);
          const float sd = bf16r(sqrtf(bf16r(bf16r(var) + 1e-5f)));
          f32x2 o = {bf16r(mean), __builtin_amdgcn_rcpf(sd)};
          *(f32x2*)(sst + (lw * 64 + i * 16 + fr) * 2) = o;
        }
      }
      if (fr == 0) {
        *(f32x4*)(scs + lw * 32 + fq * 4) = accC[0];
        *(f32x4*)(scs + lw * 32 + 16 + fq * 4) = accC[1];
      }
      WAIT_L0();
      BARRIER();
      __builtin_amdgcn_s_setprio(0);
    }
    const int sub = lw & 1;
    auto stageB = [&](int j, int slot) {
      const int t = t_of(j);
      char* dst = smem + slot * K2_STAGE + lw * 8192;
      if (lw < 2) {
        if (t < 64) {
          const u16* A = (t < 32 ? ws_re : ws_im) + (size_t)(rowB + sub * 64 + l3) * S_ + (t & 31) * 64;
#pragma unroll
          for (int i = 0; i < 8; ++i) GLDS16(A + i * 8 * S_ + ((i & 1) ? rt_c1 : rt_c0), dst + i * 1024, 0);
        } else {
          const u16* A = h + (size_t)(rowB + sub * 64 + l3) * FP_ + (t - 64) * 64;
#pragma unroll
          for (int i = 0; i < 8; ++i) GLDS16(A + i * 8 * FP_ + ((i & 1) ? rt_c1 : rt_c0), dst + i * 1024, 16);
        }
      } else {
        if (t < 64) {
          const u16* B = ab + (size_t)(t * 64 + l3) * D_ + n0 + sub * 64;
#pragma unroll
          for (int i = 0; i < 8; ++i) GLDS16(B + i * 8 * D_ + ((i & 1) ? kt_c1 : kt_c0), dst + i * 1024, 16);
        } else {
          const int k0 = (t - 64) * 64;
#pragma unroll
          for (int i = 0; i < 8; ++i) {
            const int krow = min(k0 + i * 8 + l3, F_ - 1);
            GLDS16(w_down + krow * D_ + n0 + sub * 64 + ((i & 1) ? kt_c1 : kt_c0), dst + i * 1024, 0);
          }
        }
      }
    };
    const bool early = lw < 2;
    if (!has_a) BARRIER();
    if (!early) {
      const u64 nonce_ = uniform64(*snonce);
      int last = -1;
#pragma unroll 1
      for (int j = 0; j < K2_NST - 1; ++j) {
        const int fi = dep_flag(j);
        if (fi != last) wait_flag(nonce_, fi);
        last = fi;
      }
    }
#pragma unroll 1
    for (int s = 0; s < K2_NST - 1; ++s) stageB(s, s);
    int islot = K2_NST - 1;
#pragma unroll 1
    for (int j = 0; j < nt; ++j) {
      const int rem = nt - 1 - j;
      if (rem >= 3) WAIT_V(24);
      else if (rem == 2) WAIT_V(16);
      else if (rem == 1) WAIT_V(8);
      else WAIT_V(0);
      BARRIER();
      if (j + K2_NST - 1 < nt) stageB(j + K2_NST - 1, islot);
      islot = (islot == K2_NST - 1) ? 0 : islot + 1;
    }
    return;
  }

  u64 nonce = 0;
  if (wid == 4) nonce = __hip_atomic_load((u64*)(ws + WS_SYNC), RLX_AGENT);
  auto share_nonce = [&]() {
    if (wid == 4) {
      nonce = uniform64(nonce);
      if (lane == 0) {
        *snonce = nonce;
        *scnt = 0u;
        __hip_atomic_store(flag_ptr(ws, FL_ARRIVE + bid), flag_tag(nonce, FL_ARRIVE + bid), RLX_AGENT);
      }
      WAIT_L0();
    }
  };
  const int wm = wid >> 1, wn = wid & 1;
  using C0 = std::integral_constant<int, 0>;
  using C1 = std::integral_constant<int, 1>;
  char* dummy = smem + L_DUMMY;
  auto warm = [&](int slot) {
    if (lane >= 16) return;
    if (slot < 32) {
      const int t = 16 * q + lane;
      TOUCH((t < 32 ? ws_re : ws_im) + (size_t)(rowB + cbk * 32 + slot) * S_ + (t & 31) * 64, dummy);
    } else {
      const int L = (slot - 32) * 16 + lane, per = (nt - 16) * 8;
      const int krow = hq0 * 64 + (idx >> 2) * per + (L >> 1);
      if (L < 2 * per && krow < F_) TOUCH(w_down + (size_t)krow * D_ + n0 + (L & 1) * 64, dummy);
    }
  };
  if (!has_a) {
    share_nonce();
    BARRIER();
#pragma unroll 1
    for (int i = 0; i < 6; ++i) warm(i * 8 + wid);
  }

  if (has_a) {
    f32x4 acc[4][4];
#pragma unroll
    for (int i = 0; i < 4; ++i)
#pragma unroll
      for (int j = 0; j < 4; ++j) acc[i][j] = f32x4{0.f, 0.f, 0.f, 0.f};
    const unsigned a_b0 = sbase + (wm * 64 + fr) * 128 + ch0, a_b1 = sbase + (wm * 64 + fr) * 128 + ch1;
    const unsigned n_b0 = sbase + K1_A + (wn * 64 + fr) * 128 + ch0;
    const unsigned n_b1 = sbase + K1_A + (wn * 64 + fr) * 128 + ch1;
    const unsigned t_p0 = sbase + K1_A + tr_row + 32 * ((wn * 2) ^ tswz);
    const unsigned t_p1 = sbase + K1_A + tr_row + 32 * ((wn * 2 + 1) ^ tswz);

    auto mainloop = [&](auto NKC) {
      constexpr bool NK = decltype(NKC)::value;
      bf16x8 xf0[4], wf0[4], xf1[4], wf1[4];
      auto rdg = [&](auto KSC, bf16x8(&xf)[4], bf16x8(&wf)[4], unsigned so, int g) {
        constexpr int KS = decltype(KSC)::value;
        const unsigned ab_ = (KS ? a_b1 : a_b0) + so;
        switch (g) {
          case 0: xf[0] = lds_rd128<0>(ab_); break;
          case 2: xf[1] = lds_rd128<2048>(ab_); break;
          case 4: xf[2] = lds_rd128<4096>(ab_); break;
          case 6: xf[3] = lds_rd128<6144>(ab_); break;
          default:
            if constexpr (NK) {
              const unsigned nb_ = (KS ? n_b1 : n_b0) + so;
              if (g == 1) wf[0] = lds_rd128<0>(nb_);
              if (g == 3) wf[1] = lds_rd128<2048>(nb_);
              if (g == 5) wf[2] = lds_rd128<4096>(nb_);
              if (g == 7) wf[3] = lds_rd128<6144>(nb_);
            } else {
              if (g == 1) wf[0] = tr_frag<KS * 4096>(t_p0 + so);
              if (g == 3) wf[1] = tr_frag<KS * 4096>(t_p1 + so);
              if (g == 5) wf[2] = tr_frag<KS * 4096 + 8192>(t_p0 + so);
              if (g == 7) wf[3] = tr_frag<KS * 4096 + 8192>(t_p1 + so);
            }
        }
      };
      BARRIER();
#pragma unroll
      for (int g = 0; g < 8; ++g) rdg(C0{}, xf0, wf0, 0u, g);
      WAIT_L0();
      SCHED0();
      unsigned so = 0;
#pragma unroll 1
      for (int t = 0; t < 7; ++t) {
        if (t > 0) warm((t - 1) * 8 + wid);
        mm16(acc, wf0, xf0, [&](int g) { rdg(C1{}, xf1, wf1, so, g); });
        WAIT_L0();
        SCHED0();
        so = (so == 2 * K1_STAGE) ? 0u : so + K1_STAGE;
        BARRIER();
        mm16(acc, wf1, xf1, [&](int g) { rdg(C0{}, xf0, wf0, so, g); });
        WAIT_L0();
        SCHED0();
      }
      mm16(acc, wf0, xf0, [&](int g) { rdg(C1{}, xf1, wf1, so, g); });
      WAIT_L0();
      SCHED0();
      mm16(acc, wf1, xf1, [&](int) {});
    };
    if (nk) mainloop(std::true_type{});
    else mainloop(std::false_type{});
    share_nonce();
    BARRIER();
    float mb[4], rinv[4];
#pragma unroll
    for (int mi = 0; mi < 4; ++mi) {
      f32x2 v = *(const f32x2*)(sst + (wm * 64 + mi * 16 + fr) * 2);
      mb[mi] = v[0];
      rinv[mi] = v[1];
    }
    f32x4 csv[4];
#pragma unroll
    for (int ni = 0; ni < 4; ++ni) {
      const int ci = nk ? (wn * 64 + ni * 16) : ((ni >> 1) * 64 + wn * 32 + (ni & 1) * 16);
      csv[ni] = *(const f32x4*)(scs + ci + fq * 4);
    }
    char* scr = smem + L_SCR + wid * 2048;
    const __amdgpu_buffer_rsrc_t rsw = __builtin_amdgcn_make_buffer_rsrc(ws, 0, 16u << 20, 0x00020000);
    if (nk) {
      const float sgn = cb < 4 ? 1.0f : -1.0f;
      const unsigned gbase = (unsigned)WS_AB +
          (unsigned)(((cb >> 2) * S_ + rowA + wm * 64 + (lane >> 3)) * D_ + (cb & 3) * 128 + wn * 64 + (lane & 7) * 8) * 2u;
      u32x2 o[4][4];
#pragma unroll
      for (int mi = 0; mi < 4; ++mi) {
        const float r = rinv[mi] * sgn;
#pragma unroll
        for (int ni = 0; ni < 4; ++ni) {
          f32x4 v = (acc[ni][mi] - mb[mi] * csv[ni]) * r;
          o[mi][ni] = u32x2{pack2(v[0], v[1]), pack2(v[2], v[3])};
        }
      }
#pragma unroll
      for (int mi = 0; mi < 4; ++mi) {
#pragma unroll
        for (int ni = 0; ni < 4; ++ni)
          *(u32x2*)(scr + fr * 128 + (((ni * 2 + (fq >> 1)) ^ (fr & 7)) * 16) + (fq & 1) * 8) = o[mi][ni];
#pragma unroll
        for (int hf = 0; hf < 2; ++hf) {
          const int r2 = hf * 8 + (lane >> 3);
          u32x4 v = *(const u32x4*)(scr + r2 * 128 + (((lane & 7) ^ (r2 & 7)) * 16));
          __builtin_amdgcn_raw_buffer_store_b128(v, rsw, gbase + (unsigned)((mi * 16 + hf * 8) * D_ * 2), 0, 16);
        }
      }
    } else {
      const unsigned vmask = (f0 + wn * 32) < F_ ? 0xffffffffu : 0u;
      const unsigned gbase = (unsigned)WS_H +
          (unsigned)((rowA + wm * 64 + (lane >> 2)) * FP_ + f0 + wn * 32 + (lane & 3) * 8) * 2u;
#pragma unroll
      for (int mp = 0; mp < 2; ++mp) {
#pragma unroll
        for (int mj = 0; mj < 2; ++mj) {
          const int mi = mp * 2 + mj;
#pragma unroll
          for (int ni = 0; ni < 2; ++ni) {
            f32x4 g = (acc[ni][mi] - mb[mi] * csv[ni]) * rinv[mi];
            f32x4 u = (acc[ni + 2][mi] - mb[mi] * csv[ni + 2]) * rinv[mi];
            float hv[4];
#pragma unroll
            for (int e = 0; e < 4; ++e) hv[e] = gelu_tanh(g[e]) * u[e];
            u32x2 o = {pack2(hv[0], hv[1]) & vmask, pack2(hv[2], hv[3]) & vmask};
            *(u32x2*)(scr + mj * 1024 + fr * 64 + (((ni * 2 + (fq >> 1)) ^ ((fr >> 1) & 3)) * 16) + (fq & 1) * 8) = o;
          }
        }
        const int r2 = lane >> 2;
#pragma unroll
        for (int mj = 0; mj < 2; ++mj) {
          u32x4 v = *(const u32x4*)(scr + mj * 1024 + r2 * 64 + (((lane & 3) ^ ((r2 >> 1) & 3)) * 16));
          __builtin_amdgcn_raw_buffer_store_b128(v, rsw, gbase + (unsigned)((mp * 2 + mj) * 16 * FP_ * 2), 0, 16);
        }
      }
    }
  }
  WAIT_V(0);
  if (wid != 4) nonce = uniform64(*snonce);
  if (has_a && lane == 0) {
    const unsigned old = __hip_atomic_fetch_add(scnt, 1u, __ATOMIC_RELAXED, __HIP_MEMORY_SCOPE_WORKGROUP);
    if (old == 7u) __hip_atomic_store(flag_ptr(ws, FL_READY1 + nb), flag_tag(nonce, FL_READY1 + nb), RLX_AGENT);
  }
  if (wid > 4) return;

  if (wid == 4) {
    int last = dep_flag(K2_NST - 2);
#pragma unroll 1
    for (int j = 0; j < nt; ++j) {
      const int jn = j + K2_NST - 1;
      if (jn < 16) {
        const int fi = dep_flag(jn);
        if (fi != last) wait_flag(nonce, fi);
        last = fi;
      } else if (jn == 16) {
        const int fi = dep_flag(min(16 + lane, nt - 1));
        const u64 want = flag_tag(nonce, fi);
        u64* p = flag_ptr(ws, fi);
#pragma unroll 1
        for (unsigned spins = 0; spins < SPIN_CAP; ++spins) {
          const u64 v = __hip_atomic_load(p, RLX_AGENT);
          if (__all(v == want)) break;
          __builtin_amdgcn_s_sleep(1);
        }
      }
      BARRIER();
    }
    return;
  }

  {
    f32x4 acc[4][4];
#pragma unroll
    for (int i = 0; i < 4; ++i)
#pragma unroll
      for (int j = 0; j < 4; ++j) acc[i][j] = f32x4{0.f, 0.f, 0.f, 0.f};
    const unsigned a_base0 = sbase + (wm * 64 + fr) * 128 + ch0;
    const unsigned a_base1 = sbase + (wm * 64 + fr) * 128 + ch1;
    const unsigned b_base = sbase + 16384 + wn * 8192 + tr_row;
    unsigned b_off[4];
#pragma unroll
    for (int ni = 0; ni < 4; ++ni) b_off[ni] = b_base + 32 * (ni ^ tswz);
    bf16x8 af0[4], bf0[4], af1[4], bf1[4];
    auto rdg = [&](auto KSC, bf16x8(&af)[4], bf16x8(&bfr)[4], unsigned so, int g) {
      constexpr int KS = decltype(KSC)::value;
      const unsigned ab_ = (KS ? a_base1 : a_base0) + so;
      switch (g) {
        case 0: af[0] = lds_rd128<0>(ab_); break;
        case 2: af[1] = lds_rd128<2048>(ab_); break;
        case 4: af[2] = lds_rd128<4096>(ab_); break;
        case 6: af[3] = lds_rd128<6144>(ab_); break;
        case 1: bfr[0] = tr_frag<KS * 4096>(b_off[0] + so); break;
        case 3: bfr[1] = tr_frag<KS * 4096>(b_off[1] + so); break;
        case 5: bfr[2] = tr_frag<KS * 4096>(b_off[2] + so); break;
        default: bfr[3] = tr_frag<KS * 4096>(b_off[3] + so); break;
      }
    };
    BARRIER();
#pragma unroll
    for (int g = 0; g < 8; ++g) rdg(C0{}, af0, bf0, 0u, g);
    WAIT_L0();
    SCHED0();
    unsigned so = 0;
#pragma unroll 1
    for (int j = 0; j < nt - 1; ++j) {
      mm16(acc, bf0, af0, [&](int g) { rdg(C1{}, af1, bf1, so, g); });
      WAIT_L0();
      SCHED0();
      so = (so == (K2_NST - 1) * K2_STAGE) ? 0u : so + K2_STAGE;
      BARRIER();
      mm16(acc, bf1, af1, [&](int g) { rdg(C0{}, af0, bf0, so, g); });
      WAIT_L0();
      SCHED0();
    }
    mm16(acc, bf0, af0, [&](int g) { rdg(C1{}, af1, bf1, so, g); });
    WAIT_L0();
    SCHED0();
    mm16(acc, bf1, af1, [&](int) {});
    const __amdgpu_buffer_rsrc_t rs = __builtin_amdgcn_make_buffer_rsrc(ws + WS_SLAB, 0, 8u << 20, 0x00020000);
    f32x4 own[4];
#pragma unroll
    for (int mi = 0; mi < 4; ++mi) {
      if (mi == q) {
#pragma unroll
        for (int ni = 0; ni < 4; ++ni) own[ni] = acc[ni][mi];
      } else {
        const unsigned base = (unsigned)((((tile * 4 + mi) * 4 + q) * 4 + wid) * 2) * 1024u + lane * 16u;
#pragma unroll
        for (int np = 0; np < 2; ++np) {
          const f32x4 a0 = acc[2 * np][mi], a1 = acc[2 * np + 1][mi];
          u32x4 v = {pack2(a0[0], a0[1]), pack2(a0[2], a0[3]), pack2(a1[0], a1[1]), pack2(a1[2], a1[3])};
          __builtin_amdgcn_raw_buffer_store_b128(v, rs, base + np * 1024u, 0, 16);
        }
      }
    }
    const size_t orow = (size_t)(rowB + wm * 64 + q * 16 + fr) * D_ + n0 + wn * 64 + fq * 4;
    u32x2 xv[4];
#pragma unroll
    for (int ni = 0; ni < 4; ++ni) xv[ni] = *(const u32x2*)(x + orow + ni * 16);
    WAIT_V(0);
    BARRIER();
    if (wid == 0) {
      if (lane == 0)
        __hip_atomic_store(flag_ptr(ws, FL_READY2 + tile * 4 + q), flag_tag(nonce, FL_READY2 + tile * 4 + q), RLX_AGENT);
      const int fi = FL_READY2 + tile * 4 + (lane & 3);
      const u64 want = flag_tag(nonce, fi);
      u64* p = flag_ptr(ws, fi);
#pragma unroll 1
      for (unsigned spins = 0; spins < SPIN_CAP; ++spins) {
        const u64 v = __hip_atomic_load(p, RLX_AGENT);
        if (__all(v == want || (lane & 3) == q)) break;
        __builtin_amdgcn_s_sleep(1);
      }
    }
    BARRIER();
    u32x4 pv[3][2];
#pragma unroll
    for (int sidx = 0; sidx < 3; ++sidx) {
      const int src = sidx + (sidx >= q ? 1 : 0);
      const unsigned base = (unsigned)((((tile * 4 + q) * 4 + src) * 4 + wid) * 2) * 1024u + lane * 16u;
#pragma unroll
      for (int np = 0; np < 2; ++np) pv[sidx][np] = __builtin_amdgcn_raw_buffer_load_b128(rs, base + np * 1024u, 0, 16);
    }
#pragma unroll
    for (int ni = 0; ni < 4; ++ni) {
      f32x4 v = own[ni];
#pragma unroll
      for (int sidx = 0; sidx < 3; ++sidx) {
        const unsigned w0 = pv[sidx][ni >> 1][(ni & 1) * 2], w1 = pv[sidx][ni >> 1][(ni & 1) * 2 + 1];
        v[0] += bflo(w0); v[1] += bfhi(w0); v[2] += bflo(w1); v[3] += bfhi(w1);
      }
      u32x2 o = {pack2(v[0] + bflo(xv[ni][0]), v[1] + bfhi(xv[ni][0])), pack2(v[2] + bflo(xv[ni][1]), v[3] + bfhi(xv[ni][1]))};
      *(u32x2*)(out + orow + ni * 16) = o;
    }
  }
  if (bid == 0 && wid == 0) {
#pragma unroll 1
    for (unsigned spins = 0; spins < SPIN_CAP; ++spins) {
      bool ok = true;
#pragma unroll
      for (int i = 0; i < 4; ++i) {
        const int fi = FL_ARRIVE + i * 64 + lane;
        ok &= __hip_atomic_load(flag_ptr(ws, fi), RLX_AGENT) == flag_tag(nonce, fi);
      }
      if (__all(ok)) break;
      __builtin_amdgcn_s_sleep(1);
    }
    if (lane == 0) __hip_atomic_store((u64*)(ws + WS_SYNC), nonce + 1, RLX_AGENT);
  }
}

extern "C" void kernel_launch(void* const* d_in, const int* in_sizes, int n_in, void* d_out, int out_size,
                              void* d_ws, size_t ws_size, hipStream_t stream) {
  fused_kernel<<<256, 768, 0, stream>>>((const u16*)d_in[0], (const u16*)d_in[1], (const u16*)d_in[2],
                                        (const u16*)d_in[3], (const u16*)d_in[4], (const u16*)d_in[6],
                                        (const u16*)d_in[7], (const u16*)d_in[8], (u16*)d_out, (char*)d_ws);
}
```
